```python
import torch

def _softmax(x, axis=-1):
    return torch.softmax(x, dim=axis)
def _gelu(x):
    return torch.nn.functional.gelu(x, approximate="tanh")
def _silu(x):
    return torch.nn.functional.silu(x)
def _sigmoid(x):
    return torch.sigmoid(x)
def _layernorm(x, axis=-1, eps=1e-5):
    m = x.mean(dim=axis, keepdim=True)
    v = x.var(dim=axis, keepdim=True, unbiased=False)
    return (x - m) / torch.sqrt(v + eps)
def _scatter_add(tgt, idx, src, axis=0):
    return tgt.clone().index_add_(axis, idx, src)
def _segment_sum(src, seg, n):
    out = torch.zeros((n,) + src.shape[1:], dtype=src.dtype)
    return out.index_add_(0, seg, src)
def _pad(x, pw):
    flat = tuple(v for pair in reversed(pw) for v in pair)
    return torch.nn.functional.pad(x, flat)

kernel_name = 'gated_mix__compressed_kv_cla__linear_attn_block__prompt'

def setup_inputs():

    _w = (lambda a: (lambda t: t.to(torch.bfloat16) if t.is_floating_point() else t)(torch.as_tensor(a)))
    def _A_setup():
        B, H, Sq, Sk, D, M = 1, 32, 2048, 2048, 128, 512
        Dc = 4 * D
        return {
            "x": _w((lambda *s: torch.randn(*s, dtype=torch.bfloat16) * 0.05)(B, Sq, M)),
            "c_kv": _w((lambda *s: torch.randn(*s, dtype=torch.bfloat16) * 1.6)(B, Sk, Dc)),
            "w_q": _w((lambda *s: torch.randn(*s, dtype=torch.bfloat16) * 3.2)(M, H, D) * (M ** -0.5)),
            "w_uk": _w((lambda *s: torch.randn(*s, dtype=torch.bfloat16) * 3.2)(Dc, H, D) * (Dc ** -0.5)),
            "w_uv": _w((lambda *s: torch.randn(*s, dtype=torch.bfloat16) * 1.6)(Dc, H, D) * (Dc ** -0.5)),
            "w_o": _w((lambda *s: torch.randn(*s, dtype=torch.bfloat16) * 1.6)(H, D, M) * ((H * D) ** -0.5)),
        }
    _A_in = _A_setup()

    def _B_setup():
        B, S, D = 1, 2048, 512
        return {
            "x": _w((lambda *s: torch.randn(*s, dtype=torch.bfloat16) * 0.05)(B, S, D)),
            "wqkv": _w((lambda *s: torch.randn(*s, dtype=torch.bfloat16) * 3.2)(D, 3 * D)),
            "wo": _w((lambda *s: torch.randn(*s, dtype=torch.bfloat16) * 0.4)(D, D)),
        }
    _B_in = _B_setup()

    return {
        "A_x": _A_in["x"],
        "A_c_kv": _A_in["c_kv"],
        "A_w_q": _A_in["w_q"],
        "A_w_uk": _A_in["w_uk"],
        "A_w_uv": _A_in["w_uv"],
        "A_w_o": _A_in["w_o"],
        "B_x": _B_in["x"],
        "B_wqkv": _B_in["wqkv"],
        "B_wo": _B_in["wo"],
        "gate_w": _w((lambda *s: torch.randn(*s, dtype=torch.bfloat16) * 0.05)(512) * 16.0),
    }

def reference(A_x, A_c_kv, A_w_q, A_w_uk, A_w_uv, A_w_o, B_x, B_wqkv, B_wo, gate_w):
    def _A_ref(x, c_kv, w_q, w_uk, w_uv, w_o):
        B, Sq, M = x.shape[0], x.shape[1], x.shape[2]
        Sk = c_kv.shape[1]
        H, D = w_q.shape[1], w_q.shape[2]
        q = torch.einsum("bqm,mhd->bhqd", x, w_q)
        k = torch.einsum("bkc,chd->bhkd", c_kv, w_uk)
        v = torch.einsum("bkc,chd->bhkd", c_kv, w_uv)
        scores = torch.einsum("bhqd,bhkd->bhqk", q, k) * (D ** -0.5)
        off = Sk - Sq
        qi = torch.arange(Sq)[:, None]
        kj = torch.arange(Sk)[None, :]
        cmask = kj <= (qi + off)
        scores = torch.where(cmask[None, None], scores, -1e9)
        p = _softmax(scores, axis=-1)
        ctx = torch.einsum("bhqk,bhkd->bhqd", p, v)
        return torch.einsum("bhqd,hdm->bqm", ctx, w_o)

    def _B_ref(x, wqkv, wo):
        B, S, D = x.shape
        H = 32; Dh = D // H
        qkv = (lambda x, *d: x.permute(*d))((x @ wqkv).reshape(B, S, 3, H, Dh), 2, 0, 3, 1, 4)
        q, k, v = qkv[0], qkv[1], qkv[2]
        phi = lambda t: torch.where(t > 0, t + 1, torch.exp(t)) + 1e-6
        pq, pk = phi(q), phi(k)
        kv = torch.einsum("bhsd,bhse->bhde", pk, v)
        z = torch.einsum("bhsd->bhd", pk)
        num = torch.einsum("bhqd,bhde->bhqe", pq, kv)
        den = torch.einsum("bhqd,bhd->bhq", pq, z)[..., None] + 1e-6
        o = num / den
        return (lambda x, *d: x.permute(*d))(o, 0, 2, 1, 3).reshape(B, S, D) @ wo

    x = A_x
    g = _sigmoid((x * gate_w).sum(-1, keepdims=True))
    a = _A_ref(x, A_c_kv, A_w_q, A_w_uk, A_w_uv, A_w_o)
    b = _B_ref(x, B_wqkv, B_wo)
    return g * a + (1 - g) * b
```

```cpp
#include <hip/hip_runtime.h>
#include <stdint.h>
#include <stdio.h>

typedef unsigned short bf16_t;
typedef short bf16x8 __attribute__((ext_vector_type(8)));
typedef short s16x4 __attribute__((ext_vector_type(4)));
typedef float f32x2 __attribute__((ext_vector_type(2)));
typedef float f32x4 __attribute__((ext_vector_type(4)));
typedef float f32x16 __attribute__((ext_vector_type(16)));
typedef unsigned u32x2 __attribute__((ext_vector_type(2)));
typedef unsigned u32x4 __attribute__((ext_vector_type(4)));
typedef __bf16 bf16x2_t __attribute__((ext_vector_type(2)));
typedef short v4i16_t __attribute__((ext_vector_type(4)));
#define LAS __attribute__((address_space(3)))
#define DI __device__ __forceinline__
typedef LAS const char* lds_cptr;
typedef LAS char* lds_ptr;

constexpr int SEQ = 2048, DM = 512, NH = 32, HD = 128;
constexpr int NK16 = 288;
constexpr size_t WS_K = 0;
constexpr size_t WS_V = WS_K + (size_t)NH * SEQ * HD * 2;
constexpr size_t WS_Q = WS_V + (size_t)NH * SEQ * HD * 2;
constexpr size_t WS_A = WS_Q + (size_t)NH * SEQ * HD * 2;
constexpr size_t WS_PQ = WS_A + (size_t)64 * NK16 * 1024;
constexpr size_t WS_KVP = WS_PQ + (size_t)NH * SEQ * 16 * 2;
constexpr size_t WS_G = WS_KVP + (size_t)8 * NH * 272 * 4;
constexpr size_t WS_END = WS_G + SEQ * 4;

constexpr float QSCALE = 0.08838834764831845f * 1.4426950408889634f;

DI unsigned cvtpk(float lo, float hi) { f32x2 v = {lo, hi}; bf16x2_t b = __builtin_convertvector(v, bf16x2_t); return __builtin_bit_cast(unsigned, b); }
DI float bf2f(unsigned short b) { return __uint_as_float((unsigned)b << 16); }
DI s16x4 vtr(lds_cptr p) { return __builtin_bit_cast(s16x4, __builtin_amdgcn_ds_read_tr16_b64_v4i16((LAS v4i16_t*)p)); }
DI bf16x8 cat8(s16x4 lo, s16x4 hi) { return __builtin_shufflevector(lo, hi, 0, 1, 2, 3, 4, 5, 6, 7); }
DI bf16x8 lds8(lds_cptr p) { return *(const LAS bf16x8*)p; }
DI void glds16(const void* g, lds_ptr l) { __builtin_amdgcn_global_load_lds((const unsigned*)g, (LAS unsigned*)l, 16, 0, 0); }
#define WAIT_V(n) asm volatile("s_waitcnt vmcnt(" #n ")" ::: "memory")
#define WAIT_L(n) asm volatile("s_waitcnt lgkmcnt(" #n ")" ::: "memory")
#define BAR() __builtin_amdgcn_s_barrier()
#define SCHED() __builtin_amdgcn_sched_barrier(0)
#define MFMA16(a, b, c) __builtin_amdgcn_mfma_f32_16x16x32_bf16((a), (b), (c), 0, 0, 0)
#define MFMA32(a, b, c) __builtin_amdgcn_mfma_f32_32x32x16_bf16((a), (b), (c), 0, 0, 0)
DI int crow(int r, int hi) { return (r & 3) + 8 * (r >> 2) + 4 * hi; }

constexpr int HTB = 16384;
constexpr int P_LDS = 147456;
struct ProjArgs {
    const bf16_t* xa; const bf16_t* ckv; const bf16_t* wq; const bf16_t* wuk; const bf16_t* wuv;
    const bf16_t* xb; const bf16_t* wqkv; const bf16_t* gw; char* ws;
};

__global__ __launch_bounds__(512, 2) void proj_kernel(ProjArgs p) {
    extern __shared__ __attribute__((aligned(16))) char smem_[];
    lds_ptr lds = (lds_ptr)smem_;
    const int tid = threadIdx.x, wid = __builtin_amdgcn_readfirstlane(tid >> 6);
    const int xcd = blockIdx.x & 7, idx = blockIdx.x >> 3, h = xcd * 4 + (idx >> 3), r = idx & 7;

    {
        const int lane = tid & 63;
        const int row = r * 256 + h * 8 + wid;
        const u32x4 xv = *(const u32x4*)(p.xa + (size_t)row * DM + lane * 8);
        const u32x4 gv = *(const u32x4*)(p.gw + lane * 8);
        float s = 0.f;
#pragma unroll
        for (int e = 0; e < 4; ++e) {
            s += __uint_as_float(xv[e] << 16) * __uint_as_float(gv[e] << 16);
            s += __uint_as_float(xv[e] & 0xffff0000u) * __uint_as_float(gv[e] & 0xffff0000u);
        }
#pragma unroll
        for (int o = 32; o >= 1; o >>= 1) s += __shfl_xor(s, o);
        if (lane == 0) ((float*)(p.ws + WS_G))[row] = 1.f / (1.f + __expf(-s));
    }

    const unsigned ldsw = (unsigned)wid * 1024u;
#define LANE_CONSTS() int tz_ = threadIdx.x; asm volatile("" : "+v"(tz_)); const int lane = tz_ & 63, fr = lane & 15, fq = lane >> 4; \
    const unsigned voffA = (unsigned)((8 * wid + (lane >> 3)) * (DM * 2) + (((lane & 7) ^ ((4 * (wid & 1) + (lane >> 4)) & 7)) * 16)); \
    const unsigned voffB4096 = (unsigned)(4 * wid + (lane >> 4)) * 8192u + (unsigned)((lane & 15) ^ (((lane >> 4) << 2) | (wid & 3))) * 16u; (void)voffB4096; (void)fr; (void)fq;
#define STAGE_A(off, gbase) do { const char* g_ = (const char*)(gbase) + voffA; glds16(g_, lds + (off) + ldsw); glds16(g_ + 64 * DM * 2, lds + (off) + ldsw + 8192); } while (0)
#define STAGE_B(off, gbase) do { const char* g_ = (const char*)(gbase) + voffB4096; glds16(g_, lds + (off) + ldsw); glds16(g_ + 32 * 8192, lds + (off) + ldsw + 8192); } while (0)

    {
        LANE_CONSTS();
        const int wr = wid >> 2, wc = wid & 3;
        const unsigned aoff0 = (unsigned)((wr * 64 + fr) * 128 + ((fq ^ (fr >> 1)) * 16)), aoff1 = aoff0 ^ 64u;
        const int tq = (lane & 15) >> 2, tp = lane & 3;
        unsigned boff[2][2];
#pragma unroll
        for (int n = 0; n < 2; ++n)
#pragma unroll
            for (int t = 0; t < 2; ++t) {
                const int row = 8 * fq + 4 * t + tq, ch = 4 * wc + 2 * n + (tp >> 1);
                boff[n][t] = (unsigned)(65536 + 256 * row + 16 * (ch ^ ((tq << 2) | ((2 * fq + t) & 3))) + 8 * (tp & 1));
            }
#define SA(b, hh) (((b) * 2 + (hh)) * HTB)
#define SB(b, hh) (((b) * 2 + (hh)) * HTB)
#define LDA(dst, b, hh) do { _Pragma("unroll") for (int m = 0; m < 4; ++m) { dst[m][0] = lds8(lds + aoff0 + SA(b, hh) + m * 2048); dst[m][1] = lds8(lds + aoff1 + SA(b, hh) + m * 2048); } } while (0)
#define LDB(dst, b, hh) do { _Pragma("unroll") for (int n = 0; n < 2; ++n) _Pragma("unroll") for (int k = 0; k < 2; ++k) \
            dst[n][k] = cat8(vtr(lds + boff[n][0] + SB(b, hh) + k * 8192), vtr(lds + boff[n][1] + SB(b, hh) + k * 8192)); } while (0)
#define MMA_K(ai, At, Bt) do { __builtin_amdgcn_s_setprio(1); _Pragma("unroll") for (int m = 0; m < 4; ++m) _Pragma("unroll") for (int n = 0; n < 2; ++n) _Pragma("unroll") for (int k = 0; k < 2; ++k) \
            acc[ai][0][m][n] = MFMA16(Bt[n][k], At[m][k], acc[ai][0][m][n]); __builtin_amdgcn_s_setprio(0); } while (0)
#define MMA_V(ai, At, Bt) do { __builtin_amdgcn_s_setprio(1); _Pragma("unroll") for (int m = 0; m < 4; ++m) _Pragma("unroll") for (int n = 0; n < 2; ++n) _Pragma("unroll") for (int k = 0; k < 2; ++k) \
            acc[ai][1][m][n] = MFMA16(At[m][k], Bt[n][k], acc[ai][1][m][n]); __builtin_amdgcn_s_setprio(0); } while (0)
        f32x4 acc[2][2][4][2];
#pragma unroll
        for (int a = 0; a < 2; ++a)
#pragma unroll
            for (int b = 0; b < 2; ++b)
#pragma unroll
                for (int m = 0; m < 4; ++m)
#pragma unroll
                    for (int n = 0; n < 2; ++n) acc[a][b][m][n] = (f32x4){0.f, 0.f, 0.f, 0.f};
        bf16x8 At[4][2], B0[2][2], B1[2][2];
        const char* cA = (const char*)(p.ckv + (size_t)r * 256 * DM);
        const char* cB0 = (const char*)(p.wuk + h * HD);
        const char* cB1 = (const char*)(p.wuv + h * HD);
        constexpr size_t hA = (size_t)128 * DM * 2, kA = 128, kB = (size_t)64 * 8192;
        constexpr int nt = 8;
        STAGE_B(65536 + SB(0, 0), cB0); STAGE_A(SA(0, 0), cA); STAGE_B(65536 + SB(0, 1), cB1); STAGE_A(SA(0, 1), cA + hA);
        if (wr == 1) BAR();
        WAIT_V(4); BAR();
        STAGE_B(65536 + SB(1, 0), cB0 + kB); STAGE_A(SA(1, 0), cA + kA); STAGE_B(65536 + SB(1, 1), cB1 + kB);
        WAIT_V(6); BAR();
        for (int t = 0; t < nt - 2; t += 2) {
            LDB(B0, 0, 0); SCHED(); LDA(At, 0, 0); STAGE_A(SA(1, 1), cA + hA + (t + 1) * kA);
            WAIT_L(8); BAR(); WAIT_L(0); MMA_K(0, At, B0); BAR(); SCHED();
            LDB(B1, 0, 1); STAGE_B(65536 + SB(0, 0), cB0 + (t + 2) * kB);
            BAR(); WAIT_L(0); MMA_V(0, At, B1); BAR();
            LDA(At, 0, 1); STAGE_A(SA(0, 0), cA + (t + 2) * kA);
            BAR(); WAIT_L(0); MMA_K(1, At, B0); BAR(); SCHED();
            STAGE_B(65536 + SB(0, 1), cB1 + (t + 2) * kB);
            WAIT_V(6); BAR(); MMA_V(1, At, B1); BAR();
            LDB(B0, 1, 0); SCHED(); LDA(At, 1, 0); STAGE_A(SA(0, 1), cA + hA + (t + 2) * kA);
            WAIT_L(8); BAR(); WAIT_L(0); MMA_K(0, At, B0); BAR(); SCHED();
            LDB(B1, 1, 1); STAGE_B(65536 + SB(1, 0), cB0 + (t + 3) * kB);
            BAR(); WAIT_L(0); MMA_V(0, At, B1); BAR();
            LDA(At, 1, 1); STAGE_A(SA(1, 0), cA + (t + 3) * kA);
            BAR(); WAIT_L(0); MMA_K(1, At, B0); BAR(); SCHED();
            STAGE_B(65536 + SB(1, 1), cB1 + (t + 3) * kB);
            WAIT_V(6); BAR(); MMA_V(1, At, B1); BAR();
        }
        {
            LDB(B0, 0, 0); WAIT_V(0); LDA(At, 0, 0); STAGE_A(SA(1, 1), cA + hA + (nt - 1) * kA);
            BAR(); WAIT_L(0); MMA_K(0, At, B0); BAR();
            LDB(B1, 0, 1); BAR(); WAIT_L(0); MMA_V(0, At, B1); BAR();
            LDA(At, 0, 1); BAR(); WAIT_L(0); MMA_K(1, At, B0); MMA_V(1, At, B1); BAR();
        }
        {
            LDB(B0, 1, 0); LDA(At, 1, 0); BAR(); WAIT_L(0); MMA_K(0, At, B0); BAR();
            LDB(B1, 1, 1); WAIT_V(0); BAR(); WAIT_L(0); MMA_V(0, At, B1); BAR();
            LDA(At, 1, 1); BAR(); WAIT_L(0); MMA_K(1, At, B0); MMA_V(1, At, B1); BAR();
        }
        if (wr == 0) BAR();
        int te_ = threadIdx.x; asm volatile("" : "+v"(te_));
        const int fr_e = te_ & 15, fq_e = (te_ & 63) >> 4;
#define fr fr_e
#define fq fq_e
        char* Kf = p.ws + WS_K + (size_t)h * 32 * 16384;
        char* Vf = p.ws + WS_V + (size_t)h * 32 * 16384;
#pragma unroll
        for (int ai = 0; ai < 2; ++ai) {
            const int tile = 4 * r + 2 * ai + wr;
#pragma unroll
            for (int m = 0; m < 4; ++m) {
                const f32x4 a = acc[ai][0][m][0], b = acc[ai][0][m][1];
                u32x4 w = {cvtpk(a[0], a[1]), cvtpk(a[2], a[3]), cvtpk(b[0], b[1]), cvtpk(b[2], b[3])};
                const int frag = (m >> 1) * 8 + 2 * wc + (fq >> 1);
                *(u32x4*)(Kf + (size_t)tile * 16384 + frag * 1024 + ((fq & 1) * 32 + 16 * (m & 1) + fr) * 16) = w;
            }
#pragma unroll
            for (int m = 0; m < 4; m += 2)
#pragma unroll
                for (int n = 0; n < 2; ++n) {
                    const f32x4 a = acc[ai][1][m][n], b = acc[ai][1][m + 1][n];
                    const unsigned a0 = cvtpk(a[0], a[1]), a1 = cvtpk(a[2], a[3]), b0 = cvtpk(b[0], b[1]), b1 = cvtpk(b[2], b[3]);
                    auto r0 = __builtin_amdgcn_permlane32_swap(a0, b0, false, false);
                    auto r1 = __builtin_amdgcn_permlane32_swap(a1, b1, false, false);
                    u32x4 w = {r0[0], r1[0], r0[1], r1[1]};
                    const int ks = m + (fq >> 1);
                    const int frag = ks * 4 + wc;
                    *(u32x4*)(Vf + (size_t)tile * 16384 + frag * 1024 + ((fq & 1) * 32 + 16 * n + fr) * 16) = w;
                }
        }
#undef fr
#undef fq
#undef SA
#undef SB
#undef LDA
#undef LDB
    }
    WAIT_L(0); BAR();

    {
        LANE_CONSTS();
        constexpr int STG = 49152;
        const int wm = wid >> 1, wn = wid & 1;
        const unsigned aoff0 = (unsigned)((wm >> 1) * HTB + ((wm & 1) * 64 + fr) * 128 + ((fq ^ (fr >> 1)) * 16)), aoff1 = aoff0 ^ 64u;
        const int tq = (lane & 15) >> 2, tp = lane & 3;
        unsigned boff[4][2];
#pragma unroll
        for (int n = 0; n < 4; ++n)
#pragma unroll
            for (int t = 0; t < 2; ++t) {
                const int row = 8 * fq + 4 * t + tq, ch = 8 * wn + 2 * n + (tp >> 1);
                boff[n][t] = (unsigned)(2 * HTB + 256 * row + 16 * (ch ^ ((tq << 2) | ((2 * fq + t) & 3))) + 8 * (tp & 1));
            }
        const char* cA = (const char*)(p.xa + (size_t)r * 256 * DM);
        const char* cB = (const char*)(p.wq + h * HD);
        constexpr size_t hA = (size_t)128 * DM * 2, kA = 128, kB = (size_t)64 * 8192;
#define STAGE2(s, t) do { STAGE_A((s) * STG, cA + (t) * kA); STAGE_A((s) * STG + HTB, cA + hA + (t) * kA); STAGE_B((s) * STG + 2 * HTB, cB + (t) * kB); } while (0)
        f32x4 acc[4][4];
#pragma unroll
        for (int m = 0; m < 4; ++m)
#pragma unroll
            for (int n = 0; n < 4; ++n) acc[m][n] = (f32x4){0.f, 0.f, 0.f, 0.f};
        STAGE2(0, 0); STAGE2(1, 1);
#pragma unroll
        for (int t = 0; t < 8; ++t) {
            if (t < 7) WAIT_V(6); else WAIT_V(0);
            BAR();
            if (t + 2 < 8) STAGE2((t + 2) % 3, t + 2);
            const int so = (t % 3) * STG;
#pragma unroll
            for (int k = 0; k < 2; ++k) {
                bf16x8 At[4], Bt[4];
#pragma unroll
                for (int m = 0; m < 4; ++m) At[m] = lds8(lds + so + (k ? aoff1 : aoff0) + m * 2048);
#pragma unroll
                for (int n = 0; n < 4; ++n) Bt[n] = cat8(vtr(lds + so + boff[n][0] + k * 8192), vtr(lds + so + boff[n][1] + k * 8192));
#pragma unroll
                for (int m = 0; m < 4; ++m)
#pragma unroll
                    for (int n = 0; n < 4; ++n) acc[m][n] = MFMA16(Bt[n], At[m], acc[m][n]);
            }
            WAIT_L(0);
        }
        char* Qf = p.ws + WS_Q + (size_t)h * 64 * 8192;
#pragma unroll
        for (int m = 0; m < 4; ++m) {
            const int s = wm * 64 + 16 * m;
            const int rowblk = 8 * r + (s >> 5);
#pragma unroll
            for (int u = 0; u < 2; ++u) {
                const f32x4 a = acc[m][2 * u] * QSCALE, b = acc[m][2 * u + 1] * QSCALE;
                u32x4 w = {cvtpk(a[0], a[1]), cvtpk(a[2], a[3]), cvtpk(b[0], b[1]), cvtpk(b[2], b[3])};
                const int d0 = 2 * (2 * wn + u) + (fq >> 1);
                *(u32x4*)(Qf + (size_t)rowblk * 8192 + d0 * 1024 + ((fq & 1) * 32 + (s & 31) + fr) * 16) = w;
            }
        }
#undef STAGE2
    }
    WAIT_L(0); BAR();

    {
        LANE_CONSTS();
        constexpr int STG = 40960;
        const unsigned aoff0 = (unsigned)((wid >> 2) * HTB + ((wid & 3) * 32 + fr) * 128 + ((fq ^ (fr >> 1)) * 16)), aoff1 = aoff0 ^ 64u;
        const int tq = (lane & 15) >> 2, tp = lane & 3;
        unsigned boff[3][2];
#pragma unroll
        for (int n = 0; n < 3; ++n)
#pragma unroll
            for (int t = 0; t < 2; ++t) {
                const int row = 8 * fq + 4 * t + tq, f = ((row >> 1) & 1) | (((row >> 3) & 1) << 1);
                boff[n][t] = (unsigned)(2 * HTB + 128 * row + 32 * (n ^ f) + 8 * tp);
            }
        const int brow = 8 * wid + (lane >> 3), bf_ = ((brow >> 1) & 1) | (((brow >> 3) & 1) << 1);
        int bs = ((lane & 7) >> 1) ^ bf_; if (bs == 3) bs = 0;
        const unsigned voffB3 = (unsigned)(brow * (1536 * 2) + (bs * 512 + h * 16) * 2 + (lane & 1) * 16);
        const char* cA = (const char*)(p.xb + (size_t)r * 256 * DM);
        const char* cB = (const char*)p.wqkv;
        constexpr size_t hA = (size_t)128 * DM * 2, kA = 128, kB = (size_t)64 * 1536 * 2;
#define STAGE3(s, t) do { STAGE_A((s) * STG, cA + (t) * kA); STAGE_A((s) * STG + HTB, cA + hA + (t) * kA); glds16(cB + (t) * kB + voffB3, lds + (s) * STG + 2 * HTB + ldsw); } while (0)
        f32x4 aq[2], ak[2], av[2];
#pragma unroll
        for (int m = 0; m < 2; ++m) { aq[m] = (f32x4){0.f, 0.f, 0.f, 0.f}; ak[m] = aq[m]; av[m] = aq[m]; }
        STAGE3(0, 0); STAGE3(1, 1);
#pragma unroll
        for (int t = 0; t < 8; ++t) {
            if (t < 7) WAIT_V(5); else WAIT_V(0);
            BAR();
            if (t + 2 < 8) STAGE3((t + 2) % 3, t + 2);
            const int so = (t % 3) * STG;
#pragma unroll
            for (int k = 0; k < 2; ++k) {
                bf16x8 At[2], Bt[3];
#pragma unroll
                for (int m = 0; m < 2; ++m) At[m] = lds8(lds + so + (k ? aoff1 : aoff0) + m * 2048);
#pragma unroll
                for (int n = 0; n < 3; ++n) Bt[n] = cat8(vtr(lds + so + boff[n][0] + k * 4096), vtr(lds + so + boff[n][1] + k * 4096));
#pragma unroll
                for (int m = 0; m < 2; ++m) { aq[m] = MFMA16(Bt[0], At[m], aq[m]); ak[m] = MFMA16(At[m], Bt[1], ak[m]); av[m] = MFMA16(At[m], Bt[2], av[m]); }
            }
            WAIT_L(0);
        }
#undef STAGE3
#define PHI(t) (((t) > 0.f ? (t) + 1.f : __expf(t)) + 1e-6f)
        bf16_t* PQ = (bf16_t*)(p.ws + WS_PQ) + (size_t)h * SEQ * 16;
#pragma unroll
        for (int m = 0; m < 2; ++m) {
            const int row = r * 256 + wid * 32 + 16 * m + fr;
            u32x2 w = {cvtpk(PHI(aq[m][0]), PHI(aq[m][1])), cvtpk(PHI(aq[m][2]), PHI(aq[m][3]))};
            *(u32x2*)(PQ + (size_t)row * 16 + 4 * fq) = w;
        }
        u32x4 pk = {cvtpk(PHI(ak[0][0]), PHI(ak[0][1])), cvtpk(PHI(ak[0][2]), PHI(ak[0][3])), cvtpk(PHI(ak[1][0]), PHI(ak[1][1])), cvtpk(PHI(ak[1][2]), PHI(ak[1][3]))};
        u32x4 vv = {cvtpk(av[0][0], av[0][1]), cvtpk(av[0][2], av[0][3]), cvtpk(av[1][0], av[1][1]), cvtpk(av[1][2], av[1][3])};
        const u32x4 ones = {0x3f803f80u, 0x3f803f80u, 0x3f803f80u, 0x3f803f80u};
        const f32x4 z4 = {0.f, 0.f, 0.f, 0.f};
        const f32x4 kv = MFMA16(__builtin_bit_cast(bf16x8, pk), __builtin_bit_cast(bf16x8, vv), z4);
        const f32x4 zz = MFMA16(__builtin_bit_cast(bf16x8, pk), __builtin_bit_cast(bf16x8, ones), z4);
        WAIT_L(0); BAR();
        LAS float* red = (LAS float*)lds;
#pragma unroll
        for (int e = 0; e < 4; ++e) red[wid * 272 + (4 * fq + e) * 16 + fr] = kv[e];
        if (fr == 0) {
#pragma unroll
            for (int e = 0; e < 4; ++e) red[wid * 272 + 256 + 4 * fq + e] = zz[e];
        }
        WAIT_L(0); BAR();
        if (tz_ < 272) {
            float s = 0.f;
#pragma unroll
            for (int w = 0; w < 8; ++w) s += red[w * 272 + tz_];
            ((float*)(p.ws + WS_KVP))[(size_t)(r * NH + h) * 272 + tz_] = s;
        }
#undef PHI
    }
#undef STAGE_A
#undef STAGE_B
}

constexpr int A_LDS = 163840;
struct AttnArgs { char* ws; };

__global__ __launch_bounds__(256, 1) void attn_kernel(AttnArgs p) {
    extern __shared__ __attribute__((aligned(16))) char smem_[];
    lds_ptr lds = (lds_ptr)smem_;
    const int tid = threadIdx.x, wid = __builtin_amdgcn_readfirstlane(tid >> 6), lane = tid & 63;
    const int l32 = lane & 31, hi = lane >> 5;
    const int xcd = blockIdx.x & 7, idx = blockIdx.x >> 3, h = xcd * 4 + (idx >> 3), pr = idx & 7;
    const int rr = wid & 1, c = wid >> 1;
    const char* Kf = p.ws + WS_K + (size_t)h * 32 * 16384;
    const char* Vf = p.ws + WS_V + (size_t)h * 32 * 16384;
    const char* Qf = p.ws + WS_Q + (size_t)h * 64 * 8192;
    const float* G = (const float*)(p.ws + WS_G);
    char* Af = p.ws + WS_A;

    {
        LAS float* kvs = (LAS float*)(lds + 131072);
        for (int i = tid; i < 272; i += 256) {
            float s = 0.f;
#pragma unroll
            for (int rp = 0; rp < 8; ++rp) s += ((const float*)(p.ws + WS_KVP))[(size_t)(rp * NH + h) * 272 + i];
            kvs[i] = s;
        }
        WAIT_L(0); BAR();
        u32x4 aw;
#pragma unroll
        for (int j2 = 0; j2 < 4; ++j2) {
            const int d = 8 * hi + 2 * j2;
            float v0 = 0.f, v1 = 0.f;
            if (l32 < 16) { v0 = kvs[d * 16 + l32]; v1 = kvs[(d + 1) * 16 + l32]; }
            else if (l32 == 16) { v0 = kvs[256 + d]; v1 = kvs[256 + d + 1]; }
            aw[j2] = cvtpk(v0, v1);
        }
#pragma unroll
        for (int u = 0; u < 2; ++u) {
            const int b8 = wid * 2 + u;
            const int rowblk = (b8 < 4 ? (15 - pr) : pr) * 4 + (b8 & 3);
            const int row = rowblk * 32 + l32;
            const bf16x8 pq = *(const bf16x8*)((const bf16_t*)(p.ws + WS_PQ) + ((size_t)h * SEQ + row) * 16 + 8 * hi);
            f32x16 z16 = {};
            const f32x16 o = MFMA32(__builtin_bit_cast(bf16x8, aw), pq, z16);
            float den = o[8];
            auto sw = __builtin_amdgcn_permlane32_swap(__float_as_uint(den), __float_as_uint(den), false, false);
            den = (hi ? __uint_as_float(sw[0]) : den) + 1e-6f;
            const float sc = (1.f - G[row]) / den;
            u32x4 w = {cvtpk(o[0] * sc, o[1] * sc), cvtpk(o[2] * sc, o[3] * sc), cvtpk(o[4] * sc, o[5] * sc), cvtpk(o[6] * sc, o[7] * sc)};
            *(u32x4*)(Af + ((size_t)rowblk * NK16 + 256 + h) * 1024 + lane * 16) = w;
        }
        WAIT_L(0); BAR();
    }

    for (int pass = 0; pass < 2; ++pass) {
        const int blk = pass ? pr : 15 - pr;
        const int T = blk + 1;
        bf16x8 qf[2][8];
#pragma unroll
        for (int qb = 0; qb < 2; ++qb)
#pragma unroll
            for (int d0 = 0; d0 < 8; ++d0) qf[qb][d0] = *(const bf16x8*)(Qf + (size_t)(blk * 4 + rr * 2 + qb) * 8192 + d0 * 1024 + lane * 16);
        auto dma = [&](int t) {
            const int slot = (t & 1) * 32768;
            const char* kg = Kf + (size_t)t * 32768 + wid * 8192 + lane * 16;
            const char* vg = Vf + (size_t)t * 32768 + wid * 8192 + lane * 16;
#pragma unroll
            for (int i = 0; i < 8; ++i) glds16(kg + i * 1024, lds + slot + wid * 8192 + i * 1024);
#pragma unroll
            for (int i = 0; i < 8; ++i) glds16(vg + i * 1024, lds + 65536 + slot + wid * 8192 + i * 1024);
        };
        dma(0);
        f32x16 o[4][2];
#pragma unroll
        for (int db = 0; db < 4; ++db) { o[db][0] = f32x16{}; o[db][1] = f32x16{}; }
        float m[2] = {-1e30f, -1e30f}, l[2] = {0.f, 0.f};
        for (int t = 0; t < T; ++t) {
            WAIT_V(0); BAR();
            if (t + 1 < T) dma(t + 1);
            const int ko = (t & 1) * 32768 + c * 16384 + lane * 16;
            f32x16 s[2][2];
#pragma unroll
            for (int kb = 0; kb < 2; ++kb) {
                s[kb][0] = f32x16{}; s[kb][1] = f32x16{};
#pragma unroll
                for (int d0 = 0; d0 < 8; ++d0) {
                    const bf16x8 kf = lds8(lds + ko + (kb * 8 + d0) * 1024);
                    s[kb][0] = MFMA32(kf, qf[0][d0], s[kb][0]);
                    s[kb][1] = MFMA32(kf, qf[1][d0], s[kb][1]);
                }
            }
            if (t == T - 1) {
#pragma unroll
                for (int kb = 0; kb < 2; ++kb)
#pragma unroll
                    for (int qb = 0; qb < 2; ++qb)
#pragma unroll
                        for (int e = 0; e < 16; ++e) {
                            const int key = c * 64 + kb * 32 + crow(e, hi), q = rr * 64 + qb * 32 + l32;
                            if (key > q) s[kb][qb][e] = -__builtin_inff();
                        }
            }
            bf16x8 pf[2][4];
#pragma unroll
            for (int qb = 0; qb < 2; ++qb) {
                float mx = s[0][qb][0];
#pragma unroll
                for (int e = 1; e < 16; ++e) mx = fmaxf(mx, s[0][qb][e]);
#pragma unroll
                for (int e = 0; e < 16; ++e) mx = fmaxf(mx, s[1][qb][e]);
                auto sw = __builtin_amdgcn_permlane32_swap(__float_as_uint(mx), __float_as_uint(mx), false, false);
                mx = fmaxf(__uint_as_float(sw[0]), __uint_as_float(sw[1]));
                const float mn = fmaxf(m[qb], mx);
                const float al = __builtin_amdgcn_exp2f(m[qb] - mn);
                m[qb] = mn;
                float ps = 0.f;
#pragma unroll
                for (int kb = 0; kb < 2; ++kb)
#pragma unroll
                    for (int e = 0; e < 16; ++e) { s[kb][qb][e] = __builtin_amdgcn_exp2f(s[kb][qb][e] - mn); ps += s[kb][qb][e]; }
                l[qb] = l[qb] * al + ps;
#pragma unroll
                for (int db = 0; db < 4; ++db) o[db][qb] *= al;
#pragma unroll
                for (int ks = 0; ks < 4; ++ks) {
                    const int b = 8 * (ks & 1);
                    u32x4 w = {cvtpk(s[ks >> 1][qb][b], s[ks >> 1][qb][b + 1]), cvtpk(s[ks >> 1][qb][b + 2], s[ks >> 1][qb][b + 3]),
                               cvtpk(s[ks >> 1][qb][b + 4], s[ks >> 1][qb][b + 5]), cvtpk(s[ks >> 1][qb][b + 6], s[ks >> 1][qb][b + 7])};
                    pf[qb][ks] = __builtin_bit_cast(bf16x8, w);
                }
            }
#pragma unroll
            for (int ks = 0; ks < 4; ++ks)
#pragma unroll
                for (int db = 0; db < 4; ++db) {
                    const bf16x8 vf = lds8(lds + 65536 + ko + (ks * 4 + db) * 1024);
                    o[db][0] = MFMA32(vf, pf[0][ks], o[db][0]);
                    o[db][1] = MFMA32(vf, pf[1][ks], o[db][1]);
                }
            WAIT_L(0);
        }
        BAR();
        {
            LAS float* xo = (LAS float*)(lds + wid * 16384);
            LAS float* xs = (LAS float*)(lds + 131072 + wid * 512);
#pragma unroll
            for (int db = 0; db < 4; ++db)
#pragma unroll
                for (int g4 = 0; g4 < 4; ++g4) {
                    const f32x16& src = c ? o[db][0] : o[db][1];
                    *(LAS f32x4*)(xo + ((db * 4 + g4) * 64 + lane) * 4) = (f32x4){src[4 * g4], src[4 * g4 + 1], src[4 * g4 + 2], src[4 * g4 + 3]};
                }
            float lo_ = c ? l[0] : l[1], mo_ = c ? m[0] : m[1];
            { auto sw = __builtin_amdgcn_permlane32_swap(__float_as_uint(lo_), __float_as_uint(lo_), false, false); lo_ = __uint_as_float(sw[0]) + __uint_as_float(sw[1]); }
            xs[lane] = mo_; xs[64 + lane] = lo_;
            WAIT_L(0); BAR();
            const int pw = wid ^ 2;
            LAS const float* yo = (LAS const float*)(lds + pw * 16384);
            LAS const float* ys = (LAS const float*)(lds + 131072 + pw * 512);
            const float m2 = ys[lane], l2 = ys[64 + lane];
            float l1 = c ? l[1] : l[0]; const float m1 = c ? m[1] : m[0];
            { auto sw = __builtin_amdgcn_permlane32_swap(__float_as_uint(l1), __float_as_uint(l1), false, false); l1 = __uint_as_float(sw[0]) + __uint_as_float(sw[1]); }
            const float mn = fmaxf(m1, m2), w1 = __builtin_amdgcn_exp2f(m1 - mn), w2 = __builtin_amdgcn_exp2f(m2 - mn);
            const int rowblk = blk * 4 + rr * 2 + c;
            const float sc = G[rowblk * 32 + l32] / (l1 * w1 + l2 * w2);
            const float f1 = w1 * sc, f2 = w2 * sc;
#pragma unroll
            for (int db = 0; db < 4; ++db) {
                const f32x16& own = c ? o[db][1] : o[db][0];
                float v[16];
#pragma unroll
                for (int g4 = 0; g4 < 4; ++g4) {
                    const f32x4 y = *(LAS const f32x4*)(yo + ((db * 4 + g4) * 64 + lane) * 4);
#pragma unroll
                    for (int e = 0; e < 4; ++e) v[4 * g4 + e] = own[4 * g4 + e] * f1 + y[e] * f2;
                }
#pragma unroll
                for (int s2 = 0; s2 < 2; ++s2) {
                    u32x4 w = {cvtpk(v[8 * s2], v[8 * s2 + 1]), cvtpk(v[8 * s2 + 2], v[8 * s2 + 3]), cvtpk(v[8 * s2 + 4], v[8 * s2 + 5]), cvtpk(v[8 * s2 + 6], v[8 * s2 + 7])};
                    *(u32x4*)(Af + ((size_t)rowblk * NK16 + h * 8 + 2 * db + s2) * 1024 + lane * 16) = w;
                }
            }
            WAIT_L(0); BAR();
        }
    }
}

constexpr int O_LDS = 65536;
struct OutArgs { const char* ws; const bf16_t* wo; const bf16_t* wob; bf16_t* out; };

__global__ __launch_bounds__(256, 1) void out_kernel(OutArgs p) {
    extern __shared__ __attribute__((aligned(16))) char smem_[];
    lds_ptr lds = (lds_ptr)smem_;
    const int tid = threadIdx.x, wid = __builtin_amdgcn_readfirstlane(tid >> 6), lane = tid & 63;
    const int l32 = lane & 31, hi = lane >> 5;
    const int xcd = blockIdx.x & 7, idx = blockIdx.x >> 3, rt = xcd * 4 + (idx >> 3), ct = idx & 7;
    const char* Af = p.ws + WS_A + (size_t)rt * 2 * NK16 * 1024;
    const int srow = lane >> 3;
    const unsigned wsrc = (unsigned)(srow * 1024 + ct * 128 + (((lane & 7) ^ (((srow >> 1) & 1) << 2)) * 16));
    const int tq = (lane & 15) >> 2, tp = lane & 3, tb = (lane >> 4) & 1;
    unsigned boff[2];
#pragma unroll
    for (int nt = 0; nt < 2; ++nt) boff[nt] = (unsigned)(wid * 16384 + (4 * hi + tq) * 128 + 64 * (nt ^ ((tq >> 1) & 1)) + 32 * tb + 8 * tp);
    f32x16 acc[2][2];
#pragma unroll
    for (int a = 0; a < 2; ++a) { acc[a][0] = f32x16{}; acc[a][1] = f32x16{}; }
    constexpr int NS = NK16 / 4;
    auto wptr = [&](int k16) -> const char* { return k16 < 256 ? (const char*)p.wo + (size_t)k16 * 16 * 1024 : (const char*)p.wob + (size_t)(k16 - 256) * 16 * 1024; };
    for (int i = 0; i < NS; ++i) {
        const int k16 = i * 4 + wid;
        const char* w = wptr(k16) + wsrc;
        const int slot = wid * 16384 + (i & 7) * 2048;
        glds16(w, lds + slot); glds16(w + 8 * 1024, lds + slot + 1024);
        const bf16x8 a0 = *(const bf16x8*)(Af + (size_t)k16 * 1024 + lane * 16);
        const bf16x8 a1 = *(const bf16x8*)(Af + (size_t)(NK16 + k16) * 1024 + lane * 16);
        WAIT_V(0);
        const int so = (i & 7) * 2048;
#pragma unroll
        for (int nt = 0; nt < 2; ++nt) {
            const bf16x8 wf = cat8(vtr(lds + boff[nt] + so), vtr(lds + boff[nt] + so + 1024));
            acc[nt][0] = MFMA32(wf, a0, acc[nt][0]);
            acc[nt][1] = MFMA32(wf, a1, acc[nt][1]);
        }
        WAIT_L(0);
    }
    WAIT_L(0); BAR();
    LAS float* red = (LAS float*)lds;
#pragma unroll
    for (int nt = 0; nt < 2; ++nt)
#pragma unroll
        for (int rb = 0; rb < 2; ++rb)
#pragma unroll
            for (int g4 = 0; g4 < 4; ++g4)
                *(LAS f32x4*)(red + (((wid * 4 + nt * 2 + rb) * 4 + g4) * 64 + lane) * 4) = (f32x4){acc[nt][rb][4 * g4], acc[nt][rb][4 * g4 + 1], acc[nt][rb][4 * g4 + 2], acc[nt][rb][4 * g4 + 3]};
    WAIT_L(0); BAR();
    const int nt = wid >> 1, rb = wid & 1;
    unsigned pk[8];
#pragma unroll
    for (int g4 = 0; g4 < 4; ++g4) {
        f32x4 s = {0.f, 0.f, 0.f, 0.f};
#pragma unroll
        for (int w = 0; w < 4; ++w) s += *(LAS const f32x4*)(red + (((w * 4 + wid) * 4 + g4) * 64 + lane) * 4);
        pk[2 * g4] = cvtpk(s[0], s[1]); pk[2 * g4 + 1] = cvtpk(s[2], s[3]);
    }
    bf16_t* orow = p.out + (size_t)(rt * 64 + rb * 32 + l32) * DM + ct * 64 + nt * 32;
#pragma unroll
    for (int g2 = 0; g2 < 2; ++g2) {
        auto r0 = __builtin_amdgcn_permlane32_swap(pk[4 * g2], pk[4 * g2 + 2], false, false);
        auto r1 = __builtin_amdgcn_permlane32_swap(pk[4 * g2 + 1], pk[4 * g2 + 3], false, false);
        u32x4 w = {r0[0], r1[0], r0[1], r1[1]};
        *(u32x4*)(orow + 16 * g2 + 8 * hi) = w;
    }
}

extern "C" void kernel_launch(void* const* d_in, const int* in_sizes, int n_in, void* d_out, int out_size, void* d_ws, size_t ws_size, hipStream_t stream) {
    static bool init = false;
    if (!init) {
        (void)hipFuncSetAttribute((const void*)proj_kernel, hipFuncAttributeMaxDynamicSharedMemorySize, P_LDS);
        (void)hipFuncSetAttribute((const void*)attn_kernel, hipFuncAttributeMaxDynamicSharedMemorySize, A_LDS);
        (void)hipFuncSetAttribute((const void*)out_kernel, hipFuncAttributeMaxDynamicSharedMemorySize, O_LDS);
        init = true;
    }
    ProjArgs pa;
    pa.xa = (const bf16_t*)d_in[0]; pa.ckv = (const bf16_t*)d_in[1]; pa.wq = (const bf16_t*)d_in[2]; pa.wuk = (const bf16_t*)d_in[3]; pa.wuv = (const bf16_t*)d_in[4];
    pa.xb = (const bf16_t*)d_in[0]; pa.wqkv = (const bf16_t*)d_in[7]; pa.gw = (const bf16_t*)d_in[9]; pa.ws = (char*)d_ws;
    proj_kernel<<<256, 512, P_LDS, stream>>>(pa);
    AttnArgs aa; aa.ws = (char*)d_ws;
    attn_kernel<<<256, 256, A_LDS, stream>>>(aa);
    OutArgs oa; oa.ws = (const char*)d_ws; oa.wo = (const bf16_t*)d_in[5]; oa.wob = (const bf16_t*)d_in[8]; oa.out = (bf16_t*)d_out;
    out_kernel<<<256, 256, O_LDS, stream>>>(oa);
}
```

```cpp
#include <hip/hip_runtime.h>
#include <stdint.h>

typedef unsigned short bf16_t;
typedef float f32x2 __attribute__((ext_vector_type(2)));
typedef float f32x4 __attribute__((ext_vector_type(4)));
typedef float f32x16 __attribute__((ext_vector_type(16)));
typedef unsigned u32x4 __attribute__((ext_vector_type(4)));
typedef __bf16 bf16x2_t __attribute__((ext_vector_type(2)));
#define LAS __attribute__((address_space(3)))
#define DI __device__ __forceinline__

constexpr int SEQ = 2048, DM = 512, NH = 32, HD = 128;
constexpr int NK16 = 288;
constexpr size_t WS_K = 0;
constexpr size_t WS_V = WS_K + (size_t)NH * SEQ * HD * 2;
constexpr size_t WS_A = WS_V + (size_t)NH * SEQ * HD * 2;
constexpr size_t WS_PQ = WS_A + (size_t)64 * NK16 * 1024;
constexpr size_t WS_KVF = WS_PQ + (size_t)NH * SEQ * 16 * 2;
constexpr size_t KVF_STRIDE = 8448;
constexpr size_t WS_G = WS_KVF + (size_t)NH * KVF_STRIDE;
constexpr size_t WS_F = WS_G + SEQ * 4;
constexpr size_t WS_X = WS_F + 64 * 256;
constexpr size_t WS_END = WS_X + (size_t)64 * 16 * 16384;

DI unsigned cvtpk(float lo, float hi) { f32x2 v = {lo, hi}; bf16x2_t b = __builtin_convertvector(v, bf16x2_t); return __builtin_bit_cast(unsigned, b); }

#define CODE1_LINES 448
#define CODE2_LINES 88
struct PreArgs { const bf16_t* xa; const bf16_t* gw; const bf16_t* wob; const bf16_t* wuk; const bf16_t* wuv; const bf16_t* ckv; char* ws; };
DI u32x4 make_srd(const void* base, unsigned bytes) {
    const unsigned long long a = (unsigned long long)base;
    return (u32x4){(unsigned)a, (unsigned)(a >> 32) & 0xffffu, bytes, 0x00020000u};
}
__global__ __launch_bounds__(512) void pre_kernel(PreArgs p) {
    const int tid = threadIdx.x, wid = __builtin_amdgcn_readfirstlane(tid >> 6), lane = tid & 63;
    if (wid >= 4) {
        const int gw_ = blockIdx.x * 4 + (wid - 4);
        const char* t0 = (const char*)p.wob + (size_t)(gw_ * 4 + (lane & 3)) * 128;
        unsigned long long pc1_, pc2_;
        asm volatile("s_getpc_b64 s[90:91]\n\ts_add_u32 s90, s90, _Z11attn_kernel8AttnArgs@rel32@lo+4\n\ts_addc_u32 s91, s91, _Z11attn_kernel8AttnArgs@rel32@hi+12\n\ts_mov_b64 %0, s[90:91]" : "=s"(pc1_) :: "scc", "s90", "s91");
        asm volatile("s_getpc_b64 s[90:91]\n\ts_add_u32 s90, s90, _Z10out_kernel7OutArgs@rel32@lo+4\n\ts_addc_u32 s91, s91, _Z10out_kernel7OutArgs@rel32@hi+12\n\ts_mov_b64 %0, s[90:91]" : "=s"(pc2_) :: "scc", "s90", "s91");
        const int cl_ = (gw_ & 15) * 64 + lane;
        const char* t2 = cl_ < CODE1_LINES ? (const char*)pc1_ + (size_t)cl_ * 128
                                           : (const char*)pc2_ + (size_t)(cl_ - CODE1_LINES < CODE2_LINES ? cl_ - CODE1_LINES : 0) * 128;
        const char* t3 = lane < 32 ? (const char*)p.wuk + (size_t)(gw_ * 32 + lane) * 128 : (const char*)p.wuv + (size_t)(gw_ * 32 + lane - 32) * 128;
        const char* t4 = (const char*)p.ckv + (size_t)(gw_ * 16 + (lane & 15)) * 128;
        unsigned d0_, d1_, d2_, d3_;
        asm volatile("global_load_dword %0, %4, off\n\tglobal_load_dword %1, %5, off\n\tglobal_load_dword %2, %6, off\n\tglobal_load_dword %3, %7, off"
                     : "=&v"(d0_), "=&v"(d1_), "=&v"(d2_), "=&v"(d3_) : "v"(t4), "v"(t3), "v"(t2), "v"(t0) : "memory");
        return;
    }
    if (tid == 0) ((unsigned*)(p.ws + WS_F))[blockIdx.x * 16] = 0u;
    if (blockIdx.x < NH && tid < 2) *(unsigned*)(p.ws + WS_KVF + (size_t)blockIdx.x * KVF_STRIDE + 8192 + tid * 64) = 0u;
    const int grow = blockIdx.x * 8 + wid * 2;
    const u32x4 gx0 = *(const u32x4*)(p.xa + (size_t)grow * DM + lane * 8), gx1 = *(const u32x4*)(p.xa + (size_t)(grow + 1) * DM + lane * 8);
    const u32x4 gwv = *(const u32x4*)(p.gw + lane * 8);
    float s0 = 0.f, s1 = 0.f;
#pragma unroll
    for (int e = 0; e < 4; ++e) {
        const float wl = __uint_as_float(gwv[e] << 16), wh = __uint_as_float(gwv[e] & 0xffff0000u);
        s0 += __uint_as_float(gx0[e] << 16) * wl + __uint_as_float(gx0[e] & 0xffff0000u) * wh;
        s1 += __uint_as_float(gx1[e] << 16) * wl + __uint_as_float(gx1[e] & 0xffff0000u) * wh;
    }
#pragma unroll
    for (int o = 32; o >= 1; o >>= 1) { s0 += __shfl_xor(s0, o); s1 += __shfl_xor(s1, o); }
    if (lane < 2) ((float*)(p.ws + WS_G))[grow + lane] = 1.f / (1.f + __expf(-(lane ? s1 : s0)));
}

constexpr int A_LDS = 163840;
#define ATTN_SRD_BYTES ((unsigned)WS_END)
#define ATTN_ASM_TEXT \
"v_mov_b32 v36, %16\n\t" \
"s_sub_u32 s75, %7, %8\n\t" \
"s_lshr_b32 s75, s75, 13\n\t" \
"s_lshr_b32 s76, s75, 1\n\t" \
"s_and_b32 s77, s75, 1\n\t" \
"s_lshl_b32 s77, s77, 14\n\t" \
"s_add_u32 s77, s77, %8\n\t" \
"s_lshl_b32 s54, s76, 14\n\t" \
"s_add_u32 s54, s54, %8\n\t" \
"s_sub_u32 s78, %6, 1\n\t" \
"s_lshl_b32 s78, s78, 10\n\t" \
"s_add_u32 s78, s78, %9\n\t" \
"s_add_u32 s79, %9, 8192\n\t" \
"v_add_u32 v16, s54, %16\n\t" \
"v_add_u32 v17, 0x10000, v16\n\t" \
"v_mov_b32 v37, %17\n\t" \
"s_mov_b32 s42, %1\n\t" \
"s_mov_b32 s43, %2\n\t" \
"s_mov_b32 s44, 0\n\t" \
"s_mov_b32 s46, 0\n\t" \
"s_mov_b32 s45, %5\n\t" \
"s_mov_b32 s47, %5\n\t" \
"s_mov_b32 s48, %7\n\t" \
"s_add_u32 s49, %7, 0x10000\n\t" \
"s_mov_b32 s52, 0\n\t" \
"s_sub_u32 s53, %5, 1\n\t" \
"s_mov_b32 s40, %5\n\t" \
"s_mov_b32 s60, 0xc1f00000\n\t" \
"s_lshl_b32 s58, s53, 9\n\t" \
"s_add_u32 s58, s58, %4\n\t" \
"v_lshrrev_b32 v38, 2, v36\n\t" \
"v_and_b32 v38, 0x7c, v38\n\t" \
"buffer_load_dword v38, v38, %0, s58 offen\n\t" \
"s_sub_u32 s70, %7, %8\n\t" \
"s_lshr_b32 s70, s70, 2\n\t" \
"s_add_u32 s70, s70, %8\n\t" \
"s_sub_u32 s71, %5, 1\n\t" \
"s_lshl_b32 s71, s71, 17\n\t" \
"s_sub_u32 s72, %5, 1\n\t" \
"s_lshl_b32 s72, s72, 17\n\t" \
"s_add_u32 s72, s72, 0x10000\n\t" \
"s_sub_u32 s73, %6, 1\n\t" \
"s_lshl_b32 s73, s73, 17\n\t" \
"s_sub_u32 s74, %6, 1\n\t" \
"s_lshl_b32 s74, s74, 17\n\t" \
"s_add_u32 s74, s74, 0x10000\n\t" \
"s_sub_u32 s80, %6, 1\n\t" \
"s_lshl_b32 s80, s80, 18\n\t" \
"s_add_u32 m0, s70, 0\n\t" \
"s_add_u32 s54, s80, 0\n\t" \
"buffer_load_dwordx4 %18, %26, s54 offen lds\n\t" \
"s_add_u32 m0, s70, 1024\n\t" \
"s_add_u32 s55, s80, 0\n\t" \
"buffer_load_dwordx4 %19, %26, s55 offen lds\n\t" \
"s_add_u32 m0, s70, 8192\n\t" \
"s_add_u32 s54, s80, 65536\n\t" \
"buffer_load_dwordx4 %18, %26, s54 offen lds\n\t" \
"s_add_u32 m0, s70, 9216\n\t" \
"s_add_u32 s55, s80, 65536\n\t" \
"buffer_load_dwordx4 %19, %26, s55 offen lds\n\t" \
"s_add_u32 m0, s70, 16384\n\t" \
"s_add_u32 s54, s80, 131072\n\t" \
"buffer_load_dwordx4 %18, %26, s54 offen lds\n\t" \
"s_add_u32 m0, s70, 17408\n\t" \
"s_add_u32 s55, s80, 131072\n\t" \
"buffer_load_dwordx4 %19, %26, s55 offen lds\n\t" \
"s_add_u32 m0, s70, 24576\n\t" \
"s_add_u32 s54, s80, 196608\n\t" \
"buffer_load_dwordx4 %18, %26, s54 offen lds\n\t" \
"s_add_u32 m0, s70, 25600\n\t" \
"s_add_u32 s55, s80, 196608\n\t" \
"buffer_load_dwordx4 %19, %26, s55 offen lds\n\t" \
"s_add_u32 m0, s70, 32768\n\t" \
"s_mov_b32 s54, 0\n\t" \
"buffer_load_dwordx4 %20, %27, s54 offen lds\n\t" \
"s_add_u32 m0, s70, 33792\n\t" \
"s_mov_b32 s55, 0\n\t" \
"buffer_load_dwordx4 %21, %27, s55 offen lds\n\t" \
"s_add_u32 m0, s70, 40960\n\t" \
"s_mov_b32 s54, 262144\n\t" \
"buffer_load_dwordx4 %20, %27, s54 offen lds\n\t" \
"s_add_u32 m0, s70, 41984\n\t" \
"s_mov_b32 s55, 262144\n\t" \
"buffer_load_dwordx4 %21, %27, s55 offen lds\n\t" \
"s_add_u32 m0, s70, 49152\n\t" \
"s_mov_b32 s54, 0\n\t" \
"buffer_load_dwordx4 %20, %28, s54 offen lds\n\t" \
"s_add_u32 m0, s70, 50176\n\t" \
"s_mov_b32 s55, 0\n\t" \
"buffer_load_dwordx4 %21, %28, s55 offen lds\n\t" \
"s_add_u32 m0, s70, 57344\n\t" \
"s_mov_b32 s54, 262144\n\t" \
"buffer_load_dwordx4 %20, %28, s54 offen lds\n\t" \
"s_add_u32 m0, s70, 58368\n\t" \
"s_mov_b32 s55, 262144\n\t" \
"buffer_load_dwordx4 %21, %28, s55 offen lds\n\t" \
"s_lshr_b32 s56, s75, 1\n\t" \
"s_lshl_b32 s56, s56, 14\n\t" \
"s_and_b32 s57, s75, 1\n\t" \
"s_lshl_b32 s57, s57, 7\n\t" \
"v_xor_b32 v128, 0, %24\n\t" \
"v_add_u32 v128, s56, v128\n\t" \
"v_xor_b32 v129, 32, %24\n\t" \
"v_add_u32 v129, s56, v129\n\t" \
"v_xor_b32 v130, 64, %24\n\t" \
"v_add_u32 v130, s56, v130\n\t" \
"v_xor_b32 v131, 96, %24\n\t" \
"v_add_u32 v131, s56, v131\n\t" \
"v_xor_b32 v132, 0, %25\n\t" \
"v_xor_b32 v132, s57, v132\n\t" \
"v_xor_b32 v133, 16, %25\n\t" \
"v_xor_b32 v133, s57, v133\n\t" \
"v_xor_b32 v134, 64, %25\n\t" \
"v_xor_b32 v134, s57, v134\n\t" \
"v_xor_b32 v135, 80, %25\n\t" \
"v_xor_b32 v135, s57, v135\n\t" \
"v_add_u32 v136, 0, v128\n\t" \
"v_add_u32 v137, 0, v129\n\t" \
"v_add_u32 v138, 0, v130\n\t" \
"v_add_u32 v139, 0, v131\n\t" \
"v_add_u32 v144, 32768, v132\n\t" \
"v_add_u32 v145, 32768, v133\n\t" \
"v_add_u32 v146, 32768, v134\n\t" \
"v_add_u32 v147, 32768, v135\n\t" \
"s_waitcnt vmcnt(0)\n\t" \
"s_barrier\n\t" \
"ds_read_b64_tr_b16 v[80:81], v144 offset:0\n\t" \
"ds_read_b64_tr_b16 v[82:83], v145 offset:1024\n\t" \
"ds_read_b128 v[64:67], v136 offset:0\n\t" \
"ds_read_b64_tr_b16 v[88:89], v144 offset:16384\n\t" \
"ds_read_b64_tr_b16 v[90:91], v145 offset:17408\n\t" \
"ds_read_b64_tr_b16 v[84:85], v146 offset:0\n\t" \
"ds_read_b64_tr_b16 v[86:87], v147 offset:1024\n\t" \
"ds_read_b64_tr_b16 v[92:93], v146 offset:16384\n\t" \
"ds_read_b64_tr_b16 v[94:95], v147 offset:17408\n\t" \
"ds_read_b128 v[68:71], v136 offset:4096\n\t" \
"ds_read_b128 v[72:75], v136 offset:8192\n\t" \
"ds_read_b128 v[76:79], v136 offset:12288\n\t" \
"s_waitcnt lgkmcnt(9)\n\t" \
"v_mfma_f32_32x32x16_bf16 a[0:15], v[80:83], v[64:67], 0\n\t" \
"ds_read_b64_tr_b16 v[112:113], v144 offset:4096\n\t" \
"s_add_u32 m0, s70, 65536\n\t" \
"s_add_u32 s54, s80, 128\n\t" \
"buffer_load_dwordx4 %18, %26, s54 offen lds\n\t" \
"s_waitcnt lgkmcnt(8)\n\t" \
"v_mfma_f32_32x32x16_bf16 a[128:143], v[64:67], v[88:91], 0\n\t" \
"ds_read_b64_tr_b16 v[114:115], v145 offset:5120\n\t" \
"s_waitcnt lgkmcnt(7)\n\t" \
"v_mfma_f32_32x32x16_bf16 a[16:31], v[84:87], v[64:67], 0\n\t" \
"s_add_u32 m0, s70, 66560\n\t" \
"s_add_u32 s55, s80, 128\n\t" \
"buffer_load_dwordx4 %19, %26, s55 offen lds\n\t" \
"s_waitcnt lgkmcnt(5)\n\t" \
"v_mfma_f32_32x32x16_bf16 a[144:159], v[64:67], v[92:95], 0\n\t" \
"ds_read_b128 v[96:99], v137 offset:0\n\t" \
"s_waitcnt lgkmcnt(5)\n\t" \
"v_mfma_f32_32x32x16_bf16 a[32:47], v[80:83], v[68:71], 0\n\t" \
"s_add_u32 m0, s70, 73728\n\t" \
"s_add_u32 s54, s80, 65664\n\t" \
"buffer_load_dwordx4 %18, %26, s54 offen lds\n\t" \
"ds_read_b64_tr_b16 v[120:121], v144 offset:20480\n\t" \
"v_mfma_f32_32x32x16_bf16 a[160:175], v[68:71], v[88:91], 0\n\t" \
"s_add_u32 m0, s70, 74752\n\t" \
"s_add_u32 s55, s80, 65664\n\t" \
"buffer_load_dwordx4 %19, %26, s55 offen lds\n\t" \
"v_mfma_f32_32x32x16_bf16 a[48:63], v[84:87], v[68:71], 0\n\t" \
"ds_read_b64_tr_b16 v[122:123], v145 offset:21504\n\t" \
"v_mfma_f32_32x32x16_bf16 a[176:191], v[68:71], v[92:95], 0\n\t" \
"s_add_u32 m0, s70, 81920\n\t" \
"s_add_u32 s54, s80, 131200\n\t" \
"buffer_load_dwordx4 %18, %26, s54 offen lds\n\t" \
"s_waitcnt lgkmcnt(6)\n\t" \
"v_mfma_f32_32x32x16_bf16 a[64:79], v[80:83], v[72:75], 0\n\t" \
"ds_read_b64_tr_b16 v[116:117], v146 offset:4096\n\t" \
"s_add_u32 m0, s70, 82944\n\t" \
"s_add_u32 s55, s80, 131200\n\t" \
"buffer_load_dwordx4 %19, %26, s55 offen lds\n\t" \
"v_mfma_f32_32x32x16_bf16 a[192:207], v[72:75], v[88:91], 0\n\t" \
"ds_read_b64_tr_b16 v[118:119], v147 offset:5120\n\t" \
"v_mfma_f32_32x32x16_bf16 a[80:95], v[84:87], v[72:75], 0\n\t" \
"s_add_u32 m0, s70, 90112\n\t" \
"s_add_u32 s54, s80, 196736\n\t" \
"buffer_load_dwordx4 %18, %26, s54 offen lds\n\t" \
"v_mfma_f32_32x32x16_bf16 a[208:223], v[72:75], v[92:95], 0\n\t" \
"ds_read_b64_tr_b16 v[124:125], v146 offset:20480\n\t" \
"s_waitcnt lgkmcnt(8)\n\t" \
"v_mfma_f32_32x32x16_bf16 a[96:111], v[80:83], v[76:79], 0\n\t" \
"s_add_u32 m0, s70, 91136\n\t" \
"s_add_u32 s55, s80, 196736\n\t" \
"buffer_load_dwordx4 %19, %26, s55 offen lds\n\t" \
"ds_read_b64_tr_b16 v[126:127], v147 offset:21504\n\t" \
"v_mfma_f32_32x32x16_bf16 a[224:239], v[76:79], v[88:91], 0\n\t" \
"ds_read_b128 v[100:103], v137 offset:4096\n\t" \
"v_mfma_f32_32x32x16_bf16 a[112:127], v[84:87], v[76:79], 0\n\t" \
"ds_read_b128 v[104:107], v137 offset:8192\n\t" \
"v_mfma_f32_32x32x16_bf16 a[240:255], v[76:79], v[92:95], 0\n\t" \
"ds_read_b128 v[108:111], v137 offset:12288\n\t" \
"s_waitcnt lgkmcnt(9)\n\t" \
"v_mfma_f32_32x32x16_bf16 a[0:15], v[112:115], v[96:99], a[0:15]\n\t" \
"ds_read_b64_tr_b16 v[80:81], v144 offset:8192\n\t" \
"s_add_u32 m0, s70, 98304\n\t" \
"s_mov_b32 s54, 524288\n\t" \
"buffer_load_dwordx4 %20, %27, s54 offen lds\n\t" \
"s_waitcnt lgkmcnt(8)\n\t" \
"v_mfma_f32_32x32x16_bf16 a[128:143], v[96:99], v[120:123], a[128:143]\n\t" \
"ds_read_b64_tr_b16 v[82:83], v145 offset:9216\n\t" \
"s_waitcnt lgkmcnt(7)\n\t" \
"v_mfma_f32_32x32x16_bf16 a[16:31], v[116:119], v[96:99], a[16:31]\n\t" \
"s_add_u32 m0, s70, 99328\n\t" \
"s_mov_b32 s55, 524288\n\t" \
"buffer_load_dwordx4 %21, %27, s55 offen lds\n\t" \
"s_waitcnt lgkmcnt(5)\n\t" \
"v_mfma_f32_32x32x16_bf16 a[144:159], v[96:99], v[124:127], a[144:159]\n\t" \
"ds_read_b128 v[64:67], v138 offset:0\n\t" \
"s_waitcnt lgkmcnt(5)\n\t" \
"v_mfma_f32_32x32x16_bf16 a[32:47], v[112:115], v[100:103], a[32:47]\n\t" \
"s_add_u32 m0, s70, 106496\n\t" \
"s_mov_b32 s54, 786432\n\t" \
"buffer_load_dwordx4 %20, %27, s54 offen lds\n\t" \
"ds_read_b64_tr_b16 v[88:89], v144 offset:24576\n\t" \
"v_mfma_f32_32x32x16_bf16 a[160:175], v[100:103], v[120:123], a[160:175]\n\t" \
"s_add_u32 m0, s70, 107520\n\t" \
"s_mov_b32 s55, 786432\n\t" \
"buffer_load_dwordx4 %21, %27, s55 offen lds\n\t" \
"v_mfma_f32_32x32x16_bf16 a[48:63], v[116:119], v[100:103], a[48:63]\n\t" \
"ds_read_b64_tr_b16 v[90:91], v145 offset:25600\n\t" \
"v_mfma_f32_32x32x16_bf16 a[176:191], v[100:103], v[124:127], a[176:191]\n\t" \
"s_add_u32 m0, s70, 114688\n\t" \
"s_mov_b32 s54, 524288\n\t" \
"buffer_load_dwordx4 %20, %28, s54 offen lds\n\t" \
"s_waitcnt lgkmcnt(6)\n\t" \
"v_mfma_f32_32x32x16_bf16 a[64:79], v[112:115], v[104:107], a[64:79]\n\t" \
"ds_read_b64_tr_b16 v[84:85], v146 offset:8192\n\t" \
"s_add_u32 m0, s70, 115712\n\t" \
"s_mov_b32 s55, 524288\n\t" \
"buffer_load_dwordx4 %21, %28, s55 offen lds\n\t" \
"v_mfma_f32_32x32x16_bf16 a[192:207], v[104:107], v[120:123], a[192:207]\n\t" \
"ds_read_b64_tr_b16 v[86:87], v147 offset:9216\n\t" \
"v_mfma_f32_32x32x16_bf16 a[80:95], v[116:119], v[104:107], a[80:95]\n\t" \
"s_add_u32 m0, s70, 122880\n\t" \
"s_mov_b32 s54, 786432\n\t" \
"buffer_load_dwordx4 %20, %28, s54 offen lds\n\t" \
"v_mfma_f32_32x32x16_bf16 a[208:223], v[104:107], v[124:127], a[208:223]\n\t" \
"ds_read_b64_tr_b16 v[92:93], v146 offset:24576\n\t" \
"s_waitcnt lgkmcnt(8)\n\t" \
"v_mfma_f32_32x32x16_bf16 a[96:111], v[112:115], v[108:111], a[96:111]\n\t" \
"s_add_u32 m0, s70, 123904\n\t" \
"s_mov_b32 s55, 786432\n\t" \
"buffer_load_dwordx4 %21, %28, s55 offen lds\n\t" \
"ds_read_b64_tr_b16 v[94:95], v147 offset:25600\n\t" \
"v_mfma_f32_32x32x16_bf16 a[224:239], v[108:111], v[120:123], a[224:239]\n\t" \
"ds_read_b128 v[68:71], v138 offset:4096\n\t" \
"v_mfma_f32_32x32x16_bf16 a[112:127], v[116:119], v[108:111], a[112:127]\n\t" \
"ds_read_b128 v[72:75], v138 offset:8192\n\t" \
"v_mfma_f32_32x32x16_bf16 a[240:255], v[108:111], v[124:127], a[240:255]\n\t" \
"ds_read_b128 v[76:79], v138 offset:12288\n\t" \
"s_waitcnt lgkmcnt(9)\n\t" \
"v_mfma_f32_32x32x16_bf16 a[0:15], v[80:83], v[64:67], a[0:15]\n\t" \
"ds_read_b64_tr_b16 v[112:113], v144 offset:12288\n\t" \
"s_add_u32 m0, s70, 131072\n\t" \
"s_add_u32 s54, s80, 256\n\t" \
"buffer_load_dwordx4 %18, %26, s54 offen lds\n\t" \
"s_waitcnt lgkmcnt(8)\n\t" \
"v_mfma_f32_32x32x16_bf16 a[128:143], v[64:67], v[88:91], a[128:143]\n\t" \
"ds_read_b64_tr_b16 v[114:115], v145 offset:13312\n\t" \
"s_add_u32 m0, s70, 132096\n\t" \
"s_add_u32 s55, s80, 256\n\t" \
"buffer_load_dwordx4 %19, %26, s55 offen lds\n\t" \
"s_waitcnt lgkmcnt(7)\n\t" \
"v_mfma_f32_32x32x16_bf16 a[16:31], v[84:87], v[64:67], a[16:31]\n\t" \
"ds_read_b128 v[96:99], v139 offset:0\n\t" \
"s_add_u32 m0, s70, 139264\n\t" \
"s_add_u32 s54, s80, 65792\n\t" \
"buffer_load_dwordx4 %18, %26, s54 offen lds\n\t" \
"s_waitcnt lgkmcnt(6)\n\t" \
"v_mfma_f32_32x32x16_bf16 a[144:159], v[64:67], v[92:95], a[144:159]\n\t" \
"ds_read_b64_tr_b16 v[120:121], v144 offset:28672\n\t" \
"s_waitcnt lgkmcnt(6)\n\t" \
"v_mfma_f32_32x32x16_bf16 a[32:47], v[80:83], v[68:71], a[32:47]\n\t" \
"s_add_u32 m0, s70, 140288\n\t" \
"s_add_u32 s55, s80, 65792\n\t" \
"buffer_load_dwordx4 %19, %26, s55 offen lds\n\t" \
"ds_read_b64_tr_b16 v[122:123], v145 offset:29696\n\t" \
"v_mfma_f32_32x32x16_bf16 a[160:175], v[68:71], v[88:91], a[160:175]\n\t" \
"s_add_u32 m0, s70, 147456\n\t" \
"s_add_u32 s54, s80, 131328\n\t" \
"buffer_load_dwordx4 %18, %26, s54 offen lds\n\t" \
"ds_read_b64_tr_b16 v[116:117], v146 offset:12288\n\t" \
"v_mfma_f32_32x32x16_bf16 a[48:63], v[84:87], v[68:71], a[48:63]\n\t" \
"s_add_u32 m0, s70, 148480\n\t" \
"s_add_u32 s55, s80, 131328\n\t" \
"buffer_load_dwordx4 %19, %26, s55 offen lds\n\t" \
"ds_read_b64_tr_b16 v[118:119], v147 offset:13312\n\t" \
"v_mfma_f32_32x32x16_bf16 a[176:191], v[68:71], v[92:95], a[176:191]\n\t" \
"s_add_u32 m0, s70, 155648\n\t" \
"s_add_u32 s54, s80, 196864\n\t" \
"buffer_load_dwordx4 %18, %26, s54 offen lds\n\t" \
"s_waitcnt lgkmcnt(8)\n\t" \
"v_mfma_f32_32x32x16_bf16 a[64:79], v[80:83], v[72:75], a[64:79]\n\t" \
"ds_read_b64_tr_b16 v[124:125], v146 offset:28672\n\t" \
"s_add_u32 m0, s70, 156672\n\t" \
"s_add_u32 s55, s80, 196864\n\t" \
"buffer_load_dwordx4 %19, %26, s55 offen lds\n\t" \
"v_mfma_f32_32x32x16_bf16 a[192:207], v[72:75], v[88:91], a[192:207]\n\t" \
"ds_read_b64_tr_b16 v[126:127], v147 offset:29696\n\t" \
"v_add_u32 v140, 65536, v128\n\t" \
"v_mfma_f32_32x32x16_bf16 a[80:95], v[84:87], v[72:75], a[80:95]\n\t" \
"ds_read_b128 v[100:103], v139 offset:4096\n\t" \
"v_add_u32 v141, 65536, v129\n\t" \
"v_mfma_f32_32x32x16_bf16 a[208:223], v[72:75], v[92:95], a[208:223]\n\t" \
"ds_read_b128 v[104:107], v139 offset:8192\n\t" \
"s_waitcnt lgkmcnt(11)\n\t" \
"v_mfma_f32_32x32x16_bf16 a[96:111], v[80:83], v[76:79], a[96:111]\n\t" \
"v_add_u32 v142, 65536, v130\n\t" \
"ds_read_b128 v[108:111], v139 offset:12288\n\t" \
"v_mfma_f32_32x32x16_bf16 a[224:239], v[76:79], v[88:91], a[224:239]\n\t" \
"v_add_u32 v143, 65536, v131\n\t" \
"v_add_u32 v148, 98304, v132\n\t" \
"v_mfma_f32_32x32x16_bf16 a[112:127], v[84:87], v[76:79], a[112:127]\n\t" \
"v_add_u32 v149, 98304, v133\n\t" \
"v_add_u32 v150, 98304, v134\n\t" \
"v_mfma_f32_32x32x16_bf16 a[240:255], v[76:79], v[92:95], a[240:255]\n\t" \
"v_add_u32 v151, 98304, v135\n\t" \
"s_waitcnt lgkmcnt(9)\n\t" \
"v_mfma_f32_32x32x16_bf16 a[0:15], v[112:115], v[96:99], a[0:15]\n\t" \
"s_waitcnt lgkmcnt(7)\n\t" \
"v_mfma_f32_32x32x16_bf16 a[128:143], v[96:99], v[120:123], a[128:143]\n\t" \
"s_waitcnt lgkmcnt(0)\n\t" \
"s_waitcnt vmcnt(8)\n\t" \
"s_barrier\n\t" \
"v_mfma_f32_32x32x16_bf16 a[16:31], v[116:119], v[96:99], a[16:31]\n\t" \
"ds_read_b64_tr_b16 v[80:81], v148 offset:0\n\t" \
"s_add_u32 m0, s70, 0\n\t" \
"s_mov_b32 s54, 1048576\n\t" \
"buffer_load_dwordx4 %20, %27, s54 offen lds\n\t" \
"v_mfma_f32_32x32x16_bf16 a[144:159], v[96:99], v[124:127], a[144:159]\n\t" \
"ds_read_b64_tr_b16 v[82:83], v149 offset:1024\n\t" \
"v_mfma_f32_32x32x16_bf16 a[32:47], v[112:115], v[100:103], a[32:47]\n\t" \
"s_add_u32 m0, s70, 1024\n\t" \
"s_mov_b32 s55, 1048576\n\t" \
"buffer_load_dwordx4 %21, %27, s55 offen lds\n\t" \
"v_mfma_f32_32x32x16_bf16 a[160:175], v[100:103], v[120:123], a[160:175]\n\t" \
"ds_read_b128 v[64:67], v140 offset:0\n\t" \
"v_mfma_f32_32x32x16_bf16 a[48:63], v[116:119], v[100:103], a[48:63]\n\t" \
"s_add_u32 m0, s70, 8192\n\t" \
"s_mov_b32 s54, 1310720\n\t" \
"buffer_load_dwordx4 %20, %27, s54 offen lds\n\t" \
"v_mfma_f32_32x32x16_bf16 a[176:191], v[100:103], v[124:127], a[176:191]\n\t" \
"ds_read_b64_tr_b16 v[88:89], v148 offset:16384\n\t" \
"v_mfma_f32_32x32x16_bf16 a[64:79], v[112:115], v[104:107], a[64:79]\n\t" \
"s_add_u32 m0, s70, 9216\n\t" \
"s_mov_b32 s55, 1310720\n\t" \
"buffer_load_dwordx4 %21, %27, s55 offen lds\n\t" \
"v_mfma_f32_32x32x16_bf16 a[192:207], v[104:107], v[120:123], a[192:207]\n\t" \
"ds_read_b64_tr_b16 v[90:91], v149 offset:17408\n\t" \
"ds_read_b64_tr_b16 v[84:85], v150 offset:0\n\t" \
"v_mfma_f32_32x32x16_bf16 a[80:95], v[116:119], v[104:107], a[80:95]\n\t" \
"ds_read_b64_tr_b16 v[86:87], v151 offset:1024\n\t" \
"v_mfma_f32_32x32x16_bf16 a[208:223], v[104:107], v[124:127], a[208:223]\n\t" \
"ds_read_b64_tr_b16 v[92:93], v150 offset:16384\n\t" \
"v_mfma_f32_32x32x16_bf16 a[96:111], v[112:115], v[108:111], a[96:111]\n\t" \
"ds_read_b64_tr_b16 v[94:95], v151 offset:17408\n\t" \
"v_mfma_f32_32x32x16_bf16 a[224:239], v[108:111], v[120:123], a[224:239]\n\t" \
"ds_read_b128 v[68:71], v140 offset:4096\n\t" \
"v_mfma_f32_32x32x16_bf16 a[112:127], v[116:119], v[108:111], a[112:127]\n\t" \
"ds_read_b128 v[72:75], v140 offset:8192\n\t" \
"v_mfma_f32_32x32x16_bf16 a[240:255], v[108:111], v[124:127], a[240:255]\n\t" \
"ds_read_b128 v[76:79], v140 offset:12288\n\t" \
"s_waitcnt lgkmcnt(9)\n\t" \
"v_mfma_f32_32x32x16_bf16 a[0:15], v[80:83], v[64:67], a[0:15]\n\t" \
"ds_read_b64_tr_b16 v[112:113], v148 offset:4096\n\t" \
"s_waitcnt lgkmcnt(8)\n\t" \
"v_mfma_f32_32x32x16_bf16 a[128:143], v[64:67], v[88:91], a[128:143]\n\t" \
"s_add_u32 m0, s70, 16384\n\t" \
"s_mov_b32 s54, 1048576\n\t" \
"buffer_load_dwordx4 %20, %28, s54 offen lds\n\t" \
"s_waitcnt lgkmcnt(6)\n\t" \
"v_mfma_f32_32x32x16_bf16 a[16:31], v[84:87], v[64:67], a[16:31]\n\t" \
"ds_read_b64_tr_b16 v[114:115], v149 offset:5120\n\t" \
"s_waitcnt lgkmcnt(5)\n\t" \
"v_mfma_f32_32x32x16_bf16 a[144:159], v[64:67], v[92:95], a[144:159]\n\t" \
"s_add_u32 m0, s70, 17408\n\t" \
"s_mov_b32 s55, 1048576\n\t" \
"buffer_load_dwordx4 %21, %28, s55 offen lds\n\t" \
"s_waitcnt lgkmcnt(4)\n\t" \
"v_mfma_f32_32x32x16_bf16 a[32:47], v[80:83], v[68:71], a[32:47]\n\t" \
"ds_read_b128 v[96:99], v141 offset:0\n\t" \
"v_mfma_f32_32x32x16_bf16 a[160:175], v[68:71], v[88:91], a[160:175]\n\t" \
"s_add_u32 m0, s70, 24576\n\t" \
"s_mov_b32 s54, 1310720\n\t" \
"buffer_load_dwordx4 %20, %28, s54 offen lds\n\t" \
"v_mfma_f32_32x32x16_bf16 a[48:63], v[84:87], v[68:71], a[48:63]\n\t" \
"ds_read_b64_tr_b16 v[120:121], v148 offset:20480\n\t" \
"v_mfma_f32_32x32x16_bf16 a[176:191], v[68:71], v[92:95], a[176:191]\n\t" \
"s_add_u32 m0, s70, 25600\n\t" \
"s_mov_b32 s55, 1310720\n\t" \
"buffer_load_dwordx4 %21, %28, s55 offen lds\n\t" \
"s_waitcnt lgkmcnt(5)\n\t" \
"v_mfma_f32_32x32x16_bf16 a[64:79], v[80:83], v[72:75], a[64:79]\n\t" \
"ds_read_b64_tr_b16 v[122:123], v149 offset:21504\n\t" \
"v_mfma_f32_32x32x16_bf16 a[192:207], v[72:75], v[88:91], a[192:207]\n\t" \
"ds_read_b64_tr_b16 v[116:117], v150 offset:4096\n\t" \
"v_mfma_f32_32x32x16_bf16 a[80:95], v[84:87], v[72:75], a[80:95]\n\t" \
"ds_read_b64_tr_b16 v[118:119], v151 offset:5120\n\t" \
"v_mfma_f32_32x32x16_bf16 a[208:223], v[72:75], v[92:95], a[208:223]\n\t" \
"ds_read_b64_tr_b16 v[124:125], v150 offset:20480\n\t" \
"s_waitcnt lgkmcnt(8)\n\t" \
"v_mfma_f32_32x32x16_bf16 a[96:111], v[80:83], v[76:79], a[96:111]\n\t" \
"ds_read_b64_tr_b16 v[126:127], v151 offset:21504\n\t" \
"v_mfma_f32_32x32x16_bf16 a[224:239], v[76:79], v[88:91], a[224:239]\n\t" \
"ds_read_b128 v[100:103], v141 offset:4096\n\t" \
"v_mfma_f32_32x32x16_bf16 a[112:127], v[84:87], v[76:79], a[112:127]\n\t" \
"ds_read_b128 v[104:107], v141 offset:8192\n\t" \
"v_mfma_f32_32x32x16_bf16 a[240:255], v[76:79], v[92:95], a[240:255]\n\t" \
"ds_read_b128 v[108:111], v141 offset:12288\n\t" \
"s_waitcnt lgkmcnt(9)\n\t" \
"v_mfma_f32_32x32x16_bf16 a[0:15], v[112:115], v[96:99], a[0:15]\n\t" \
"ds_read_b64_tr_b16 v[80:81], v148 offset:8192\n\t" \
"s_waitcnt lgkmcnt(8)\n\t" \
"v_mfma_f32_32x32x16_bf16 a[128:143], v[96:99], v[120:123], a[128:143]\n\t" \
"s_add_u32 m0, s70, 32768\n\t" \
"s_add_u32 s54, s80, 384\n\t" \
"buffer_load_dwordx4 %18, %26, s54 offen lds\n\t" \
"s_waitcnt lgkmcnt(6)\n\t" \
"v_mfma_f32_32x32x16_bf16 a[16:31], v[116:119], v[96:99], a[16:31]\n\t" \
"ds_read_b64_tr_b16 v[82:83], v149 offset:9216\n\t" \
"s_waitcnt lgkmcnt(5)\n\t" \
"v_mfma_f32_32x32x16_bf16 a[144:159], v[96:99], v[124:127], a[144:159]\n\t" \
"s_add_u32 m0, s70, 33792\n\t" \
"s_add_u32 s55, s80, 384\n\t" \
"buffer_load_dwordx4 %19, %26, s55 offen lds\n\t" \
"s_waitcnt lgkmcnt(4)\n\t" \
"v_mfma_f32_32x32x16_bf16 a[32:47], v[112:115], v[100:103], a[32:47]\n\t" \
"ds_read_b128 v[64:67], v142 offset:0\n\t" \
"v_mfma_f32_32x32x16_bf16 a[160:175], v[100:103], v[120:123], a[160:175]\n\t" \
"s_add_u32 m0, s70, 40960\n\t" \
"s_add_u32 s54, s80, 65920\n\t" \
"buffer_load_dwordx4 %18, %26, s54 offen lds\n\t" \
"v_mfma_f32_32x32x16_bf16 a[48:63], v[116:119], v[100:103], a[48:63]\n\t" \
"ds_read_b64_tr_b16 v[88:89], v148 offset:24576\n\t" \
"v_mfma_f32_32x32x16_bf16 a[176:191], v[100:103], v[124:127], a[176:191]\n\t" \
"s_add_u32 m0, s70, 41984\n\t" \
"s_add_u32 s55, s80, 65920\n\t" \
"buffer_load_dwordx4 %19, %26, s55 offen lds\n\t" \
"s_waitcnt lgkmcnt(5)\n\t" \
"v_mfma_f32_32x32x16_bf16 a[64:79], v[112:115], v[104:107], a[64:79]\n\t" \
"ds_read_b64_tr_b16 v[90:91], v149 offset:25600\n\t" \
"v_mfma_f32_32x32x16_bf16 a[192:207], v[104:107], v[120:123], a[192:207]\n\t" \
"ds_read_b64_tr_b16 v[84:85], v150 offset:8192\n\t" \
"v_mfma_f32_32x32x16_bf16 a[80:95], v[116:119], v[104:107], a[80:95]\n\t" \
"ds_read_b64_tr_b16 v[86:87], v151 offset:9216\n\t" \
"v_mfma_f32_32x32x16_bf16 a[208:223], v[104:107], v[124:127], a[208:223]\n\t" \
"ds_read_b64_tr_b16 v[92:93], v150 offset:24576\n\t" \
"s_waitcnt lgkmcnt(8)\n\t" \
"v_mfma_f32_32x32x16_bf16 a[96:111], v[112:115], v[108:111], a[96:111]\n\t" \
"ds_read_b64_tr_b16 v[94:95], v151 offset:25600\n\t" \
"v_mfma_f32_32x32x16_bf16 a[224:239], v[108:111], v[120:123], a[224:239]\n\t" \
"ds_read_b128 v[68:71], v142 offset:4096\n\t" \
"v_mfma_f32_32x32x16_bf16 a[112:127], v[116:119], v[108:111], a[112:127]\n\t" \
"ds_read_b128 v[72:75], v142 offset:8192\n\t" \
"v_mfma_f32_32x32x16_bf16 a[240:255], v[108:111], v[124:127], a[240:255]\n\t" \
"ds_read_b128 v[76:79], v142 offset:12288\n\t" \
"s_waitcnt lgkmcnt(9)\n\t" \
"v_mfma_f32_32x32x16_bf16 a[0:15], v[80:83], v[64:67], a[0:15]\n\t" \
"ds_read_b64_tr_b16 v[112:113], v148 offset:12288\n\t" \
"s_add_u32 m0, s70, 49152\n\t" \
"s_add_u32 s54, s80, 131456\n\t" \
"buffer_load_dwordx4 %18, %26, s54 offen lds\n\t" \
"s_waitcnt lgkmcnt(8)\n\t" \
"v_mfma_f32_32x32x16_bf16 a[128:143], v[64:67], v[88:91], a[128:143]\n\t" \
"ds_read_b64_tr_b16 v[114:115], v149 offset:13312\n\t" \
"s_waitcnt lgkmcnt(7)\n\t" \
"v_mfma_f32_32x32x16_bf16 a[16:31], v[84:87], v[64:67], a[16:31]\n\t" \
"s_add_u32 m0, s70, 50176\n\t" \
"s_add_u32 s55, s80, 131456\n\t" \
"buffer_load_dwordx4 %19, %26, s55 offen lds\n\t" \
"ds_read_b128 v[96:99], v143 offset:0\n\t" \
"s_waitcnt lgkmcnt(6)\n\t" \
"v_mfma_f32_32x32x16_bf16 a[144:159], v[64:67], v[92:95], a[144:159]\n\t" \
"s_add_u32 m0, s70, 57344\n\t" \
"s_add_u32 s54, s80, 196992\n\t" \
"buffer_load_dwordx4 %18, %26, s54 offen lds\n\t" \
"s_waitcnt lgkmcnt(5)\n\t" \
"v_mfma_f32_32x32x16_bf16 a[32:47], v[80:83], v[68:71], a[32:47]\n\t" \
"ds_read_b64_tr_b16 v[120:121], v148 offset:28672\n\t" \
"s_add_u32 m0, s70, 58368\n\t" \
"s_add_u32 s55, s80, 196992\n\t" \
"buffer_load_dwordx4 %19, %26, s55 offen lds\n\t" \
"v_mfma_f32_32x32x16_bf16 a[160:175], v[68:71], v[88:91], a[160:175]\n\t" \
"ds_read_b64_tr_b16 v[122:123], v149 offset:29696\n\t" \
"v_mfma_f32_32x32x16_bf16 a[48:63], v[84:87], v[68:71], a[48:63]\n\t" \
"v_add_u32 v136, 131072, v128\n\t" \
"ds_read_b64_tr_b16 v[116:117], v150 offset:12288\n\t" \
"v_mfma_f32_32x32x16_bf16 a[176:191], v[68:71], v[92:95], a[176:191]\n\t" \
"v_add_u32 v137, 131072, v129\n\t" \
"s_waitcnt lgkmcnt(7)\n\t" \
"v_mfma_f32_32x32x16_bf16 a[64:79], v[80:83], v[72:75], a[64:79]\n\t" \
"ds_read_b64_tr_b16 v[118:119], v151 offset:13312\n\t" \
"v_add_u32 v138, 131072, v130\n\t" \
"v_mfma_f32_32x32x16_bf16 a[192:207], v[72:75], v[88:91], a[192:207]\n\t" \
"ds_read_b64_tr_b16 v[124:125], v150 offset:28672\n\t" \
"v_mfma_f32_32x32x16_bf16 a[80:95], v[84:87], v[72:75], a[80:95]\n\t" \
"v_add_u32 v139, 131072, v131\n\t" \
"ds_read_b64_tr_b16 v[126:127], v151 offset:29696\n\t" \
"v_mfma_f32_32x32x16_bf16 a[208:223], v[72:75], v[92:95], a[208:223]\n\t" \
"v_add_u32 v144, 0, v132\n\t" \
"s_waitcnt lgkmcnt(9)\n\t" \
"v_mfma_f32_32x32x16_bf16 a[96:111], v[80:83], v[76:79], a[96:111]\n\t" \
"ds_read_b128 v[100:103], v143 offset:4096\n\t" \
"v_add_u32 v145, 0, v133\n\t" \
"v_mfma_f32_32x32x16_bf16 a[224:239], v[76:79], v[88:91], a[224:239]\n\t" \
"ds_read_b128 v[104:107], v143 offset:8192\n\t" \
"v_mfma_f32_32x32x16_bf16 a[112:127], v[84:87], v[76:79], a[112:127]\n\t" \
"v_add_u32 v146, 0, v134\n\t" \
"ds_read_b128 v[108:111], v143 offset:12288\n\t" \
"v_mfma_f32_32x32x16_bf16 a[240:255], v[76:79], v[92:95], a[240:255]\n\t" \
"v_add_u32 v147, 0, v135\n\t" \
"s_waitcnt lgkmcnt(9)\n\t" \
"v_mfma_f32_32x32x16_bf16 a[0:15], v[112:115], v[96:99], a[0:15]\n\t" \
"s_waitcnt lgkmcnt(7)\n\t" \
"v_mfma_f32_32x32x16_bf16 a[128:143], v[96:99], v[120:123], a[128:143]\n\t" \
"s_waitcnt lgkmcnt(0)\n\t" \
"s_waitcnt vmcnt(8)\n\t" \
"s_barrier\n\t" \
"v_mfma_f32_32x32x16_bf16 a[16:31], v[116:119], v[96:99], a[16:31]\n\t" \
"ds_read_b64_tr_b16 v[80:81], v144 offset:0\n\t" \
"s_add_u32 m0, s70, 65536\n\t" \
"s_mov_b32 s54, 1572864\n\t" \
"buffer_load_dwordx4 %20, %27, s54 offen lds\n\t" \
"v_mfma_f32_32x32x16_bf16 a[144:159], v[96:99], v[124:127], a[144:159]\n\t" \
"ds_read_b64_tr_b16 v[82:83], v145 offset:1024\n\t" \
"v_mfma_f32_32x32x16_bf16 a[32:47], v[112:115], v[100:103], a[32:47]\n\t" \
"s_add_u32 m0, s70, 66560\n\t" \
"s_mov_b32 s55, 1572864\n\t" \
"buffer_load_dwordx4 %21, %27, s55 offen lds\n\t" \
"v_mfma_f32_32x32x16_bf16 a[160:175], v[100:103], v[120:123], a[160:175]\n\t" \
"ds_read_b128 v[64:67], v136 offset:0\n\t" \
"v_mfma_f32_32x32x16_bf16 a[48:63], v[116:119], v[100:103], a[48:63]\n\t" \
"s_add_u32 m0, s70, 73728\n\t" \
"s_mov_b32 s54, 1835008\n\t" \
"buffer_load_dwordx4 %20, %27, s54 offen lds\n\t" \
"v_mfma_f32_32x32x16_bf16 a[176:191], v[100:103], v[124:127], a[176:191]\n\t" \
"ds_read_b64_tr_b16 v[88:89], v144 offset:16384\n\t" \
"v_mfma_f32_32x32x16_bf16 a[64:79], v[112:115], v[104:107], a[64:79]\n\t" \
"s_add_u32 m0, s70, 74752\n\t" \
"s_mov_b32 s55, 1835008\n\t" \
"buffer_load_dwordx4 %21, %27, s55 offen lds\n\t" \
"v_mfma_f32_32x32x16_bf16 a[192:207], v[104:107], v[120:123], a[192:207]\n\t" \
"ds_read_b64_tr_b16 v[90:91], v145 offset:17408\n\t" \
"ds_read_b64_tr_b16 v[84:85], v146 offset:0\n\t" \
"v_mfma_f32_32x32x16_bf16 a[80:95], v[116:119], v[104:107], a[80:95]\n\t" \
"ds_read_b64_tr_b16 v[86:87], v147 offset:1024\n\t" \
"v_mfma_f32_32x32x16_bf16 a[208:223], v[104:107], v[124:127], a[208:223]\n\t" \
"ds_read_b64_tr_b16 v[92:93], v146 offset:16384\n\t" \
"v_mfma_f32_32x32x16_bf16 a[96:111], v[112:115], v[108:111], a[96:111]\n\t" \
"ds_read_b64_tr_b16 v[94:95], v147 offset:17408\n\t" \
"v_mfma_f32_32x32x16_bf16 a[224:239], v[108:111], v[120:123], a[224:239]\n\t" \
"ds_read_b128 v[68:71], v136 offset:4096\n\t" \
"v_mfma_f32_32x32x16_bf16 a[112:127], v[116:119], v[108:111], a[112:127]\n\t" \
"ds_read_b128 v[72:75], v136 offset:8192\n\t" \
"v_mfma_f32_32x32x16_bf16 a[240:255], v[108:111], v[124:127], a[240:255]\n\t" \
"ds_read_b128 v[76:79], v136 offset:12288\n\t" \
"s_waitcnt lgkmcnt(9)\n\t" \
"v_mfma_f32_32x32x16_bf16 a[0:15], v[80:83], v[64:67], a[0:15]\n\t" \
"ds_read_b64_tr_b16 v[112:113], v144 offset:4096\n\t" \
"s_waitcnt lgkmcnt(8)\n\t" \
"v_mfma_f32_32x32x16_bf16 a[128:143], v[64:67], v[88:91], a[128:143]\n\t" \
"s_add_u32 m0, s70, 81920\n\t" \
"s_mov_b32 s54, 1572864\n\t" \
"buffer_load_dwordx4 %20, %28, s54 offen lds\n\t" \
"s_waitcnt lgkmcnt(6)\n\t" \
"v_mfma_f32_32x32x16_bf16 a[16:31], v[84:87], v[64:67], a[16:31]\n\t" \
"ds_read_b64_tr_b16 v[114:115], v145 offset:5120\n\t" \
"s_waitcnt lgkmcnt(5)\n\t" \
"v_mfma_f32_32x32x16_bf16 a[144:159], v[64:67], v[92:95], a[144:159]\n\t" \
"s_add_u32 m0, s70, 82944\n\t" \
"s_mov_b32 s55, 1572864\n\t" \
"buffer_load_dwordx4 %21, %28, s55 offen lds\n\t" \
"s_waitcnt lgkmcnt(4)\n\t" \
"v_mfma_f32_32x32x16_bf16 a[32:47], v[80:83], v[68:71], a[32:47]\n\t" \
"ds_read_b128 v[96:99], v137 offset:0\n\t" \
"v_mfma_f32_32x32x16_bf16 a[160:175], v[68:71], v[88:91], a[160:175]\n\t" \
"s_add_u32 m0, s70, 90112\n\t" \
"s_mov_b32 s54, 1835008\n\t" \
"buffer_load_dwordx4 %20, %28, s54 offen lds\n\t" \
"v_mfma_f32_32x32x16_bf16 a[48:63], v[84:87], v[68:71], a[48:63]\n\t" \
"ds_read_b64_tr_b16 v[120:121], v144 offset:20480\n\t" \
"v_mfma_f32_32x32x16_bf16 a[176:191], v[68:71], v[92:95], a[176:191]\n\t" \
"s_add_u32 m0, s70, 91136\n\t" \
"s_mov_b32 s55, 1835008\n\t" \
"buffer_load_dwordx4 %21, %28, s55 offen lds\n\t" \
"s_waitcnt lgkmcnt(5)\n\t" \
"v_mfma_f32_32x32x16_bf16 a[64:79], v[80:83], v[72:75], a[64:79]\n\t" \
"ds_read_b64_tr_b16 v[122:123], v145 offset:21504\n\t" \
"v_mfma_f32_32x32x16_bf16 a[192:207], v[72:75], v[88:91], a[192:207]\n\t" \
"ds_read_b64_tr_b16 v[116:117], v146 offset:4096\n\t" \
"v_mfma_f32_32x32x16_bf16 a[80:95], v[84:87], v[72:75], a[80:95]\n\t" \
"ds_read_b64_tr_b16 v[118:119], v147 offset:5120\n\t" \
"v_mfma_f32_32x32x16_bf16 a[208:223], v[72:75], v[92:95], a[208:223]\n\t" \
"ds_read_b64_tr_b16 v[124:125], v146 offset:20480\n\t" \
"s_waitcnt lgkmcnt(8)\n\t" \
"v_mfma_f32_32x32x16_bf16 a[96:111], v[80:83], v[76:79], a[96:111]\n\t" \
"ds_read_b64_tr_b16 v[126:127], v147 offset:21504\n\t" \
"v_mfma_f32_32x32x16_bf16 a[224:239], v[76:79], v[88:91], a[224:239]\n\t" \
"ds_read_b128 v[100:103], v137 offset:4096\n\t" \
"v_mfma_f32_32x32x16_bf16 a[112:127], v[84:87], v[76:79], a[112:127]\n\t" \
"ds_read_b128 v[104:107], v137 offset:8192\n\t" \
"v_mfma_f32_32x32x16_bf16 a[240:255], v[76:79], v[92:95], a[240:255]\n\t" \
"ds_read_b128 v[108:111], v137 offset:12288\n\t" \
"s_waitcnt lgkmcnt(9)\n\t" \
"v_mfma_f32_32x32x16_bf16 a[0:15], v[112:115], v[96:99], a[0:15]\n\t" \
"ds_read_b64_tr_b16 v[80:81], v144 offset:8192\n\t" \
"s_waitcnt lgkmcnt(8)\n\t" \
"v_mfma_f32_32x32x16_bf16 a[128:143], v[96:99], v[120:123], a[128:143]\n\t" \
"s_add_u32 m0, s70, 98304\n\t" \
"s_add_u32 s54, s80, 512\n\t" \
"buffer_load_dwordx4 %18, %26, s54 offen lds\n\t" \
"s_waitcnt lgkmcnt(6)\n\t" \
"v_mfma_f32_32x32x16_bf16 a[16:31], v[116:119], v[96:99], a[16:31]\n\t" \
"ds_read_b64_tr_b16 v[82:83], v145 offset:9216\n\t" \
"s_waitcnt lgkmcnt(5)\n\t" \
"v_mfma_f32_32x32x16_bf16 a[144:159], v[96:99], v[124:127], a[144:159]\n\t" \
"s_add_u32 m0, s70, 99328\n\t" \
"s_add_u32 s55, s80, 512\n\t" \
"buffer_load_dwordx4 %19, %26, s55 offen lds\n\t" \
"s_waitcnt lgkmcnt(4)\n\t" \
"v_mfma_f32_32x32x16_bf16 a[32:47], v[112:115], v[100:103], a[32:47]\n\t" \
"ds_read_b128 v[64:67], v138 offset:0\n\t" \
"v_mfma_f32_32x32x16_bf16 a[160:175], v[100:103], v[120:123], a[160:175]\n\t" \
"s_add_u32 m0, s70, 106496\n\t" \
"s_add_u32 s54, s80, 66048\n\t" \
"buffer_load_dwordx4 %18, %26, s54 offen lds\n\t" \
"v_mfma_f32_32x32x16_bf16 a[48:63], v[116:119], v[100:103], a[48:63]\n\t" \
"ds_read_b64_tr_b16 v[88:89], v144 offset:24576\n\t" \
"v_mfma_f32_32x32x16_bf16 a[176:191], v[100:103], v[124:127], a[176:191]\n\t" \
"s_add_u32 m0, s70, 107520\n\t" \
"s_add_u32 s55, s80, 66048\n\t" \
"buffer_load_dwordx4 %19, %26, s55 offen lds\n\t" \
"s_waitcnt lgkmcnt(5)\n\t" \
"v_mfma_f32_32x32x16_bf16 a[64:79], v[112:115], v[104:107], a[64:79]\n\t" \
"ds_read_b64_tr_b16 v[90:91], v145 offset:25600\n\t" \
"v_mfma_f32_32x32x16_bf16 a[192:207], v[104:107], v[120:123], a[192:207]\n\t" \
"ds_read_b64_tr_b16 v[84:85], v146 offset:8192\n\t" \
"v_mfma_f32_32x32x16_bf16 a[80:95], v[116:119], v[104:107], a[80:95]\n\t" \
"ds_read_b64_tr_b16 v[86:87], v147 offset:9216\n\t" \
"v_mfma_f32_32x32x16_bf16 a[208:223], v[104:107], v[124:127], a[208:223]\n\t" \
"ds_read_b64_tr_b16 v[92:93], v146 offset:24576\n\t" \
"s_waitcnt lgkmcnt(8)\n\t" \
"v_mfma_f32_32x32x16_bf16 a[96:111], v[112:115], v[108:111], a[96:111]\n\t" \
"ds_read_b64_tr_b16 v[94:95], v147 offset:25600\n\t" \
"v_mfma_f32_32x32x16_bf16 a[224:239], v[108:111], v[120:123], a[224:239]\n\t" \
"ds_read_b128 v[68:71], v138 offset:4096\n\t" \
"v_mfma_f32_32x32x16_bf16 a[112:127], v[116:119], v[108:111], a[112:127]\n\t" \
"ds_read_b128 v[72:75], v138 offset:8192\n\t" \
"v_mfma_f32_32x32x16_bf16 a[240:255], v[108:111], v[124:127], a[240:255]\n\t" \
"ds_read_b128 v[76:79], v138 offset:12288\n\t" \
"s_waitcnt lgkmcnt(9)\n\t" \
"v_mfma_f32_32x32x16_bf16 a[0:15], v[80:83], v[64:67], a[0:15]\n\t" \
"ds_read_b64_tr_b16 v[112:113], v144 offset:12288\n\t" \
"s_add_u32 m0, s70, 114688\n\t" \
"s_add_u32 s54, s80, 131584\n\t" \
"buffer_load_dwordx4 %18, %26, s54 offen lds\n\t" \
"s_waitcnt lgkmcnt(8)\n\t" \
"v_mfma_f32_32x32x16_bf16 a[128:143], v[64:67], v[88:91], a[128:143]\n\t" \
"ds_read_b64_tr_b16 v[114:115], v145 offset:13312\n\t" \
"s_waitcnt lgkmcnt(7)\n\t" \
"v_mfma_f32_32x32x16_bf16 a[16:31], v[84:87], v[64:67], a[16:31]\n\t" \
"s_add_u32 m0, s70, 115712\n\t" \
"s_add_u32 s55, s80, 131584\n\t" \
"buffer_load_dwordx4 %19, %26, s55 offen lds\n\t" \
"ds_read_b128 v[96:99], v139 offset:0\n\t" \
"s_waitcnt lgkmcnt(6)\n\t" \
"v_mfma_f32_32x32x16_bf16 a[144:159], v[64:67], v[92:95], a[144:159]\n\t" \
"s_add_u32 m0, s70, 122880\n\t" \
"s_add_u32 s54, s80, 197120\n\t" \
"buffer_load_dwordx4 %18, %26, s54 offen lds\n\t" \
"s_waitcnt lgkmcnt(5)\n\t" \
"v_mfma_f32_32x32x16_bf16 a[32:47], v[80:83], v[68:71], a[32:47]\n\t" \
"ds_read_b64_tr_b16 v[120:121], v144 offset:28672\n\t" \
"s_add_u32 m0, s70, 123904\n\t" \
"s_add_u32 s55, s80, 197120\n\t" \
"buffer_load_dwordx4 %19, %26, s55 offen lds\n\t" \
"v_mfma_f32_32x32x16_bf16 a[160:175], v[68:71], v[88:91], a[160:175]\n\t" \
"ds_read_b64_tr_b16 v[122:123], v145 offset:29696\n\t" \
"v_mfma_f32_32x32x16_bf16 a[48:63], v[84:87], v[68:71], a[48:63]\n\t" \
"v_add_u32 v140, 32768, v128\n\t" \
"ds_read_b64_tr_b16 v[116:117], v146 offset:12288\n\t" \
"v_mfma_f32_32x32x16_bf16 a[176:191], v[68:71], v[92:95], a[176:191]\n\t" \
"v_add_u32 v141, 32768, v129\n\t" \
"s_waitcnt lgkmcnt(7)\n\t" \
"v_mfma_f32_32x32x16_bf16 a[64:79], v[80:83], v[72:75], a[64:79]\n\t" \
"ds_read_b64_tr_b16 v[118:119], v147 offset:13312\n\t" \
"v_add_u32 v142, 32768, v130\n\t" \
"v_mfma_f32_32x32x16_bf16 a[192:207], v[72:75], v[88:91], a[192:207]\n\t" \
"ds_read_b64_tr_b16 v[124:125], v146 offset:28672\n\t" \
"v_mfma_f32_32x32x16_bf16 a[80:95], v[84:87], v[72:75], a[80:95]\n\t" \
"v_add_u32 v143, 32768, v131\n\t" \
"ds_read_b64_tr_b16 v[126:127], v147 offset:29696\n\t" \
"v_mfma_f32_32x32x16_bf16 a[208:223], v[72:75], v[92:95], a[208:223]\n\t" \
"v_add_u32 v148, 65536, v132\n\t" \
"s_waitcnt lgkmcnt(9)\n\t" \
"v_mfma_f32_32x32x16_bf16 a[96:111], v[80:83], v[76:79], a[96:111]\n\t" \
"ds_read_b128 v[100:103], v139 offset:4096\n\t" \
"v_add_u32 v149, 65536, v133\n\t" \
"v_mfma_f32_32x32x16_bf16 a[224:239], v[76:79], v[88:91], a[224:239]\n\t" \
"ds_read_b128 v[104:107], v139 offset:8192\n\t" \
"v_mfma_f32_32x32x16_bf16 a[112:127], v[84:87], v[76:79], a[112:127]\n\t" \
"v_add_u32 v150, 65536, v134\n\t" \
"ds_read_b128 v[108:111], v139 offset:12288\n\t" \
"v_mfma_f32_32x32x16_bf16 a[240:255], v[76:79], v[92:95], a[240:255]\n\t" \
"v_add_u32 v151, 65536, v135\n\t" \
"s_waitcnt lgkmcnt(9)\n\t" \
"v_mfma_f32_32x32x16_bf16 a[0:15], v[112:115], v[96:99], a[0:15]\n\t" \
"s_waitcnt lgkmcnt(7)\n\t" \
"v_mfma_f32_32x32x16_bf16 a[128:143], v[96:99], v[120:123], a[128:143]\n\t" \
"s_waitcnt lgkmcnt(0)\n\t" \
"s_waitcnt vmcnt(8)\n\t" \
"s_barrier\n\t" \
"v_lshrrev_b32 v253, 4, v36\n\t" \
"s_sub_u32 s56, %6, 1\n\t" \
"s_lshl_b32 s56, s56, 2\n\t" \
"s_add_u32 s56, s56, s75\n\t" \
"v_lshrrev_b32 v250, 1, v253\n\t" \
"v_lshlrev_b32 v250, 13, v250\n\t" \
"v_and_b32 v254, 1, v253\n\t" \
"v_lshl_add_u32 v250, v254, 7, v250\n\t" \
"v_cmp_gt_u32 vcc, 32, v253\n\t" \
"v_mov_b32 v254, 0x7fffffff\n\t" \
"s_nop 0\n\t" \
"v_cndmask_b32 v250, v254, v250, vcc\n\t" \
"s_lshl_b32 s57, s56, 17\n\t" \
"buffer_load_dword v250, v250, %14, s57 offen\n\t" \
"v_and_b32 v251, 15, v253\n\t" \
"v_mul_u32_u24 v251, 3072, v251\n\t" \
"v_lshrrev_b32 v252, 4, v253\n\t" \
"v_lshl_add_u32 v251, v252, 10, v251\n\t" \
"v_cmp_gt_u32 vcc, 48, v253\n\t" \
"s_nop 1\n\t" \
"v_cndmask_b32 v251, v254, v251, vcc\n\t" \
"s_mul_i32 s57, s56, 49152\n\t" \
"buffer_load_dword v251, v251, %15, s57 offen\n\t" \
"s_lshr_b32 s57, %1, 19\n\t" \
"s_lshl_b32 s57, s57, 5\n\t" \
"s_add_u32 s57, s57, s56\n\t" \
"s_lshl_b32 s57, s57, 12\n\t" \
"v_lshlrev_b32 v252, 7, v253\n\t" \
"v_cmp_gt_u32 vcc, 32, v253\n\t" \
"s_nop 1\n\t" \
"v_cndmask_b32 v252, v254, v252, vcc\n\t" \
"buffer_load_dword v252, v252, %29, s57 offen\n\t" \
"v_mfma_f32_32x32x16_bf16 a[16:31], v[116:119], v[96:99], a[16:31]\n\t" \
"ds_read_b64_tr_b16 v[80:81], v148 offset:0\n\t" \
"s_add_u32 m0, s70, 131072\n\t" \
"s_mov_b32 s54, 2097152\n\t" \
"buffer_load_dwordx4 %20, %27, s54 offen lds\n\t" \
"v_mfma_f32_32x32x16_bf16 a[144:159], v[96:99], v[124:127], a[144:159]\n\t" \
"ds_read_b64_tr_b16 v[82:83], v149 offset:1024\n\t" \
"v_mfma_f32_32x32x16_bf16 a[32:47], v[112:115], v[100:103], a[32:47]\n\t" \
"s_add_u32 m0, s70, 132096\n\t" \
"s_mov_b32 s55, 2097152\n\t" \
"buffer_load_dwordx4 %21, %27, s55 offen lds\n\t" \
"v_mfma_f32_32x32x16_bf16 a[160:175], v[100:103], v[120:123], a[160:175]\n\t" \
"ds_read_b128 v[64:67], v140 offset:0\n\t" \
"v_mfma_f32_32x32x16_bf16 a[48:63], v[116:119], v[100:103], a[48:63]\n\t" \
"s_add_u32 m0, s70, 139264\n\t" \
"s_mov_b32 s54, 2359296\n\t" \
"buffer_load_dwordx4 %20, %27, s54 offen lds\n\t" \
"v_mfma_f32_32x32x16_bf16 a[176:191], v[100:103], v[124:127], a[176:191]\n\t" \
"ds_read_b64_tr_b16 v[88:89], v148 offset:16384\n\t" \
"v_mfma_f32_32x32x16_bf16 a[64:79], v[112:115], v[104:107], a[64:79]\n\t" \
"s_add_u32 m0, s70, 140288\n\t" \
"s_mov_b32 s55, 2359296\n\t" \
"buffer_load_dwordx4 %21, %27, s55 offen lds\n\t" \
"v_mfma_f32_32x32x16_bf16 a[192:207], v[104:107], v[120:123], a[192:207]\n\t" \
"ds_read_b64_tr_b16 v[90:91], v149 offset:17408\n\t" \
"ds_read_b64_tr_b16 v[84:85], v150 offset:0\n\t" \
"v_mfma_f32_32x32x16_bf16 a[80:95], v[116:119], v[104:107], a[80:95]\n\t" \
"ds_read_b64_tr_b16 v[86:87], v151 offset:1024\n\t" \
"v_mfma_f32_32x32x16_bf16 a[208:223], v[104:107], v[124:127], a[208:223]\n\t" \
"ds_read_b64_tr_b16 v[92:93], v150 offset:16384\n\t" \
"v_mfma_f32_32x32x16_bf16 a[96:111], v[112:115], v[108:111], a[96:111]\n\t" \
"ds_read_b64_tr_b16 v[94:95], v151 offset:17408\n\t" \
"v_mfma_f32_32x32x16_bf16 a[224:239], v[108:111], v[120:123], a[224:239]\n\t" \
"ds_read_b128 v[68:71], v140 offset:4096\n\t" \
"v_mfma_f32_32x32x16_bf16 a[112:127], v[116:119], v[108:111], a[112:127]\n\t" \
"ds_read_b128 v[72:75], v140 offset:8192\n\t" \
"v_mfma_f32_32x32x16_bf16 a[240:255], v[108:111], v[124:127], a[240:255]\n\t" \
"ds_read_b128 v[76:79], v140 offset:12288\n\t" \
"s_waitcnt lgkmcnt(9)\n\t" \
"v_mfma_f32_32x32x16_bf16 a[0:15], v[80:83], v[64:67], a[0:15]\n\t" \
"ds_read_b64_tr_b16 v[112:113], v148 offset:4096\n\t" \
"s_waitcnt lgkmcnt(8)\n\t" \
"v_mfma_f32_32x32x16_bf16 a[128:143], v[64:67], v[88:91], a[128:143]\n\t" \
"s_add_u32 m0, s70, 147456\n\t" \
"s_mov_b32 s54, 2097152\n\t" \
"buffer_load_dwordx4 %20, %28, s54 offen lds\n\t" \
"s_waitcnt lgkmcnt(6)\n\t" \
"v_mfma_f32_32x32x16_bf16 a[16:31], v[84:87], v[64:67], a[16:31]\n\t" \
"ds_read_b64_tr_b16 v[114:115], v149 offset:5120\n\t" \
"s_waitcnt lgkmcnt(5)\n\t" \
"v_mfma_f32_32x32x16_bf16 a[144:159], v[64:67], v[92:95], a[144:159]\n\t" \
"s_add_u32 m0, s70, 148480\n\t" \
"s_mov_b32 s55, 2097152\n\t" \
"buffer_load_dwordx4 %21, %28, s55 offen lds\n\t" \
"s_waitcnt lgkmcnt(4)\n\t" \
"v_mfma_f32_32x32x16_bf16 a[32:47], v[80:83], v[68:71], a[32:47]\n\t" \
"ds_read_b128 v[96:99], v141 offset:0\n\t" \
"v_mfma_f32_32x32x16_bf16 a[160:175], v[68:71], v[88:91], a[160:175]\n\t" \
"s_add_u32 m0, s70, 155648\n\t" \
"s_mov_b32 s54, 2359296\n\t" \
"buffer_load_dwordx4 %20, %28, s54 offen lds\n\t" \
"v_mfma_f32_32x32x16_bf16 a[48:63], v[84:87], v[68:71], a[48:63]\n\t" \
"ds_read_b64_tr_b16 v[120:121], v148 offset:20480\n\t" \
"v_mfma_f32_32x32x16_bf16 a[176:191], v[68:71], v[92:95], a[176:191]\n\t" \
"s_add_u32 m0, s70, 156672\n\t" \
"s_mov_b32 s55, 2359296\n\t" \
"buffer_load_dwordx4 %21, %28, s55 offen lds\n\t" \
"s_waitcnt lgkmcnt(5)\n\t" \
"v_mfma_f32_32x32x16_bf16 a[64:79], v[80:83], v[72:75], a[64:79]\n\t" \
"ds_read_b64_tr_b16 v[122:123], v149 offset:21504\n\t" \
"v_mfma_f32_32x32x16_bf16 a[192:207], v[72:75], v[88:91], a[192:207]\n\t" \
"ds_read_b64_tr_b16 v[116:117], v150 offset:4096\n\t" \
"v_mfma_f32_32x32x16_bf16 a[80:95], v[84:87], v[72:75], a[80:95]\n\t" \
"ds_read_b64_tr_b16 v[118:119], v151 offset:5120\n\t" \
"v_mfma_f32_32x32x16_bf16 a[208:223], v[72:75], v[92:95], a[208:223]\n\t" \
"ds_read_b64_tr_b16 v[124:125], v150 offset:20480\n\t" \
"s_waitcnt lgkmcnt(8)\n\t" \
"v_mfma_f32_32x32x16_bf16 a[96:111], v[80:83], v[76:79], a[96:111]\n\t" \
"ds_read_b64_tr_b16 v[126:127], v151 offset:21504\n\t" \
"v_mfma_f32_32x32x16_bf16 a[224:239], v[76:79], v[88:91], a[224:239]\n\t" \
"ds_read_b128 v[100:103], v141 offset:4096\n\t" \
"v_mfma_f32_32x32x16_bf16 a[112:127], v[84:87], v[76:79], a[112:127]\n\t" \
"ds_read_b128 v[104:107], v141 offset:8192\n\t" \
"v_mfma_f32_32x32x16_bf16 a[240:255], v[76:79], v[92:95], a[240:255]\n\t" \
"ds_read_b128 v[108:111], v141 offset:12288\n\t" \
"s_waitcnt lgkmcnt(9)\n\t" \
"v_mfma_f32_32x32x16_bf16 a[0:15], v[112:115], v[96:99], a[0:15]\n\t" \
"ds_read_b64_tr_b16 v[80:81], v148 offset:8192\n\t" \
"s_waitcnt lgkmcnt(8)\n\t" \
"v_mfma_f32_32x32x16_bf16 a[128:143], v[96:99], v[120:123], a[128:143]\n\t" \
"s_add_u32 m0, s70, 0\n\t" \
"s_add_u32 s54, s80, 640\n\t" \
"buffer_load_dwordx4 %18, %26, s54 offen lds\n\t" \
"s_waitcnt lgkmcnt(6)\n\t" \
"v_mfma_f32_32x32x16_bf16 a[16:31], v[116:119], v[96:99], a[16:31]\n\t" \
"ds_read_b64_tr_b16 v[82:83], v149 offset:9216\n\t" \
"s_waitcnt lgkmcnt(5)\n\t" \
"v_mfma_f32_32x32x16_bf16 a[144:159], v[96:99], v[124:127], a[144:159]\n\t" \
"s_add_u32 m0, s70, 1024\n\t" \
"s_add_u32 s55, s80, 640\n\t" \
"buffer_load_dwordx4 %19, %26, s55 offen lds\n\t" \
"s_waitcnt lgkmcnt(4)\n\t" \
"v_mfma_f32_32x32x16_bf16 a[32:47], v[112:115], v[100:103], a[32:47]\n\t" \
"ds_read_b128 v[64:67], v142 offset:0\n\t" \
"v_mfma_f32_32x32x16_bf16 a[160:175], v[100:103], v[120:123], a[160:175]\n\t" \
"s_add_u32 m0, s70, 8192\n\t" \
"s_add_u32 s54, s80, 66176\n\t" \
"buffer_load_dwordx4 %18, %26, s54 offen lds\n\t" \
"v_mfma_f32_32x32x16_bf16 a[48:63], v[116:119], v[100:103], a[48:63]\n\t" \
"ds_read_b64_tr_b16 v[88:89], v148 offset:24576\n\t" \
"v_mfma_f32_32x32x16_bf16 a[176:191], v[100:103], v[124:127], a[176:191]\n\t" \
"s_add_u32 m0, s70, 9216\n\t" \
"s_add_u32 s55, s80, 66176\n\t" \
"buffer_load_dwordx4 %19, %26, s55 offen lds\n\t" \
"s_waitcnt lgkmcnt(5)\n\t" \
"v_mfma_f32_32x32x16_bf16 a[64:79], v[112:115], v[104:107], a[64:79]\n\t" \
"ds_read_b64_tr_b16 v[90:91], v149 offset:25600\n\t" \
"v_mfma_f32_32x32x16_bf16 a[192:207], v[104:107], v[120:123], a[192:207]\n\t" \
"ds_read_b64_tr_b16 v[84:85], v150 offset:8192\n\t" \
"v_mfma_f32_32x32x16_bf16 a[80:95], v[116:119], v[104:107], a[80:95]\n\t" \
"ds_read_b64_tr_b16 v[86:87], v151 offset:9216\n\t" \
"v_mfma_f32_32x32x16_bf16 a[208:223], v[104:107], v[124:127], a[208:223]\n\t" \
"ds_read_b64_tr_b16 v[92:93], v150 offset:24576\n\t" \
"s_waitcnt lgkmcnt(8)\n\t" \
"v_mfma_f32_32x32x16_bf16 a[96:111], v[112:115], v[108:111], a[96:111]\n\t" \
"ds_read_b64_tr_b16 v[94:95], v151 offset:25600\n\t" \
"v_mfma_f32_32x32x16_bf16 a[224:239], v[108:111], v[120:123], a[224:239]\n\t" \
"ds_read_b128 v[68:71], v142 offset:4096\n\t" \
"v_mfma_f32_32x32x16_bf16 a[112:127], v[116:119], v[108:111], a[112:127]\n\t" \
"ds_read_b128 v[72:75], v142 offset:8192\n\t" \
"v_mfma_f32_32x32x16_bf16 a[240:255], v[108:111], v[124:127], a[240:255]\n\t" \
"ds_read_b128 v[76:79], v142 offset:12288\n\t" \
"s_waitcnt lgkmcnt(9)\n\t" \
"v_mfma_f32_32x32x16_bf16 a[0:15], v[80:83], v[64:67], a[0:15]\n\t" \
"ds_read_b64_tr_b16 v[112:113], v148 offset:12288\n\t" \
"s_add_u32 m0, s70, 16384\n\t" \
"s_add_u32 s54, s80, 131712\n\t" \
"buffer_load_dwordx4 %18, %26, s54 offen lds\n\t" \
"s_waitcnt lgkmcnt(8)\n\t" \
"v_mfma_f32_32x32x16_bf16 a[128:143], v[64:67], v[88:91], a[128:143]\n\t" \
"ds_read_b64_tr_b16 v[114:115], v149 offset:13312\n\t" \
"s_waitcnt lgkmcnt(7)\n\t" \
"v_mfma_f32_32x32x16_bf16 a[16:31], v[84:87], v[64:67], a[16:31]\n\t" \
"s_add_u32 m0, s70, 17408\n\t" \
"s_add_u32 s55, s80, 131712\n\t" \
"buffer_load_dwordx4 %19, %26, s55 offen lds\n\t" \
"ds_read_b128 v[96:99], v143 offset:0\n\t" \
"s_waitcnt lgkmcnt(6)\n\t" \
"v_mfma_f32_32x32x16_bf16 a[144:159], v[64:67], v[92:95], a[144:159]\n\t" \
"s_add_u32 m0, s70, 24576\n\t" \
"s_add_u32 s54, s80, 197248\n\t" \
"buffer_load_dwordx4 %18, %26, s54 offen lds\n\t" \
"s_waitcnt lgkmcnt(5)\n\t" \
"v_mfma_f32_32x32x16_bf16 a[32:47], v[80:83], v[68:71], a[32:47]\n\t" \
"ds_read_b64_tr_b16 v[120:121], v148 offset:28672\n\t" \
"s_add_u32 m0, s70, 25600\n\t" \
"s_add_u32 s55, s80, 197248\n\t" \
"buffer_load_dwordx4 %19, %26, s55 offen lds\n\t" \
"v_mfma_f32_32x32x16_bf16 a[160:175], v[68:71], v[88:91], a[160:175]\n\t" \
"ds_read_b64_tr_b16 v[122:123], v149 offset:29696\n\t" \
"v_mfma_f32_32x32x16_bf16 a[48:63], v[84:87], v[68:71], a[48:63]\n\t" \
"v_add_u32 v136, 98304, v128\n\t" \
"ds_read_b64_tr_b16 v[116:117], v150 offset:12288\n\t" \
"v_mfma_f32_32x32x16_bf16 a[176:191], v[68:71], v[92:95], a[176:191]\n\t" \
"v_add_u32 v137, 98304, v129\n\t" \
"s_waitcnt lgkmcnt(7)\n\t" \
"v_mfma_f32_32x32x16_bf16 a[64:79], v[80:83], v[72:75], a[64:79]\n\t" \
"ds_read_b64_tr_b16 v[118:119], v151 offset:13312\n\t" \
"v_add_u32 v138, 98304, v130\n\t" \
"v_mfma_f32_32x32x16_bf16 a[192:207], v[72:75], v[88:91], a[192:207]\n\t" \
"ds_read_b64_tr_b16 v[124:125], v150 offset:28672\n\t" \
"v_mfma_f32_32x32x16_bf16 a[80:95], v[84:87], v[72:75], a[80:95]\n\t" \
"v_add_u32 v139, 98304, v131\n\t" \
"ds_read_b64_tr_b16 v[126:127], v151 offset:29696\n\t" \
"v_mfma_f32_32x32x16_bf16 a[208:223], v[72:75], v[92:95], a[208:223]\n\t" \
"v_add_u32 v144, 131072, v132\n\t" \
"s_waitcnt lgkmcnt(9)\n\t" \
"v_mfma_f32_32x32x16_bf16 a[96:111], v[80:83], v[76:79], a[96:111]\n\t" \
"ds_read_b128 v[100:103], v143 offset:4096\n\t" \
"v_add_u32 v145, 131072, v133\n\t" \
"v_mfma_f32_32x32x16_bf16 a[224:239], v[76:79], v[88:91], a[224:239]\n\t" \
"ds_read_b128 v[104:107], v143 offset:8192\n\t" \
"v_mfma_f32_32x32x16_bf16 a[112:127], v[84:87], v[76:79], a[112:127]\n\t" \
"v_add_u32 v146, 131072, v134\n\t" \
"ds_read_b128 v[108:111], v143 offset:12288\n\t" \
"v_mfma_f32_32x32x16_bf16 a[240:255], v[76:79], v[92:95], a[240:255]\n\t" \
"v_add_u32 v147, 131072, v135\n\t" \
"s_waitcnt lgkmcnt(9)\n\t" \
"v_mfma_f32_32x32x16_bf16 a[0:15], v[112:115], v[96:99], a[0:15]\n\t" \
"s_waitcnt lgkmcnt(7)\n\t" \
"v_mfma_f32_32x32x16_bf16 a[128:143], v[96:99], v[120:123], a[128:143]\n\t" \
"s_waitcnt lgkmcnt(0)\n\t" \
"s_waitcnt vmcnt(8)\n\t" \
"s_barrier\n\t" \
"v_mfma_f32_32x32x16_bf16 a[16:31], v[116:119], v[96:99], a[16:31]\n\t" \
"ds_read_b64_tr_b16 v[80:81], v144 offset:0\n\t" \
"s_add_u32 m0, s70, 32768\n\t" \
"s_mov_b32 s54, 2621440\n\t" \
"buffer_load_dwordx4 %20, %27, s54 offen lds\n\t" \
"v_mfma_f32_32x32x16_bf16 a[144:159], v[96:99], v[124:127], a[144:159]\n\t" \
"ds_read_b64_tr_b16 v[82:83], v145 offset:1024\n\t" \
"v_mfma_f32_32x32x16_bf16 a[32:47], v[112:115], v[100:103], a[32:47]\n\t" \
"s_add_u32 m0, s70, 33792\n\t" \
"s_mov_b32 s55, 2621440\n\t" \
"buffer_load_dwordx4 %21, %27, s55 offen lds\n\t" \
"v_mfma_f32_32x32x16_bf16 a[160:175], v[100:103], v[120:123], a[160:175]\n\t" \
"ds_read_b128 v[64:67], v136 offset:0\n\t" \
"v_mfma_f32_32x32x16_bf16 a[48:63], v[116:119], v[100:103], a[48:63]\n\t" \
"s_add_u32 m0, s70, 40960\n\t" \
"s_mov_b32 s54, 2883584\n\t" \
"buffer_load_dwordx4 %20, %27, s54 offen lds\n\t" \
"v_mfma_f32_32x32x16_bf16 a[176:191], v[100:103], v[124:127], a[176:191]\n\t" \
"ds_read_b64_tr_b16 v[88:89], v144 offset:16384\n\t" \
"v_mfma_f32_32x32x16_bf16 a[64:79], v[112:115], v[104:107], a[64:79]\n\t" \
"s_add_u32 m0, s70, 41984\n\t" \
"s_mov_b32 s55, 2883584\n\t" \
"buffer_load_dwordx4 %21, %27, s55 offen lds\n\t" \
"v_mfma_f32_32x32x16_bf16 a[192:207], v[104:107], v[120:123], a[192:207]\n\t" \
"ds_read_b64_tr_b16 v[90:91], v145 offset:17408\n\t" \
"ds_read_b64_tr_b16 v[84:85], v146 offset:0\n\t" \
"v_mfma_f32_32x32x16_bf16 a[80:95], v[116:119], v[104:107], a[80:95]\n\t" \
"ds_read_b64_tr_b16 v[86:87], v147 offset:1024\n\t" \
"v_mfma_f32_32x32x16_bf16 a[208:223], v[104:107], v[124:127], a[208:223]\n\t" \
"ds_read_b64_tr_b16 v[92:93], v146 offset:16384\n\t" \
"v_mfma_f32_32x32x16_bf16 a[96:111], v[112:115], v[108:111], a[96:111]\n\t" \
"ds_read_b64_tr_b16 v[94:95], v147 offset:17408\n\t" \
"v_mfma_f32_32x32x16_bf16 a[224:239], v[108:111], v[120:123], a[224:239]\n\t" \
"ds_read_b128 v[68:71], v136 offset:4096\n\t" \
"v_mfma_f32_32x32x16_bf16 a[112:127], v[116:119], v[108:111], a[112:127]\n\t" \
"ds_read_b128 v[72:75], v136 offset:8192\n\t" \
"v_mfma_f32_32x32x16_bf16 a[240:255], v[108:111], v[124:127], a[240:255]\n\t" \
"ds_read_b128 v[76:79], v136 offset:12288\n\t" \
"s_waitcnt lgkmcnt(9)\n\t" \
"v_mfma_f32_32x32x16_bf16 a[0:15], v[80:83], v[64:67], a[0:15]\n\t" \
"ds_read_b64_tr_b16 v[112:113], v144 offset:4096\n\t" \
"s_waitcnt lgkmcnt(8)\n\t" \
"v_mfma_f32_32x32x16_bf16 a[128:143], v[64:67], v[88:91], a[128:143]\n\t" \
"s_add_u32 m0, s70, 49152\n\t" \
"s_mov_b32 s54, 2621440\n\t" \
"buffer_load_dwordx4 %20, %28, s54 offen lds\n\t" \
"s_waitcnt lgkmcnt(6)\n\t" \
"v_mfma_f32_32x32x16_bf16 a[16:31], v[84:87], v[64:67], a[16:31]\n\t" \
"ds_read_b64_tr_b16 v[114:115], v145 offset:5120\n\t" \
"s_waitcnt lgkmcnt(5)\n\t" \
"v_mfma_f32_32x32x16_bf16 a[144:159], v[64:67], v[92:95], a[144:159]\n\t" \
"s_add_u32 m0, s70, 50176\n\t" \
"s_mov_b32 s55, 2621440\n\t" \
"buffer_load_dwordx4 %21, %28, s55 offen lds\n\t" \
"s_waitcnt lgkmcnt(4)\n\t" \
"v_mfma_f32_32x32x16_bf16 a[32:47], v[80:83], v[68:71], a[32:47]\n\t" \
"ds_read_b128 v[96:99], v137 offset:0\n\t" \
"v_mfma_f32_32x32x16_bf16 a[160:175], v[68:71], v[88:91], a[160:175]\n\t" \
"s_add_u32 m0, s70, 57344\n\t" \
"s_mov_b32 s54, 2883584\n\t" \
"buffer_load_dwordx4 %20, %28, s54 offen lds\n\t" \
"v_mfma_f32_32x32x16_bf16 a[48:63], v[84:87], v[68:71], a[48:63]\n\t" \
"ds_read_b64_tr_b16 v[120:121], v144 offset:20480\n\t" \
"v_mfma_f32_32x32x16_bf16 a[176:191], v[68:71], v[92:95], a[176:191]\n\t" \
"s_add_u32 m0, s70, 58368\n\t" \
"s_mov_b32 s55, 2883584\n\t" \
"buffer_load_dwordx4 %21, %28, s55 offen lds\n\t" \
"s_waitcnt lgkmcnt(5)\n\t" \
"v_mfma_f32_32x32x16_bf16 a[64:79], v[80:83], v[72:75], a[64:79]\n\t" \
"ds_read_b64_tr_b16 v[122:123], v145 offset:21504\n\t" \
"v_mfma_f32_32x32x16_bf16 a[192:207], v[72:75], v[88:91], a[192:207]\n\t" \
"ds_read_b64_tr_b16 v[116:117], v146 offset:4096\n\t" \
"v_mfma_f32_32x32x16_bf16 a[80:95], v[84:87], v[72:75], a[80:95]\n\t" \
"ds_read_b64_tr_b16 v[118:119], v147 offset:5120\n\t" \
"v_mfma_f32_32x32x16_bf16 a[208:223], v[72:75], v[92:95], a[208:223]\n\t" \
"ds_read_b64_tr_b16 v[124:125], v146 offset:20480\n\t" \
"s_waitcnt lgkmcnt(8)\n\t" \
"v_mfma_f32_32x32x16_bf16 a[96:111], v[80:83], v[76:79], a[96:111]\n\t" \
"ds_read_b64_tr_b16 v[126:127], v147 offset:21504\n\t" \
"v_mfma_f32_32x32x16_bf16 a[224:239], v[76:79], v[88:91], a[224:239]\n\t" \
"ds_read_b128 v[100:103], v137 offset:4096\n\t" \
"v_mfma_f32_32x32x16_bf16 a[112:127], v[84:87], v[76:79], a[112:127]\n\t" \
"ds_read_b128 v[104:107], v137 offset:8192\n\t" \
"v_mfma_f32_32x32x16_bf16 a[240:255], v[76:79], v[92:95], a[240:255]\n\t" \
"ds_read_b128 v[108:111], v137 offset:12288\n\t" \
"s_waitcnt lgkmcnt(9)\n\t" \
"v_mfma_f32_32x32x16_bf16 a[0:15], v[112:115], v[96:99], a[0:15]\n\t" \
"ds_read_b64_tr_b16 v[80:81], v144 offset:8192\n\t" \
"s_waitcnt lgkmcnt(8)\n\t" \
"v_mfma_f32_32x32x16_bf16 a[128:143], v[96:99], v[120:123], a[128:143]\n\t" \
"s_add_u32 m0, s70, 65536\n\t" \
"s_add_u32 s54, s80, 768\n\t" \
"buffer_load_dwordx4 %18, %26, s54 offen lds\n\t" \
"s_waitcnt lgkmcnt(6)\n\t" \
"v_mfma_f32_32x32x16_bf16 a[16:31], v[116:119], v[96:99], a[16:31]\n\t" \
"ds_read_b64_tr_b16 v[82:83], v145 offset:9216\n\t" \
"s_waitcnt lgkmcnt(5)\n\t" \
"v_mfma_f32_32x32x16_bf16 a[144:159], v[96:99], v[124:127], a[144:159]\n\t" \
"s_add_u32 m0, s70, 66560\n\t" \
"s_add_u32 s55, s80, 768\n\t" \
"buffer_load_dwordx4 %19, %26, s55 offen lds\n\t" \
"s_waitcnt lgkmcnt(4)\n\t" \
"v_mfma_f32_32x32x16_bf16 a[32:47], v[112:115], v[100:103], a[32:47]\n\t" \
"ds_read_b128 v[64:67], v138 offset:0\n\t" \
"v_mfma_f32_32x32x16_bf16 a[160:175], v[100:103], v[120:123], a[160:175]\n\t" \
"s_add_u32 m0, s70, 73728\n\t" \
"s_add_u32 s54, s80, 66304\n\t" \
"buffer_load_dwordx4 %18, %26, s54 offen lds\n\t" \
"v_mfma_f32_32x32x16_bf16 a[48:63], v[116:119], v[100:103], a[48:63]\n\t" \
"ds_read_b64_tr_b16 v[88:89], v144 offset:24576\n\t" \
"v_mfma_f32_32x32x16_bf16 a[176:191], v[100:103], v[124:127], a[176:191]\n\t" \
"s_add_u32 m0, s70, 74752\n\t" \
"s_add_u32 s55, s80, 66304\n\t" \
"buffer_load_dwordx4 %19, %26, s55 offen lds\n\t" \
"s_waitcnt lgkmcnt(5)\n\t" \
"v_mfma_f32_32x32x16_bf16 a[64:79], v[112:115], v[104:107], a[64:79]\n\t" \
"ds_read_b64_tr_b16 v[90:91], v145 offset:25600\n\t" \
"v_mfma_f32_32x32x16_bf16 a[192:207], v[104:107], v[120:123], a[192:207]\n\t" \
"ds_read_b64_tr_b16 v[84:85], v146 offset:8192\n\t" \
"v_mfma_f32_32x32x16_bf16 a[80:95], v[116:119], v[104:107], a[80:95]\n\t" \
"ds_read_b64_tr_b16 v[86:87], v147 offset:9216\n\t" \
"v_mfma_f32_32x32x16_bf16 a[208:223], v[104:107], v[124:127], a[208:223]\n\t" \
"ds_read_b64_tr_b16 v[92:93], v146 offset:24576\n\t" \
"s_waitcnt lgkmcnt(8)\n\t" \
"v_mfma_f32_32x32x16_bf16 a[96:111], v[112:115], v[108:111], a[96:111]\n\t" \
"ds_read_b64_tr_b16 v[94:95], v147 offset:25600\n\t" \
"v_mfma_f32_32x32x16_bf16 a[224:239], v[108:111], v[120:123], a[224:239]\n\t" \
"ds_read_b128 v[68:71], v138 offset:4096\n\t" \
"v_mfma_f32_32x32x16_bf16 a[112:127], v[116:119], v[108:111], a[112:127]\n\t" \
"ds_read_b128 v[72:75], v138 offset:8192\n\t" \
"v_mfma_f32_32x32x16_bf16 a[240:255], v[108:111], v[124:127], a[240:255]\n\t" \
"ds_read_b128 v[76:79], v138 offset:12288\n\t" \
"s_waitcnt lgkmcnt(9)\n\t" \
"v_mfma_f32_32x32x16_bf16 a[0:15], v[80:83], v[64:67], a[0:15]\n\t" \
"ds_read_b64_tr_b16 v[112:113], v144 offset:12288\n\t" \
"s_add_u32 m0, s70, 81920\n\t" \
"s_add_u32 s54, s80, 131840\n\t" \
"buffer_load_dwordx4 %18, %26, s54 offen lds\n\t" \
"s_waitcnt lgkmcnt(8)\n\t" \
"v_mfma_f32_32x32x16_bf16 a[128:143], v[64:67], v[88:91], a[128:143]\n\t" \
"ds_read_b64_tr_b16 v[114:115], v145 offset:13312\n\t" \
"s_waitcnt lgkmcnt(7)\n\t" \
"v_mfma_f32_32x32x16_bf16 a[16:31], v[84:87], v[64:67], a[16:31]\n\t" \
"s_add_u32 m0, s70, 82944\n\t" \
"s_add_u32 s55, s80, 131840\n\t" \
"buffer_load_dwordx4 %19, %26, s55 offen lds\n\t" \
"ds_read_b128 v[96:99], v139 offset:0\n\t" \
"s_waitcnt lgkmcnt(6)\n\t" \
"v_mfma_f32_32x32x16_bf16 a[144:159], v[64:67], v[92:95], a[144:159]\n\t" \
"s_add_u32 m0, s70, 90112\n\t" \
"s_add_u32 s54, s80, 197376\n\t" \
"buffer_load_dwordx4 %18, %26, s54 offen lds\n\t" \
"s_waitcnt lgkmcnt(5)\n\t" \
"v_mfma_f32_32x32x16_bf16 a[32:47], v[80:83], v[68:71], a[32:47]\n\t" \
"ds_read_b64_tr_b16 v[120:121], v144 offset:28672\n\t" \
"s_add_u32 m0, s70, 91136\n\t" \
"s_add_u32 s55, s80, 197376\n\t" \
"buffer_load_dwordx4 %19, %26, s55 offen lds\n\t" \
"v_mfma_f32_32x32x16_bf16 a[160:175], v[68:71], v[88:91], a[160:175]\n\t" \
"ds_read_b64_tr_b16 v[122:123], v145 offset:29696\n\t" \
"v_mfma_f32_32x32x16_bf16 a[48:63], v[84:87], v[68:71], a[48:63]\n\t" \
"v_add_u32 v140, 0, v128\n\t" \
"ds_read_b64_tr_b16 v[116:117], v146 offset:12288\n\t" \
"v_mfma_f32_32x32x16_bf16 a[176:191], v[68:71], v[92:95], a[176:191]\n\t" \
"v_add_u32 v141, 0, v129\n\t" \
"s_waitcnt lgkmcnt(7)\n\t" \
"v_mfma_f32_32x32x16_bf16 a[64:79], v[80:83], v[72:75], a[64:79]\n\t" \
"ds_read_b64_tr_b16 v[118:119], v147 offset:13312\n\t" \
"v_add_u32 v142, 0, v130\n\t" \
"v_mfma_f32_32x32x16_bf16 a[192:207], v[72:75], v[88:91], a[192:207]\n\t" \
"ds_read_b64_tr_b16 v[124:125], v146 offset:28672\n\t" \
"v_mfma_f32_32x32x16_bf16 a[80:95], v[84:87], v[72:75], a[80:95]\n\t" \
"v_add_u32 v143, 0, v131\n\t" \
"ds_read_b64_tr_b16 v[126:127], v147 offset:29696\n\t" \
"v_mfma_f32_32x32x16_bf16 a[208:223], v[72:75], v[92:95], a[208:223]\n\t" \
"v_add_u32 v148, 32768, v132\n\t" \
"s_waitcnt lgkmcnt(9)\n\t" \
"v_mfma_f32_32x32x16_bf16 a[96:111], v[80:83], v[76:79], a[96:111]\n\t" \
"ds_read_b128 v[100:103], v139 offset:4096\n\t" \
"v_add_u32 v149, 32768, v133\n\t" \
"v_mfma_f32_32x32x16_bf16 a[224:239], v[76:79], v[88:91], a[224:239]\n\t" \
"ds_read_b128 v[104:107], v139 offset:8192\n\t" \
"v_mfma_f32_32x32x16_bf16 a[112:127], v[84:87], v[76:79], a[112:127]\n\t" \
"v_add_u32 v150, 32768, v134\n\t" \
"ds_read_b128 v[108:111], v139 offset:12288\n\t" \
"v_mfma_f32_32x32x16_bf16 a[240:255], v[76:79], v[92:95], a[240:255]\n\t" \
"v_add_u32 v151, 32768, v135\n\t" \
"s_waitcnt lgkmcnt(9)\n\t" \
"v_mfma_f32_32x32x16_bf16 a[0:15], v[112:115], v[96:99], a[0:15]\n\t" \
"s_waitcnt lgkmcnt(7)\n\t" \
"v_mfma_f32_32x32x16_bf16 a[128:143], v[96:99], v[120:123], a[128:143]\n\t" \
"s_waitcnt lgkmcnt(0)\n\t" \
"s_waitcnt vmcnt(8)\n\t" \
"s_barrier\n\t" \
"v_mfma_f32_32x32x16_bf16 a[16:31], v[116:119], v[96:99], a[16:31]\n\t" \
"ds_read_b64_tr_b16 v[80:81], v148 offset:0\n\t" \
"s_add_u32 m0, s70, 98304\n\t" \
"s_mov_b32 s54, 3145728\n\t" \
"buffer_load_dwordx4 %20, %27, s54 offen lds\n\t" \
"v_mfma_f32_32x32x16_bf16 a[144:159], v[96:99], v[124:127], a[144:159]\n\t" \
"ds_read_b64_tr_b16 v[82:83], v149 offset:1024\n\t" \
"v_mfma_f32_32x32x16_bf16 a[32:47], v[112:115], v[100:103], a[32:47]\n\t" \
"s_add_u32 m0, s70, 99328\n\t" \
"s_mov_b32 s55, 3145728\n\t" \
"buffer_load_dwordx4 %21, %27, s55 offen lds\n\t" \
"v_mfma_f32_32x32x16_bf16 a[160:175], v[100:103], v[120:123], a[160:175]\n\t" \
"ds_read_b128 v[64:67], v140 offset:0\n\t" \
"v_mfma_f32_32x32x16_bf16 a[48:63], v[116:119], v[100:103], a[48:63]\n\t" \
"s_add_u32 m0, s70, 106496\n\t" \
"s_mov_b32 s54, 3407872\n\t" \
"buffer_load_dwordx4 %20, %27, s54 offen lds\n\t" \
"v_mfma_f32_32x32x16_bf16 a[176:191], v[100:103], v[124:127], a[176:191]\n\t" \
"ds_read_b64_tr_b16 v[88:89], v148 offset:16384\n\t" \
"v_mfma_f32_32x32x16_bf16 a[64:79], v[112:115], v[104:107], a[64:79]\n\t" \
"s_add_u32 m0, s70, 107520\n\t" \
"s_mov_b32 s55, 3407872\n\t" \
"buffer_load_dwordx4 %21, %27, s55 offen lds\n\t" \
"v_mfma_f32_32x32x16_bf16 a[192:207], v[104:107], v[120:123], a[192:207]\n\t" \
"ds_read_b64_tr_b16 v[90:91], v149 offset:17408\n\t" \
"ds_read_b64_tr_b16 v[84:85], v150 offset:0\n\t" \
"v_mfma_f32_32x32x16_bf16 a[80:95], v[116:119], v[104:107], a[80:95]\n\t" \
"ds_read_b64_tr_b16 v[86:87], v151 offset:1024\n\t" \
"v_mfma_f32_32x32x16_bf16 a[208:223], v[104:107], v[124:127], a[208:223]\n\t" \
"ds_read_b64_tr_b16 v[92:93], v150 offset:16384\n\t" \
"v_mfma_f32_32x32x16_bf16 a[96:111], v[112:115], v[108:111], a[96:111]\n\t" \
"ds_read_b64_tr_b16 v[94:95], v151 offset:17408\n\t" \
"v_mfma_f32_32x32x16_bf16 a[224:239], v[108:111], v[120:123], a[224:239]\n\t" \
"ds_read_b128 v[68:71], v140 offset:4096\n\t" \
"v_mfma_f32_32x32x16_bf16 a[112:127], v[116:119], v[108:111], a[112:127]\n\t" \
"ds_read_b128 v[72:75], v140 offset:8192\n\t" \
"v_mfma_f32_32x32x16_bf16 a[240:255], v[108:111], v[124:127], a[240:255]\n\t" \
"ds_read_b128 v[76:79], v140 offset:12288\n\t" \
"s_waitcnt lgkmcnt(9)\n\t" \
"v_mfma_f32_32x32x16_bf16 a[0:15], v[80:83], v[64:67], a[0:15]\n\t" \
"ds_read_b64_tr_b16 v[112:113], v148 offset:4096\n\t" \
"s_waitcnt lgkmcnt(8)\n\t" \
"v_mfma_f32_32x32x16_bf16 a[128:143], v[64:67], v[88:91], a[128:143]\n\t" \
"s_add_u32 m0, s70, 114688\n\t" \
"s_mov_b32 s54, 3145728\n\t" \
"buffer_load_dwordx4 %20, %28, s54 offen lds\n\t" \
"s_waitcnt lgkmcnt(6)\n\t" \
"v_mfma_f32_32x32x16_bf16 a[16:31], v[84:87], v[64:67], a[16:31]\n\t" \
"ds_read_b64_tr_b16 v[114:115], v149 offset:5120\n\t" \
"s_waitcnt lgkmcnt(5)\n\t" \
"v_mfma_f32_32x32x16_bf16 a[144:159], v[64:67], v[92:95], a[144:159]\n\t" \
"s_add_u32 m0, s70, 115712\n\t" \
"s_mov_b32 s55, 3145728\n\t" \
"buffer_load_dwordx4 %21, %28, s55 offen lds\n\t" \
"s_waitcnt lgkmcnt(4)\n\t" \
"v_mfma_f32_32x32x16_bf16 a[32:47], v[80:83], v[68:71], a[32:47]\n\t" \
"ds_read_b128 v[96:99], v141 offset:0\n\t" \
"v_mfma_f32_32x32x16_bf16 a[160:175], v[68:71], v[88:91], a[160:175]\n\t" \
"s_add_u32 m0, s70, 122880\n\t" \
"s_mov_b32 s54, 3407872\n\t" \
"buffer_load_dwordx4 %20, %28, s54 offen lds\n\t" \
"v_mfma_f32_32x32x16_bf16 a[48:63], v[84:87], v[68:71], a[48:63]\n\t" \
"ds_read_b64_tr_b16 v[120:121], v148 offset:20480\n\t" \
"v_mfma_f32_32x32x16_bf16 a[176:191], v[68:71], v[92:95], a[176:191]\n\t" \
"s_add_u32 m0, s70, 123904\n\t" \
"s_mov_b32 s55, 3407872\n\t" \
"buffer_load_dwordx4 %21, %28, s55 offen lds\n\t" \
"s_waitcnt lgkmcnt(5)\n\t" \
"v_mfma_f32_32x32x16_bf16 a[64:79], v[80:83], v[72:75], a[64:79]\n\t" \
"ds_read_b64_tr_b16 v[122:123], v149 offset:21504\n\t" \
"v_mfma_f32_32x32x16_bf16 a[192:207], v[72:75], v[88:91], a[192:207]\n\t" \
"ds_read_b64_tr_b16 v[116:117], v150 offset:4096\n\t" \
"v_mfma_f32_32x32x16_bf16 a[80:95], v[84:87], v[72:75], a[80:95]\n\t" \
"ds_read_b64_tr_b16 v[118:119], v151 offset:5120\n\t" \
"v_mfma_f32_32x32x16_bf16 a[208:223], v[72:75], v[92:95], a[208:223]\n\t" \
"ds_read_b64_tr_b16 v[124:125], v150 offset:20480\n\t" \
"s_waitcnt lgkmcnt(8)\n\t" \
"v_mfma_f32_32x32x16_bf16 a[96:111], v[80:83], v[76:79], a[96:111]\n\t" \
"ds_read_b64_tr_b16 v[126:127], v151 offset:21504\n\t" \
"v_mfma_f32_32x32x16_bf16 a[224:239], v[76:79], v[88:91], a[224:239]\n\t" \
"ds_read_b128 v[100:103], v141 offset:4096\n\t" \
"v_mfma_f32_32x32x16_bf16 a[112:127], v[84:87], v[76:79], a[112:127]\n\t" \
"ds_read_b128 v[104:107], v141 offset:8192\n\t" \
"v_mfma_f32_32x32x16_bf16 a[240:255], v[76:79], v[92:95], a[240:255]\n\t" \
"ds_read_b128 v[108:111], v141 offset:12288\n\t" \
"s_waitcnt lgkmcnt(9)\n\t" \
"v_mfma_f32_32x32x16_bf16 a[0:15], v[112:115], v[96:99], a[0:15]\n\t" \
"ds_read_b64_tr_b16 v[80:81], v148 offset:8192\n\t" \
"s_waitcnt lgkmcnt(8)\n\t" \
"v_mfma_f32_32x32x16_bf16 a[128:143], v[96:99], v[120:123], a[128:143]\n\t" \
"s_add_u32 m0, s70, 131072\n\t" \
"s_add_u32 s54, s80, 896\n\t" \
"buffer_load_dwordx4 %18, %26, s54 offen lds\n\t" \
"s_waitcnt lgkmcnt(6)\n\t" \
"v_mfma_f32_32x32x16_bf16 a[16:31], v[116:119], v[96:99], a[16:31]\n\t" \
"ds_read_b64_tr_b16 v[82:83], v149 offset:9216\n\t" \
"s_waitcnt lgkmcnt(5)\n\t" \
"v_mfma_f32_32x32x16_bf16 a[144:159], v[96:99], v[124:127], a[144:159]\n\t" \
"s_add_u32 m0, s70, 132096\n\t" \
"s_add_u32 s55, s80, 896\n\t" \
"buffer_load_dwordx4 %19, %26, s55 offen lds\n\t" \
"s_waitcnt lgkmcnt(4)\n\t" \
"v_mfma_f32_32x32x16_bf16 a[32:47], v[112:115], v[100:103], a[32:47]\n\t" \
"ds_read_b128 v[64:67], v142 offset:0\n\t" \
"v_mfma_f32_32x32x16_bf16 a[160:175], v[100:103], v[120:123], a[160:175]\n\t" \
"s_add_u32 m0, s70, 139264\n\t" \
"s_add_u32 s54, s80, 66432\n\t" \
"buffer_load_dwordx4 %18, %26, s54 offen lds\n\t" \
"v_mfma_f32_32x32x16_bf16 a[48:63], v[116:119], v[100:103], a[48:63]\n\t" \
"ds_read_b64_tr_b16 v[88:89], v148 offset:24576\n\t" \
"v_mfma_f32_32x32x16_bf16 a[176:191], v[100:103], v[124:127], a[176:191]\n\t" \
"s_add_u32 m0, s70, 140288\n\t" \
"s_add_u32 s55, s80, 66432\n\t" \
"buffer_load_dwordx4 %19, %26, s55 offen lds\n\t" \
"s_waitcnt lgkmcnt(5)\n\t" \
"v_mfma_f32_32x32x16_bf16 a[64:79], v[112:115], v[104:107], a[64:79]\n\t" \
"ds_read_b64_tr_b16 v[90:91], v149 offset:25600\n\t" \
"v_mfma_f32_32x32x16_bf16 a[192:207], v[104:107], v[120:123], a[192:207]\n\t" \
"ds_read_b64_tr_b16 v[84:85], v150 offset:8192\n\t" \
"v_mfma_f32_32x32x16_bf16 a[80:95], v[116:119], v[104:107], a[80:95]\n\t" \
"ds_read_b64_tr_b16 v[86:87], v151 offset:9216\n\t" \
"v_mfma_f32_32x32x16_bf16 a[208:223], v[104:107], v[124:127], a[208:223]\n\t" \
"ds_read_b64_tr_b16 v[92:93], v150 offset:24576\n\t" \
"s_waitcnt lgkmcnt(8)\n\t" \
"v_mfma_f32_32x32x16_bf16 a[96:111], v[112:115], v[108:111], a[96:111]\n\t" \
"ds_read_b64_tr_b16 v[94:95], v151 offset:25600\n\t" \
"v_mfma_f32_32x32x16_bf16 a[224:239], v[108:111], v[120:123], a[224:239]\n\t" \
"ds_read_b128 v[68:71], v142 offset:4096\n\t" \
"v_mfma_f32_32x32x16_bf16 a[112:127], v[116:119], v[108:111], a[112:127]\n\t" \
"ds_read_b128 v[72:75], v142 offset:8192\n\t" \
"v_mfma_f32_32x32x16_bf16 a[240:255], v[108:111], v[124:127], a[240:255]\n\t" \
"ds_read_b128 v[76:79], v142 offset:12288\n\t" \
"s_waitcnt lgkmcnt(9)\n\t" \
"v_mfma_f32_32x32x16_bf16 a[0:15], v[80:83], v[64:67], a[0:15]\n\t" \
"ds_read_b64_tr_b16 v[112:113], v148 offset:12288\n\t" \
"s_add_u32 m0, s70, 147456\n\t" \
"s_add_u32 s54, s80, 131968\n\t" \
"buffer_load_dwordx4 %18, %26, s54 offen lds\n\t" \
"s_waitcnt lgkmcnt(8)\n\t" \
"v_mfma_f32_32x32x16_bf16 a[128:143], v[64:67], v[88:91], a[128:143]\n\t" \
"ds_read_b64_tr_b16 v[114:115], v149 offset:13312\n\t" \
"s_waitcnt lgkmcnt(7)\n\t" \
"v_mfma_f32_32x32x16_bf16 a[16:31], v[84:87], v[64:67], a[16:31]\n\t" \
"s_add_u32 m0, s70, 148480\n\t" \
"s_add_u32 s55, s80, 131968\n\t" \
"buffer_load_dwordx4 %19, %26, s55 offen lds\n\t" \
"ds_read_b128 v[96:99], v143 offset:0\n\t" \
"s_waitcnt lgkmcnt(6)\n\t" \
"v_mfma_f32_32x32x16_bf16 a[144:159], v[64:67], v[92:95], a[144:159]\n\t" \
"s_add_u32 m0, s70, 155648\n\t" \
"s_add_u32 s54, s80, 197504\n\t" \
"buffer_load_dwordx4 %18, %26, s54 offen lds\n\t" \
"s_waitcnt lgkmcnt(5)\n\t" \
"v_mfma_f32_32x32x16_bf16 a[32:47], v[80:83], v[68:71], a[32:47]\n\t" \
"ds_read_b64_tr_b16 v[120:121], v148 offset:28672\n\t" \
"s_add_u32 m0, s70, 156672\n\t" \
"s_add_u32 s55, s80, 197504\n\t" \
"buffer_load_dwordx4 %19, %26, s55 offen lds\n\t" \
"v_mfma_f32_32x32x16_bf16 a[160:175], v[68:71], v[88:91], a[160:175]\n\t" \
"ds_read_b64_tr_b16 v[122:123], v149 offset:29696\n\t" \
"v_mfma_f32_32x32x16_bf16 a[48:63], v[84:87], v[68:71], a[48:63]\n\t" \
"v_add_u32 v136, 65536, v128\n\t" \
"ds_read_b64_tr_b16 v[116:117], v150 offset:12288\n\t" \
"v_mfma_f32_32x32x16_bf16 a[176:191], v[68:71], v[92:95], a[176:191]\n\t" \
"v_add_u32 v137, 65536, v129\n\t" \
"s_waitcnt lgkmcnt(7)\n\t" \
"v_mfma_f32_32x32x16_bf16 a[64:79], v[80:83], v[72:75], a[64:79]\n\t" \
"ds_read_b64_tr_b16 v[118:119], v151 offset:13312\n\t" \
"v_add_u32 v138, 65536, v130\n\t" \
"v_mfma_f32_32x32x16_bf16 a[192:207], v[72:75], v[88:91], a[192:207]\n\t" \
"ds_read_b64_tr_b16 v[124:125], v150 offset:28672\n\t" \
"v_mfma_f32_32x32x16_bf16 a[80:95], v[84:87], v[72:75], a[80:95]\n\t" \
"v_add_u32 v139, 65536, v131\n\t" \
"ds_read_b64_tr_b16 v[126:127], v151 offset:29696\n\t" \
"v_mfma_f32_32x32x16_bf16 a[208:223], v[72:75], v[92:95], a[208:223]\n\t" \
"v_add_u32 v144, 98304, v132\n\t" \
"s_waitcnt lgkmcnt(9)\n\t" \
"v_mfma_f32_32x32x16_bf16 a[96:111], v[80:83], v[76:79], a[96:111]\n\t" \
"ds_read_b128 v[100:103], v143 offset:4096\n\t" \
"v_add_u32 v145, 98304, v133\n\t" \
"v_mfma_f32_32x32x16_bf16 a[224:239], v[76:79], v[88:91], a[224:239]\n\t" \
"ds_read_b128 v[104:107], v143 offset:8192\n\t" \
"v_mfma_f32_32x32x16_bf16 a[112:127], v[84:87], v[76:79], a[112:127]\n\t" \
"v_add_u32 v146, 98304, v134\n\t" \
"ds_read_b128 v[108:111], v143 offset:12288\n\t" \
"v_mfma_f32_32x32x16_bf16 a[240:255], v[76:79], v[92:95], a[240:255]\n\t" \
"v_add_u32 v147, 98304, v135\n\t" \
"s_waitcnt lgkmcnt(9)\n\t" \
"v_mfma_f32_32x32x16_bf16 a[0:15], v[112:115], v[96:99], a[0:15]\n\t" \
"s_waitcnt lgkmcnt(7)\n\t" \
"v_mfma_f32_32x32x16_bf16 a[128:143], v[96:99], v[120:123], a[128:143]\n\t" \
"s_waitcnt lgkmcnt(0)\n\t" \
"s_waitcnt vmcnt(8)\n\t" \
"s_barrier\n\t" \
"v_mfma_f32_32x32x16_bf16 a[16:31], v[116:119], v[96:99], a[16:31]\n\t" \
"ds_read_b64_tr_b16 v[80:81], v144 offset:0\n\t" \
"s_add_u32 m0, s70, 0\n\t" \
"s_mov_b32 s54, 3670016\n\t" \
"buffer_load_dwordx4 %20, %27, s54 offen lds\n\t" \
"v_mfma_f32_32x32x16_bf16 a[144:159], v[96:99], v[124:127], a[144:159]\n\t" \
"ds_read_b64_tr_b16 v[82:83], v145 offset:1024\n\t" \
"v_mfma_f32_32x32x16_bf16 a[32:47], v[112:115], v[100:103], a[32:47]\n\t" \
"s_add_u32 m0, s70, 1024\n\t" \
"s_mov_b32 s55, 3670016\n\t" \
"buffer_load_dwordx4 %21, %27, s55 offen lds\n\t" \
"v_mfma_f32_32x32x16_bf16 a[160:175], v[100:103], v[120:123], a[160:175]\n\t" \
"ds_read_b128 v[64:67], v136 offset:0\n\t" \
"v_mfma_f32_32x32x16_bf16 a[48:63], v[116:119], v[100:103], a[48:63]\n\t" \
"s_add_u32 m0, s70, 8192\n\t" \
"s_mov_b32 s54, 3932160\n\t" \
"buffer_load_dwordx4 %20, %27, s54 offen lds\n\t" \
"v_mfma_f32_32x32x16_bf16 a[176:191], v[100:103], v[124:127], a[176:191]\n\t" \
"ds_read_b64_tr_b16 v[88:89], v144 offset:16384\n\t" \
"v_mfma_f32_32x32x16_bf16 a[64:79], v[112:115], v[104:107], a[64:79]\n\t" \
"s_add_u32 m0, s70, 9216\n\t" \
"s_mov_b32 s55, 3932160\n\t" \
"buffer_load_dwordx4 %21, %27, s55 offen lds\n\t" \
"v_mfma_f32_32x32x16_bf16 a[192:207], v[104:107], v[120:123], a[192:207]\n\t" \
"ds_read_b64_tr_b16 v[90:91], v145 offset:17408\n\t" \
"ds_read_b64_tr_b16 v[84:85], v146 offset:0\n\t" \
"v_mfma_f32_32x32x16_bf16 a[80:95], v[116:119], v[104:107], a[80:95]\n\t" \
"ds_read_b64_tr_b16 v[86:87], v147 offset:1024\n\t" \
"v_mfma_f32_32x32x16_bf16 a[208:223], v[104:107], v[124:127], a[208:223]\n\t" \
"ds_read_b64_tr_b16 v[92:93], v146 offset:16384\n\t" \
"v_mfma_f32_32x32x16_bf16 a[96:111], v[112:115], v[108:111], a[96:111]\n\t" \
"ds_read_b64_tr_b16 v[94:95], v147 offset:17408\n\t" \
"v_mfma_f32_32x32x16_bf16 a[224:239], v[108:111], v[120:123], a[224:239]\n\t" \
"ds_read_b128 v[68:71], v136 offset:4096\n\t" \
"v_mfma_f32_32x32x16_bf16 a[112:127], v[116:119], v[108:111], a[112:127]\n\t" \
"ds_read_b128 v[72:75], v136 offset:8192\n\t" \
"v_mfma_f32_32x32x16_bf16 a[240:255], v[108:111], v[124:127], a[240:255]\n\t" \
"ds_read_b128 v[76:79], v136 offset:12288\n\t" \
"s_waitcnt lgkmcnt(9)\n\t" \
"v_mfma_f32_32x32x16_bf16 a[0:15], v[80:83], v[64:67], a[0:15]\n\t" \
"ds_read_b64_tr_b16 v[112:113], v144 offset:4096\n\t" \
"s_waitcnt lgkmcnt(8)\n\t" \
"v_mfma_f32_32x32x16_bf16 a[128:143], v[64:67], v[88:91], a[128:143]\n\t" \
"s_add_u32 m0, s70, 16384\n\t" \
"s_mov_b32 s54, 3670016\n\t" \
"buffer_load_dwordx4 %20, %28, s54 offen lds\n\t" \
"s_waitcnt lgkmcnt(6)\n\t" \
"v_mfma_f32_32x32x16_bf16 a[16:31], v[84:87], v[64:67], a[16:31]\n\t" \
"ds_read_b64_tr_b16 v[114:115], v145 offset:5120\n\t" \
"s_waitcnt lgkmcnt(5)\n\t" \
"v_mfma_f32_32x32x16_bf16 a[144:159], v[64:67], v[92:95], a[144:159]\n\t" \
"s_add_u32 m0, s70, 17408\n\t" \
"s_mov_b32 s55, 3670016\n\t" \
"buffer_load_dwordx4 %21, %28, s55 offen lds\n\t" \
"s_waitcnt lgkmcnt(4)\n\t" \
"v_mfma_f32_32x32x16_bf16 a[32:47], v[80:83], v[68:71], a[32:47]\n\t" \
"ds_read_b128 v[96:99], v137 offset:0\n\t" \
"v_mfma_f32_32x32x16_bf16 a[160:175], v[68:71], v[88:91], a[160:175]\n\t" \
"s_add_u32 m0, s70, 24576\n\t" \
"s_mov_b32 s54, 3932160\n\t" \
"buffer_load_dwordx4 %20, %28, s54 offen lds\n\t" \
"v_mfma_f32_32x32x16_bf16 a[48:63], v[84:87], v[68:71], a[48:63]\n\t" \
"ds_read_b64_tr_b16 v[120:121], v144 offset:20480\n\t" \
"v_mfma_f32_32x32x16_bf16 a[176:191], v[68:71], v[92:95], a[176:191]\n\t" \
"s_add_u32 m0, s70, 25600\n\t" \
"s_mov_b32 s55, 3932160\n\t" \
"buffer_load_dwordx4 %21, %28, s55 offen lds\n\t" \
"s_waitcnt lgkmcnt(5)\n\t" \
"v_mfma_f32_32x32x16_bf16 a[64:79], v[80:83], v[72:75], a[64:79]\n\t" \
"ds_read_b64_tr_b16 v[122:123], v145 offset:21504\n\t" \
"v_mfma_f32_32x32x16_bf16 a[192:207], v[72:75], v[88:91], a[192:207]\n\t" \
"ds_read_b64_tr_b16 v[116:117], v146 offset:4096\n\t" \
"v_mfma_f32_32x32x16_bf16 a[80:95], v[84:87], v[72:75], a[80:95]\n\t" \
"ds_read_b64_tr_b16 v[118:119], v147 offset:5120\n\t" \
"v_mfma_f32_32x32x16_bf16 a[208:223], v[72:75], v[92:95], a[208:223]\n\t" \
"ds_read_b64_tr_b16 v[124:125], v146 offset:20480\n\t" \
"s_waitcnt lgkmcnt(8)\n\t" \
"v_mfma_f32_32x32x16_bf16 a[96:111], v[80:83], v[76:79], a[96:111]\n\t" \
"ds_read_b64_tr_b16 v[126:127], v147 offset:21504\n\t" \
"v_mfma_f32_32x32x16_bf16 a[224:239], v[76:79], v[88:91], a[224:239]\n\t" \
"ds_read_b128 v[100:103], v137 offset:4096\n\t" \
"v_mfma_f32_32x32x16_bf16 a[112:127], v[84:87], v[76:79], a[112:127]\n\t" \
"ds_read_b128 v[104:107], v137 offset:8192\n\t" \
"v_mfma_f32_32x32x16_bf16 a[240:255], v[76:79], v[92:95], a[240:255]\n\t" \
"ds_read_b128 v[108:111], v137 offset:12288\n\t" \
"s_waitcnt lgkmcnt(9)\n\t" \
"v_mfma_f32_32x32x16_bf16 a[0:15], v[112:115], v[96:99], a[0:15]\n\t" \
"ds_read_b64_tr_b16 v[80:81], v144 offset:8192\n\t" \
"s_waitcnt lgkmcnt(8)\n\t" \
"v_mfma_f32_32x32x16_bf16 a[128:143], v[96:99], v[120:123], a[128:143]\n\t" \
"s_add_u32 m0, s70, 32768\n\t" \
"s_add_u32 s54, s71, 0\n\t" \
"buffer_load_dwordx4 %18, %13, s54 offen lds\n\t" \
"s_waitcnt lgkmcnt(6)\n\t" \
"v_mfma_f32_32x32x16_bf16 a[16:31], v[116:119], v[96:99], a[16:31]\n\t" \
"ds_read_b64_tr_b16 v[82:83], v145 offset:9216\n\t" \
"s_waitcnt lgkmcnt(5)\n\t" \
"v_mfma_f32_32x32x16_bf16 a[144:159], v[96:99], v[124:127], a[144:159]\n\t" \
"s_add_u32 m0, s70, 33792\n\t" \
"s_add_u32 s55, s71, 0\n\t" \
"buffer_load_dwordx4 %19, %13, s55 offen lds\n\t" \
"s_waitcnt lgkmcnt(4)\n\t" \
"v_mfma_f32_32x32x16_bf16 a[32:47], v[112:115], v[100:103], a[32:47]\n\t" \
"ds_read_b128 v[64:67], v138 offset:0\n\t" \
"v_mfma_f32_32x32x16_bf16 a[160:175], v[100:103], v[120:123], a[160:175]\n\t" \
"s_add_u32 m0, s70, 40960\n\t" \
"s_add_u32 s54, s72, 0\n\t" \
"buffer_load_dwordx4 %18, %13, s54 offen lds\n\t" \
"v_mfma_f32_32x32x16_bf16 a[48:63], v[116:119], v[100:103], a[48:63]\n\t" \
"ds_read_b64_tr_b16 v[88:89], v144 offset:24576\n\t" \
"v_mfma_f32_32x32x16_bf16 a[176:191], v[100:103], v[124:127], a[176:191]\n\t" \
"s_add_u32 m0, s70, 41984\n\t" \
"s_add_u32 s55, s72, 0\n\t" \
"buffer_load_dwordx4 %19, %13, s55 offen lds\n\t" \
"s_waitcnt lgkmcnt(5)\n\t" \
"v_mfma_f32_32x32x16_bf16 a[64:79], v[112:115], v[104:107], a[64:79]\n\t" \
"ds_read_b64_tr_b16 v[90:91], v145 offset:25600\n\t" \
"v_mfma_f32_32x32x16_bf16 a[192:207], v[104:107], v[120:123], a[192:207]\n\t" \
"ds_read_b64_tr_b16 v[84:85], v146 offset:8192\n\t" \
"v_mfma_f32_32x32x16_bf16 a[80:95], v[116:119], v[104:107], a[80:95]\n\t" \
"ds_read_b64_tr_b16 v[86:87], v147 offset:9216\n\t" \
"v_mfma_f32_32x32x16_bf16 a[208:223], v[104:107], v[124:127], a[208:223]\n\t" \
"ds_read_b64_tr_b16 v[92:93], v146 offset:24576\n\t" \
"s_waitcnt lgkmcnt(8)\n\t" \
"v_mfma_f32_32x32x16_bf16 a[96:111], v[112:115], v[108:111], a[96:111]\n\t" \
"ds_read_b64_tr_b16 v[94:95], v147 offset:25600\n\t" \
"v_mfma_f32_32x32x16_bf16 a[224:239], v[108:111], v[120:123], a[224:239]\n\t" \
"ds_read_b128 v[68:71], v138 offset:4096\n\t" \
"v_mfma_f32_32x32x16_bf16 a[112:127], v[116:119], v[108:111], a[112:127]\n\t" \
"ds_read_b128 v[72:75], v138 offset:8192\n\t" \
"v_mfma_f32_32x32x16_bf16 a[240:255], v[108:111], v[124:127], a[240:255]\n\t" \
"ds_read_b128 v[76:79], v138 offset:12288\n\t" \
"s_waitcnt lgkmcnt(9)\n\t" \
"v_mfma_f32_32x32x16_bf16 a[0:15], v[80:83], v[64:67], a[0:15]\n\t" \
"ds_read_b64_tr_b16 v[112:113], v144 offset:12288\n\t" \
"s_add_u32 m0, s70, 49152\n\t" \
"s_add_u32 s54, s73, 0\n\t" \
"buffer_load_dwordx4 %18, %13, s54 offen lds\n\t" \
"s_waitcnt lgkmcnt(8)\n\t" \
"v_mfma_f32_32x32x16_bf16 a[128:143], v[64:67], v[88:91], a[128:143]\n\t" \
"ds_read_b64_tr_b16 v[114:115], v145 offset:13312\n\t" \
"s_waitcnt lgkmcnt(7)\n\t" \
"v_mfma_f32_32x32x16_bf16 a[16:31], v[84:87], v[64:67], a[16:31]\n\t" \
"s_add_u32 m0, s70, 50176\n\t" \
"s_add_u32 s55, s73, 0\n\t" \
"buffer_load_dwordx4 %19, %13, s55 offen lds\n\t" \
"ds_read_b128 v[96:99], v139 offset:0\n\t" \
"s_waitcnt lgkmcnt(6)\n\t" \
"v_mfma_f32_32x32x16_bf16 a[144:159], v[64:67], v[92:95], a[144:159]\n\t" \
"s_add_u32 m0, s70, 57344\n\t" \
"s_add_u32 s54, s74, 0\n\t" \
"buffer_load_dwordx4 %18, %13, s54 offen lds\n\t" \
"s_waitcnt lgkmcnt(5)\n\t" \
"v_mfma_f32_32x32x16_bf16 a[32:47], v[80:83], v[68:71], a[32:47]\n\t" \
"ds_read_b64_tr_b16 v[120:121], v144 offset:28672\n\t" \
"s_add_u32 m0, s70, 58368\n\t" \
"s_add_u32 s55, s74, 0\n\t" \
"buffer_load_dwordx4 %19, %13, s55 offen lds\n\t" \
"v_mfma_f32_32x32x16_bf16 a[160:175], v[68:71], v[88:91], a[160:175]\n\t" \
"ds_read_b64_tr_b16 v[122:123], v145 offset:29696\n\t" \
"v_mfma_f32_32x32x16_bf16 a[48:63], v[84:87], v[68:71], a[48:63]\n\t" \
"v_add_u32 v140, 131072, v128\n\t" \
"ds_read_b64_tr_b16 v[116:117], v146 offset:12288\n\t" \
"v_mfma_f32_32x32x16_bf16 a[176:191], v[68:71], v[92:95], a[176:191]\n\t" \
"v_add_u32 v141, 131072, v129\n\t" \
"s_waitcnt lgkmcnt(7)\n\t" \
"v_mfma_f32_32x32x16_bf16 a[64:79], v[80:83], v[72:75], a[64:79]\n\t" \
"ds_read_b64_tr_b16 v[118:119], v147 offset:13312\n\t" \
"v_add_u32 v142, 131072, v130\n\t" \
"v_mfma_f32_32x32x16_bf16 a[192:207], v[72:75], v[88:91], a[192:207]\n\t" \
"ds_read_b64_tr_b16 v[124:125], v146 offset:28672\n\t" \
"v_mfma_f32_32x32x16_bf16 a[80:95], v[84:87], v[72:75], a[80:95]\n\t" \
"v_add_u32 v143, 131072, v131\n\t" \
"ds_read_b64_tr_b16 v[126:127], v147 offset:29696\n\t" \
"v_mfma_f32_32x32x16_bf16 a[208:223], v[72:75], v[92:95], a[208:223]\n\t" \
"v_add_u32 v148, 0, v132\n\t" \
"s_waitcnt lgkmcnt(9)\n\t" \
"v_mfma_f32_32x32x16_bf16 a[96:111], v[80:83], v[76:79], a[96:111]\n\t" \
"ds_read_b128 v[100:103], v139 offset:4096\n\t" \
"v_add_u32 v149, 0, v133\n\t" \
"v_mfma_f32_32x32x16_bf16 a[224:239], v[76:79], v[88:91], a[224:239]\n\t" \
"ds_read_b128 v[104:107], v139 offset:8192\n\t" \
"v_mfma_f32_32x32x16_bf16 a[112:127], v[84:87], v[76:79], a[112:127]\n\t" \
"v_add_u32 v150, 0, v134\n\t" \
"ds_read_b128 v[108:111], v139 offset:12288\n\t" \
"v_mfma_f32_32x32x16_bf16 a[240:255], v[76:79], v[92:95], a[240:255]\n\t" \
"v_add_u32 v151, 0, v135\n\t" \
"s_waitcnt lgkmcnt(9)\n\t" \
"v_mfma_f32_32x32x16_bf16 a[0:15], v[112:115], v[96:99], a[0:15]\n\t" \
"s_waitcnt lgkmcnt(7)\n\t" \
"v_mfma_f32_32x32x16_bf16 a[128:143], v[96:99], v[120:123], a[128:143]\n\t" \
"s_waitcnt lgkmcnt(0)\n\t" \
"s_waitcnt vmcnt(8)\n\t" \
"s_barrier\n\t" \
"v_mfma_f32_32x32x16_bf16 a[16:31], v[116:119], v[96:99], a[16:31]\n\t" \
"ds_read_b64_tr_b16 v[80:81], v148 offset:0\n\t" \
"s_add_u32 m0, s70, 65536\n\t" \
"s_mov_b32 s54, 0\n\t" \
"buffer_load_dwordx4 %20, %14, s54 offen lds\n\t" \
"v_mfma_f32_32x32x16_bf16 a[144:159], v[96:99], v[124:127], a[144:159]\n\t" \
"ds_read_b64_tr_b16 v[82:83], v149 offset:1024\n\t" \
"v_mfma_f32_32x32x16_bf16 a[32:47], v[112:115], v[100:103], a[32:47]\n\t" \
"s_add_u32 m0, s70, 66560\n\t" \
"s_mov_b32 s55, 0\n\t" \
"buffer_load_dwordx4 %21, %14, s55 offen lds\n\t" \
"v_mfma_f32_32x32x16_bf16 a[160:175], v[100:103], v[120:123], a[160:175]\n\t" \
"ds_read_b128 v[64:67], v140 offset:0\n\t" \
"v_mfma_f32_32x32x16_bf16 a[48:63], v[116:119], v[100:103], a[48:63]\n\t" \
"s_add_u32 m0, s70, 73728\n\t" \
"s_mov_b32 s54, 262144\n\t" \
"buffer_load_dwordx4 %20, %14, s54 offen lds\n\t" \
"v_mfma_f32_32x32x16_bf16 a[176:191], v[100:103], v[124:127], a[176:191]\n\t" \
"ds_read_b64_tr_b16 v[88:89], v148 offset:16384\n\t" \
"v_mfma_f32_32x32x16_bf16 a[64:79], v[112:115], v[104:107], a[64:79]\n\t" \
"s_add_u32 m0, s70, 74752\n\t" \
"s_mov_b32 s55, 262144\n\t" \
"buffer_load_dwordx4 %21, %14, s55 offen lds\n\t" \
"v_mfma_f32_32x32x16_bf16 a[192:207], v[104:107], v[120:123], a[192:207]\n\t" \
"ds_read_b64_tr_b16 v[90:91], v149 offset:17408\n\t" \
"ds_read_b64_tr_b16 v[84:85], v150 offset:0\n\t" \
"v_mfma_f32_32x32x16_bf16 a[80:95], v[116:119], v[104:107], a[80:95]\n\t" \
"ds_read_b64_tr_b16 v[86:87], v151 offset:1024\n\t" \
"v_mfma_f32_32x32x16_bf16 a[208:223], v[104:107], v[124:127], a[208:223]\n\t" \
"ds_read_b64_tr_b16 v[92:93], v150 offset:16384\n\t" \
"v_mfma_f32_32x32x16_bf16 a[96:111], v[112:115], v[108:111], a[96:111]\n\t" \
"ds_read_b64_tr_b16 v[94:95], v151 offset:17408\n\t" \
"v_mfma_f32_32x32x16_bf16 a[224:239], v[108:111], v[120:123], a[224:239]\n\t" \
"ds_read_b128 v[68:71], v140 offset:4096\n\t" \
"v_mfma_f32_32x32x16_bf16 a[112:127], v[116:119], v[108:111], a[112:127]\n\t" \
"ds_read_b128 v[72:75], v140 offset:8192\n\t" \
"v_mfma_f32_32x32x16_bf16 a[240:255], v[108:111], v[124:127], a[240:255]\n\t" \
"ds_read_b128 v[76:79], v140 offset:12288\n\t" \
"s_waitcnt lgkmcnt(9)\n\t" \
"v_mfma_f32_32x32x16_bf16 a[0:15], v[80:83], v[64:67], a[0:15]\n\t" \
"ds_read_b64_tr_b16 v[112:113], v148 offset:4096\n\t" \
"s_waitcnt lgkmcnt(8)\n\t" \
"v_mfma_f32_32x32x16_bf16 a[128:143], v[64:67], v[88:91], a[128:143]\n\t" \
"s_add_u32 m0, s70, 81920\n\t" \
"s_mov_b32 s54, 0\n\t" \
"buffer_load_dwordx4 %22, %15, s54 offen lds\n\t" \
"s_waitcnt lgkmcnt(6)\n\t" \
"v_mfma_f32_32x32x16_bf16 a[16:31], v[84:87], v[64:67], a[16:31]\n\t" \
"ds_read_b64_tr_b16 v[114:115], v149 offset:5120\n\t" \
"s_waitcnt lgkmcnt(5)\n\t" \
"v_mfma_f32_32x32x16_bf16 a[144:159], v[64:67], v[92:95], a[144:159]\n\t" \
"s_add_u32 m0, s70, 82944\n\t" \
"s_mov_b32 s55, 0\n\t" \
"buffer_load_dwordx4 %23, %15, s55 offen lds\n\t" \
"s_waitcnt lgkmcnt(4)\n\t" \
"v_mfma_f32_32x32x16_bf16 a[32:47], v[80:83], v[68:71], a[32:47]\n\t" \
"ds_read_b128 v[96:99], v141 offset:0\n\t" \
"v_mfma_f32_32x32x16_bf16 a[160:175], v[68:71], v[88:91], a[160:175]\n\t" \
"s_add_u32 m0, s70, 90112\n\t" \
"s_add_u32 s54, s71, 128\n\t" \
"buffer_load_dwordx4 %18, %13, s54 offen lds\n\t" \
"v_mfma_f32_32x32x16_bf16 a[48:63], v[84:87], v[68:71], a[48:63]\n\t" \
"ds_read_b64_tr_b16 v[120:121], v148 offset:20480\n\t" \
"v_mfma_f32_32x32x16_bf16 a[176:191], v[68:71], v[92:95], a[176:191]\n\t" \
"s_add_u32 m0, s70, 91136\n\t" \
"s_add_u32 s55, s71, 128\n\t" \
"buffer_load_dwordx4 %19, %13, s55 offen lds\n\t" \
"s_waitcnt lgkmcnt(5)\n\t" \
"v_mfma_f32_32x32x16_bf16 a[64:79], v[80:83], v[72:75], a[64:79]\n\t" \
"ds_read_b64_tr_b16 v[122:123], v149 offset:21504\n\t" \
"v_mfma_f32_32x32x16_bf16 a[192:207], v[72:75], v[88:91], a[192:207]\n\t" \
"ds_read_b64_tr_b16 v[116:117], v150 offset:4096\n\t" \
"v_mfma_f32_32x32x16_bf16 a[80:95], v[84:87], v[72:75], a[80:95]\n\t" \
"ds_read_b64_tr_b16 v[118:119], v151 offset:5120\n\t" \
"v_mfma_f32_32x32x16_bf16 a[208:223], v[72:75], v[92:95], a[208:223]\n\t" \
"ds_read_b64_tr_b16 v[124:125], v150 offset:20480\n\t" \
"s_waitcnt lgkmcnt(8)\n\t" \
"v_mfma_f32_32x32x16_bf16 a[96:111], v[80:83], v[76:79], a[96:111]\n\t" \
"ds_read_b64_tr_b16 v[126:127], v151 offset:21504\n\t" \
"v_mfma_f32_32x32x16_bf16 a[224:239], v[76:79], v[88:91], a[224:239]\n\t" \
"ds_read_b128 v[100:103], v141 offset:4096\n\t" \
"v_mfma_f32_32x32x16_bf16 a[112:127], v[84:87], v[76:79], a[112:127]\n\t" \
"ds_read_b128 v[104:107], v141 offset:8192\n\t" \
"v_mfma_f32_32x32x16_bf16 a[240:255], v[76:79], v[92:95], a[240:255]\n\t" \
"ds_read_b128 v[108:111], v141 offset:12288\n\t" \
"s_waitcnt lgkmcnt(9)\n\t" \
"v_mfma_f32_32x32x16_bf16 a[0:15], v[112:115], v[96:99], a[0:15]\n\t" \
"ds_read_b64_tr_b16 v[80:81], v148 offset:8192\n\t" \
"s_waitcnt lgkmcnt(8)\n\t" \
"v_mfma_f32_32x32x16_bf16 a[128:143], v[96:99], v[120:123], a[128:143]\n\t" \
"s_add_u32 m0, s70, 98304\n\t" \
"s_add_u32 s54, s72, 128\n\t" \
"buffer_load_dwordx4 %18, %13, s54 offen lds\n\t" \
"s_waitcnt lgkmcnt(6)\n\t" \
"v_mfma_f32_32x32x16_bf16 a[16:31], v[116:119], v[96:99], a[16:31]\n\t" \
"ds_read_b64_tr_b16 v[82:83], v149 offset:9216\n\t" \
"s_waitcnt lgkmcnt(5)\n\t" \
"v_mfma_f32_32x32x16_bf16 a[144:159], v[96:99], v[124:127], a[144:159]\n\t" \
"s_add_u32 m0, s70, 99328\n\t" \
"s_add_u32 s55, s72, 128\n\t" \
"buffer_load_dwordx4 %19, %13, s55 offen lds\n\t" \
"s_waitcnt lgkmcnt(4)\n\t" \
"v_mfma_f32_32x32x16_bf16 a[32:47], v[112:115], v[100:103], a[32:47]\n\t" \
"ds_read_b128 v[64:67], v142 offset:0\n\t" \
"v_mfma_f32_32x32x16_bf16 a[160:175], v[100:103], v[120:123], a[160:175]\n\t" \
"s_add_u32 m0, s70, 106496\n\t" \
"s_add_u32 s54, s73, 128\n\t" \
"buffer_load_dwordx4 %18, %13, s54 offen lds\n\t" \
"v_mfma_f32_32x32x16_bf16 a[48:63], v[116:119], v[100:103], a[48:63]\n\t" \
"ds_read_b64_tr_b16 v[88:89], v148 offset:24576\n\t" \
"v_mfma_f32_32x32x16_bf16 a[176:191], v[100:103], v[124:127], a[176:191]\n\t" \
"s_add_u32 m0, s70, 107520\n\t" \
"s_add_u32 s55, s73, 128\n\t" \
"buffer_load_dwordx4 %19, %13, s55 offen lds\n\t" \
"s_waitcnt lgkmcnt(5)\n\t" \
"v_mfma_f32_32x32x16_bf16 a[64:79], v[112:115], v[104:107], a[64:79]\n\t" \
"ds_read_b64_tr_b16 v[90:91], v149 offset:25600\n\t" \
"v_mfma_f32_32x32x16_bf16 a[192:207], v[104:107], v[120:123], a[192:207]\n\t" \
"ds_read_b64_tr_b16 v[84:85], v150 offset:8192\n\t" \
"v_mfma_f32_32x32x16_bf16 a[80:95], v[116:119], v[104:107], a[80:95]\n\t" \
"ds_read_b64_tr_b16 v[86:87], v151 offset:9216\n\t" \
"v_mfma_f32_32x32x16_bf16 a[208:223], v[104:107], v[124:127], a[208:223]\n\t" \
"ds_read_b64_tr_b16 v[92:93], v150 offset:24576\n\t" \
"s_waitcnt lgkmcnt(8)\n\t" \
"v_mfma_f32_32x32x16_bf16 a[96:111], v[112:115], v[108:111], a[96:111]\n\t" \
"ds_read_b64_tr_b16 v[94:95], v151 offset:25600\n\t" \
"v_mfma_f32_32x32x16_bf16 a[224:239], v[108:111], v[120:123], a[224:239]\n\t" \
"ds_read_b128 v[68:71], v142 offset:4096\n\t" \
"v_mfma_f32_32x32x16_bf16 a[112:127], v[116:119], v[108:111], a[112:127]\n\t" \
"ds_read_b128 v[72:75], v142 offset:8192\n\t" \
"v_mfma_f32_32x32x16_bf16 a[240:255], v[108:111], v[124:127], a[240:255]\n\t" \
"ds_read_b128 v[76:79], v142 offset:12288\n\t" \
"s_waitcnt lgkmcnt(9)\n\t" \
"v_mfma_f32_32x32x16_bf16 a[0:15], v[80:83], v[64:67], a[0:15]\n\t" \
"ds_read_b64_tr_b16 v[112:113], v148 offset:12288\n\t" \
"s_waitcnt lgkmcnt(8)\n\t" \
"v_mfma_f32_32x32x16_bf16 a[128:143], v[64:67], v[88:91], a[128:143]\n\t" \
"s_add_u32 m0, s70, 114688\n\t" \
"s_add_u32 s54, s74, 128\n\t" \
"buffer_load_dwordx4 %18, %13, s54 offen lds\n\t" \
"s_waitcnt lgkmcnt(6)\n\t" \
"v_mfma_f32_32x32x16_bf16 a[16:31], v[84:87], v[64:67], a[16:31]\n\t" \
"ds_read_b64_tr_b16 v[114:115], v149 offset:13312\n\t" \
"s_waitcnt lgkmcnt(5)\n\t" \
"v_mfma_f32_32x32x16_bf16 a[144:159], v[64:67], v[92:95], a[144:159]\n\t" \
"s_add_u32 m0, s70, 115712\n\t" \
"s_add_u32 s55, s74, 128\n\t" \
"buffer_load_dwordx4 %19, %13, s55 offen lds\n\t" \
"s_waitcnt lgkmcnt(4)\n\t" \
"v_mfma_f32_32x32x16_bf16 a[32:47], v[80:83], v[68:71], a[32:47]\n\t" \
"ds_read_b128 v[96:99], v143 offset:0\n\t" \
"v_mfma_f32_32x32x16_bf16 a[160:175], v[68:71], v[88:91], a[160:175]\n\t" \
"s_add_u32 m0, s70, 122880\n\t" \
"s_mov_b32 s54, 524288\n\t" \
"buffer_load_dwordx4 %20, %14, s54 offen lds\n\t" \
"v_mfma_f32_32x32x16_bf16 a[48:63], v[84:87], v[68:71], a[48:63]\n\t" \
"ds_read_b64_tr_b16 v[120:121], v148 offset:28672\n\t" \
"v_mfma_f32_32x32x16_bf16 a[176:191], v[68:71], v[92:95], a[176:191]\n\t" \
"s_add_u32 m0, s70, 123904\n\t" \
"s_mov_b32 s55, 524288\n\t" \
"buffer_load_dwordx4 %21, %14, s55 offen lds\n\t" \
"s_waitcnt lgkmcnt(5)\n\t" \
"v_mfma_f32_32x32x16_bf16 a[64:79], v[80:83], v[72:75], a[64:79]\n\t" \
"ds_read_b64_tr_b16 v[122:123], v149 offset:29696\n\t" \
"v_mfma_f32_32x32x16_bf16 a[192:207], v[72:75], v[88:91], a[192:207]\n\t" \
"ds_read_b64_tr_b16 v[116:117], v150 offset:12288\n\t" \
"v_mfma_f32_32x32x16_bf16 a[80:95], v[84:87], v[72:75], a[80:95]\n\t" \
"ds_read_b64_tr_b16 v[118:119], v151 offset:13312\n\t" \
"v_mfma_f32_32x32x16_bf16 a[208:223], v[72:75], v[92:95], a[208:223]\n\t" \
"ds_read_b64_tr_b16 v[124:125], v150 offset:28672\n\t" \
"s_waitcnt lgkmcnt(8)\n\t" \
"v_mfma_f32_32x32x16_bf16 a[96:111], v[80:83], v[76:79], a[96:111]\n\t" \
"ds_read_b64_tr_b16 v[126:127], v151 offset:29696\n\t" \
"v_mfma_f32_32x32x16_bf16 a[224:239], v[76:79], v[88:91], a[224:239]\n\t" \
"ds_read_b128 v[100:103], v143 offset:4096\n\t" \
"v_mfma_f32_32x32x16_bf16 a[112:127], v[84:87], v[76:79], a[112:127]\n\t" \
"ds_read_b128 v[104:107], v143 offset:8192\n\t" \
"v_mfma_f32_32x32x16_bf16 a[240:255], v[76:79], v[92:95], a[240:255]\n\t" \
"ds_read_b128 v[108:111], v143 offset:12288\n\t" \
"s_waitcnt lgkmcnt(9)\n\t" \
"v_mfma_f32_32x32x16_bf16 a[0:15], v[112:115], v[96:99], a[0:15]\n\t" \
"s_waitcnt lgkmcnt(7)\n\t" \
"v_mfma_f32_32x32x16_bf16 a[128:143], v[96:99], v[120:123], a[128:143]\n\t" \
"s_waitcnt lgkmcnt(0)\n\t" \
"s_waitcnt vmcnt(10)\n\t" \
"s_barrier\n\t" \
"v_mfma_f32_32x32x16_bf16 a[16:31], v[116:119], v[96:99], a[16:31]\n\t" \
"s_add_u32 m0, s70, 131072\n\t" \
"s_mov_b32 s54, 786432\n\t" \
"buffer_load_dwordx4 %20, %14, s54 offen lds\n\t" \
"v_mfma_f32_32x32x16_bf16 a[144:159], v[96:99], v[124:127], a[144:159]\n\t" \
"v_mfma_f32_32x32x16_bf16 a[32:47], v[112:115], v[100:103], a[32:47]\n\t" \
"v_mfma_f32_32x32x16_bf16 a[160:175], v[100:103], v[120:123], a[160:175]\n\t" \
"s_add_u32 m0, s70, 132096\n\t" \
"s_mov_b32 s55, 786432\n\t" \
"buffer_load_dwordx4 %21, %14, s55 offen lds\n\t" \
"v_mfma_f32_32x32x16_bf16 a[48:63], v[116:119], v[100:103], a[48:63]\n\t" \
"v_mfma_f32_32x32x16_bf16 a[176:191], v[100:103], v[124:127], a[176:191]\n\t" \
"v_mfma_f32_32x32x16_bf16 a[64:79], v[112:115], v[104:107], a[64:79]\n\t" \
"v_mfma_f32_32x32x16_bf16 a[192:207], v[104:107], v[120:123], a[192:207]\n\t" \
"s_add_u32 m0, s70, 139264\n\t" \
"s_mov_b32 s54, 196608\n\t" \
"buffer_load_dwordx4 %22, %15, s54 offen lds\n\t" \
"v_mfma_f32_32x32x16_bf16 a[80:95], v[116:119], v[104:107], a[80:95]\n\t" \
"v_mfma_f32_32x32x16_bf16 a[208:223], v[104:107], v[124:127], a[208:223]\n\t" \
"v_mfma_f32_32x32x16_bf16 a[96:111], v[112:115], v[108:111], a[96:111]\n\t" \
"s_add_u32 m0, s70, 140288\n\t" \
"s_mov_b32 s55, 196608\n\t" \
"buffer_load_dwordx4 %23, %15, s55 offen lds\n\t" \
"v_mfma_f32_32x32x16_bf16 a[224:239], v[108:111], v[120:123], a[224:239]\n\t" \
"v_mfma_f32_32x32x16_bf16 a[112:127], v[116:119], v[108:111], a[112:127]\n\t" \
"v_mfma_f32_32x32x16_bf16 a[240:255], v[108:111], v[124:127], a[240:255]\n\t" \
"s_add_u32 m0, s70, 147456\n\t" \
"s_add_u32 s54, s71, 256\n\t" \
"buffer_load_dwordx4 %18, %13, s54 offen lds\n\t" \
"s_add_u32 m0, s70, 148480\n\t" \
"s_add_u32 s55, s71, 256\n\t" \
"buffer_load_dwordx4 %19, %13, s55 offen lds\n\t" \
"s_add_u32 m0, s70, 155648\n\t" \
"s_add_u32 s54, s72, 256\n\t" \
"buffer_load_dwordx4 %18, %13, s54 offen lds\n\t" \
"s_add_u32 m0, s70, 156672\n\t" \
"s_add_u32 s55, s72, 256\n\t" \
"buffer_load_dwordx4 %19, %13, s55 offen lds\n\t" \
"s_add_u32 m0, s70, 0\n\t" \
"s_add_u32 s54, s73, 256\n\t" \
"buffer_load_dwordx4 %18, %13, s54 offen lds\n\t" \
"s_add_u32 m0, s70, 1024\n\t" \
"s_add_u32 s55, s73, 256\n\t" \
"buffer_load_dwordx4 %19, %13, s55 offen lds\n\t" \
"s_add_u32 m0, s70, 8192\n\t" \
"s_add_u32 s54, s74, 256\n\t" \
"buffer_load_dwordx4 %18, %13, s54 offen lds\n\t" \
"s_add_u32 m0, s70, 9216\n\t" \
"s_add_u32 s55, s74, 256\n\t" \
"buffer_load_dwordx4 %19, %13, s55 offen lds\n\t" \
"s_add_u32 m0, s70, 16384\n\t" \
"s_mov_b32 s54, 1048576\n\t" \
"buffer_load_dwordx4 %20, %14, s54 offen lds\n\t" \
"s_add_u32 m0, s70, 17408\n\t" \
"s_mov_b32 s55, 1048576\n\t" \
"buffer_load_dwordx4 %21, %14, s55 offen lds\n\t" \
"s_add_u32 m0, s70, 24576\n\t" \
"s_mov_b32 s54, 1310720\n\t" \
"buffer_load_dwordx4 %20, %14, s54 offen lds\n\t" \
"s_add_u32 m0, s70, 25600\n\t" \
"s_mov_b32 s55, 1310720\n\t" \
"buffer_load_dwordx4 %21, %14, s55 offen lds\n\t" \
"s_lshl_b32 s56, s75, 13\n\t" \
"s_sub_u32 s81, %1, s56\n\t" \
"s_sub_u32 s82, %2, s56\n\t" \
"s_lshr_b32 s56, s80, 2\n\t" \
"s_lshr_b32 s57, s75, 1\n\t" \
"s_lshl_b32 s57, s57, 15\n\t" \
"s_add_u32 s56, s56, s57\n\t" \
"s_add_u32 s81, s81, s56\n\t" \
"s_add_u32 s82, s82, s56\n\t" \
"s_and_b32 s57, s75, 1\n\t" \
"s_lshl_b32 s56, s57, 12\n\t" \
"s_add_u32 s81, s81, s56\n\t" \
"s_lshl_b32 s56, s57, 11\n\t" \
"s_add_u32 s82, s82, s56\n\t" \
"v_accvgpr_read_b32 v64, a0\n\t" \
"v_accvgpr_read_b32 v65, a1\n\t" \
"v_accvgpr_read_b32 v66, a2\n\t" \
"v_accvgpr_read_b32 v67, a3\n\t" \
"v_accvgpr_read_b32 v68, a4\n\t" \
"v_accvgpr_read_b32 v69, a5\n\t" \
"v_accvgpr_read_b32 v70, a6\n\t" \
"v_accvgpr_read_b32 v71, a7\n\t" \
"v_cvt_pk_bf16_f32 v72, v64, v65\n\t" \
"v_cvt_pk_bf16_f32 v73, v66, v67\n\t" \
"v_cvt_pk_bf16_f32 v74, v68, v69\n\t" \
"v_cvt_pk_bf16_f32 v75, v70, v71\n\t" \
"s_add_u32 s59, s81, 0\n\t" \
"buffer_store_dwordx4 v[72:75], v36, %0, s59 offen sc1\n\t" \
"v_accvgpr_read_b32 v76, a8\n\t" \
"v_accvgpr_read_b32 v77, a9\n\t" \
"v_accvgpr_read_b32 v78, a10\n\t" \
"v_accvgpr_read_b32 v79, a11\n\t" \
"v_accvgpr_read_b32 v80, a12\n\t" \
"v_accvgpr_read_b32 v81, a13\n\t" \
"v_accvgpr_read_b32 v82, a14\n\t" \
"v_accvgpr_read_b32 v83, a15\n\t" \
"v_cvt_pk_bf16_f32 v84, v76, v77\n\t" \
"v_cvt_pk_bf16_f32 v85, v78, v79\n\t" \
"v_cvt_pk_bf16_f32 v86, v80, v81\n\t" \
"v_cvt_pk_bf16_f32 v87, v82, v83\n\t" \
"s_add_u32 s59, s81, 1024\n\t" \
"buffer_store_dwordx4 v[84:87], v36, %0, s59 offen sc1\n\t" \
"v_accvgpr_read_b32 v88, a16\n\t" \
"v_accvgpr_read_b32 v89, a17\n\t" \
"v_accvgpr_read_b32 v90, a18\n\t" \
"v_accvgpr_read_b32 v91, a19\n\t" \
"v_accvgpr_read_b32 v92, a20\n\t" \
"v_accvgpr_read_b32 v93, a21\n\t" \
"v_accvgpr_read_b32 v94, a22\n\t" \
"v_accvgpr_read_b32 v95, a23\n\t" \
"v_cvt_pk_bf16_f32 v96, v88, v89\n\t" \
"v_cvt_pk_bf16_f32 v97, v90, v91\n\t" \
"v_cvt_pk_bf16_f32 v98, v92, v93\n\t" \
"v_cvt_pk_bf16_f32 v99, v94, v95\n\t" \
"s_add_u32 s59, s81, 2048\n\t" \
"buffer_store_dwordx4 v[96:99], v36, %0, s59 offen sc1\n\t" \
"v_accvgpr_read_b32 v100, a24\n\t" \
"v_accvgpr_read_b32 v101, a25\n\t" \
"v_accvgpr_read_b32 v102, a26\n\t" \
"v_accvgpr_read_b32 v103, a27\n\t" \
"v_accvgpr_read_b32 v104, a28\n\t" \
"v_accvgpr_read_b32 v105, a29\n\t" \
"v_accvgpr_read_b32 v106, a30\n\t" \
"v_accvgpr_read_b32 v107, a31\n\t" \
"v_cvt_pk_bf16_f32 v108, v100, v101\n\t" \
"v_cvt_pk_bf16_f32 v109, v102, v103\n\t" \
"v_cvt_pk_bf16_f32 v110, v104, v105\n\t" \
"v_cvt_pk_bf16_f32 v111, v106, v107\n\t" \
"s_add_u32 s59, s81, 3072\n\t" \
"buffer_store_dwordx4 v[108:111], v36, %0, s59 offen sc1\n\t" \
"v_accvgpr_read_b32 v64, a32\n\t" \
"v_accvgpr_read_b32 v65, a33\n\t" \
"v_accvgpr_read_b32 v66, a34\n\t" \
"v_accvgpr_read_b32 v67, a35\n\t" \
"v_accvgpr_read_b32 v68, a36\n\t" \
"v_accvgpr_read_b32 v69, a37\n\t" \
"v_accvgpr_read_b32 v70, a38\n\t" \
"v_accvgpr_read_b32 v71, a39\n\t" \
"v_cvt_pk_bf16_f32 v72, v64, v65\n\t" \
"v_cvt_pk_bf16_f32 v73, v66, v67\n\t" \
"v_cvt_pk_bf16_f32 v74, v68, v69\n\t" \
"v_cvt_pk_bf16_f32 v75, v70, v71\n\t" \
"s_add_u32 s59, s81, 8192\n\t" \
"buffer_store_dwordx4 v[72:75], v36, %0, s59 offen sc1\n\t" \
"v_accvgpr_read_b32 v76, a40\n\t" \
"v_accvgpr_read_b32 v77, a41\n\t" \
"v_accvgpr_read_b32 v78, a42\n\t" \
"v_accvgpr_read_b32 v79, a43\n\t" \
"v_accvgpr_read_b32 v80, a44\n\t" \
"v_accvgpr_read_b32 v81, a45\n\t" \
"v_accvgpr_read_b32 v82, a46\n\t" \
"v_accvgpr_read_b32 v83, a47\n\t" \
"v_cvt_pk_bf16_f32 v84, v76, v77\n\t" \
"v_cvt_pk_bf16_f32 v85, v78, v79\n\t" \
"v_cvt_pk_bf16_f32 v86, v80, v81\n\t" \
"v_cvt_pk_bf16_f32 v87, v82, v83\n\t" \
"s_add_u32 s59, s81, 9216\n\t" \
"buffer_store_dwordx4 v[84:87], v36, %0, s59 offen sc1\n\t" \
"v_accvgpr_read_b32 v88, a48\n\t" \
"v_accvgpr_read_b32 v89, a49\n\t" \
"v_accvgpr_read_b32 v90, a50\n\t" \
"v_accvgpr_read_b32 v91, a51\n\t" \
"v_accvgpr_read_b32 v92, a52\n\t" \
"v_accvgpr_read_b32 v93, a53\n\t" \
"v_accvgpr_read_b32 v94, a54\n\t" \
"v_accvgpr_read_b32 v95, a55\n\t" \
"v_cvt_pk_bf16_f32 v96, v88, v89\n\t" \
"v_cvt_pk_bf16_f32 v97, v90, v91\n\t" \
"v_cvt_pk_bf16_f32 v98, v92, v93\n\t" \
"v_cvt_pk_bf16_f32 v99, v94, v95\n\t" \
"s_add_u32 s59, s81, 10240\n\t" \
"buffer_store_dwordx4 v[96:99], v36, %0, s59 offen sc1\n\t" \
"v_accvgpr_read_b32 v100, a56\n\t" \
"v_accvgpr_read_b32 v101, a57\n\t" \
"v_accvgpr_read_b32 v102, a58\n\t" \
"v_accvgpr_read_b32 v103, a59\n\t" \
"v_accvgpr_read_b32 v104, a60\n\t" \
"v_accvgpr_read_b32 v105, a61\n\t" \
"v_accvgpr_read_b32 v106, a62\n\t" \
"v_accvgpr_read_b32 v107, a63\n\t" \
"v_cvt_pk_bf16_f32 v108, v100, v101\n\t" \
"v_cvt_pk_bf16_f32 v109, v102, v103\n\t" \
"v_cvt_pk_bf16_f32 v110, v104, v105\n\t" \
"v_cvt_pk_bf16_f32 v111, v106, v107\n\t" \
"s_add_u32 s59, s81, 11264\n\t" \
"buffer_store_dwordx4 v[108:111], v36, %0, s59 offen sc1\n\t" \
"s_add_u32 s56, s75, 4\n\t" \
"s_lshl_b32 s83, s56, 13\n\t" \
"v_xor_b32 v18, 0, %24\n\t" \
"v_add_u32 v18, s83, v18\n\t" \
"v_xor_b32 v19, 32, %24\n\t" \
"v_add_u32 v19, s83, v19\n\t" \
"v_xor_b32 v20, 64, %24\n\t" \
"v_add_u32 v20, s83, v20\n\t" \
"v_xor_b32 v21, 96, %24\n\t" \
"v_add_u32 v21, s83, v21\n\t" \
"v_xor_b32 v39, 0, %25\n\t" \
"v_add_u32 v39, 65536, v39\n\t" \
"v_xor_b32 v40, 16, %25\n\t" \
"v_add_u32 v40, 65536, v40\n\t" \
"v_xor_b32 v41, 64, %25\n\t" \
"v_add_u32 v41, 65536, v41\n\t" \
"v_xor_b32 v42, 80, %25\n\t" \
"v_add_u32 v42, 65536, v42\n\t" \
"v_xor_b32 v43, 128, %25\n\t" \
"v_add_u32 v43, 65536, v43\n\t" \
"v_xor_b32 v44, 144, %25\n\t" \
"v_add_u32 v44, 65536, v44\n\t" \
"v_xor_b32 v45, 192, %25\n\t" \
"v_add_u32 v45, 65536, v45\n\t" \
"v_xor_b32 v46, 208, %25\n\t" \
"v_add_u32 v46, 65536, v46\n\t" \
"v_xor_b32 v47, 0, %25\n\t" \
"v_add_u32 v47, 73728, v47\n\t" \
"v_xor_b32 v48, 16, %25\n\t" \
"v_add_u32 v48, 73728, v48\n\t" \
"v_xor_b32 v49, 64, %25\n\t" \
"v_add_u32 v49, 73728, v49\n\t" \
"v_xor_b32 v50, 80, %25\n\t" \
"v_add_u32 v50, 73728, v50\n\t" \
"v_xor_b32 v51, 128, %25\n\t" \
"v_add_u32 v51, 73728, v51\n\t" \
"v_xor_b32 v52, 144, %25\n\t" \
"v_add_u32 v52, 73728, v52\n\t" \
"v_xor_b32 v53, 192, %25\n\t" \
"v_add_u32 v53, 73728, v53\n\t" \
"v_xor_b32 v54, 208, %25\n\t" \
"v_add_u32 v54, 73728, v54\n\t" \
"v_lshrrev_b32 v32, 4, v36\n\t" \
"v_bfe_u32 v33, v32, 2, 2\n\t" \
"v_lshrrev_b32 v34, 5, v32\n\t" \
"v_lshl_add_u32 v34, v34, 3, v33\n\t" \
"v_lshlrev_b32 v34, 7, v34\n\t" \
"v_and_b32 v33, 2, v33\n\t" \
"v_lshl_add_u32 v34, v33, 5, v34\n\t" \
"v_bfe_u32 v33, v32, 4, 1\n\t" \
"v_lshl_add_u32 v34, v33, 5, v34\n\t" \
"v_and_b32 v33, 3, v32\n\t" \
"v_lshl_add_u32 v34, v33, 3, v34\n\t" \
"v_add_u32 v34, 81920, v34\n\t" \
"v_add_u32 v22, %8, v34\n\t" \
"v_xor_b32 v23, 64, v22\n\t" \
"ds_read_b64_tr_b16 v[200:201], v39 offset:0\n\t" \
"ds_read_b64_tr_b16 v[202:203], v40 offset:1024\n\t" \
"ds_read_b128 v[192:195], v18 offset:0\n\t" \
"ds_read_b128 v[196:199], v18 offset:4096\n\t" \
"ds_read_b64_tr_b16 v[204:205], v41 offset:0\n\t" \
"ds_read_b64_tr_b16 v[206:207], v42 offset:1024\n\t" \
"ds_read_b64_tr_b16 v[208:209], v43 offset:0\n\t" \
"ds_read_b64_tr_b16 v[210:211], v44 offset:1024\n\t" \
"ds_read_b64_tr_b16 v[212:213], v45 offset:0\n\t" \
"ds_read_b64_tr_b16 v[214:215], v46 offset:1024\n\t" \
"ds_read_b64_tr_b16 v[216:217], v22 offset:0\n\t" \
"ds_read_b64_tr_b16 v[218:219], v22 offset:512\n\t" \
"ds_read_b64_tr_b16 v[220:221], v23 offset:0\n\t" \
"ds_read_b64_tr_b16 v[222:223], v23 offset:512\n\t" \
"s_waitcnt lgkmcnt(11)\n\t" \
"v_mfma_f32_32x32x16_bf16 v[64:79], v[200:203], v[192:195], 0\n\t" \
"ds_read_b64_tr_b16 v[232:233], v39 offset:4096\n\t" \
"v_accvgpr_read_b32 v24, a64\n\t" \
"ds_read_b64_tr_b16 v[234:235], v40 offset:5120\n\t" \
"v_accvgpr_read_b32 v25, a65\n\t" \
"s_waitcnt lgkmcnt(12)\n\t" \
"v_mfma_f32_32x32x16_bf16 v[128:143], v[200:203], v[196:199], 0\n\t" \
"ds_read_b128 v[224:227], v19 offset:0\n\t" \
"v_accvgpr_read_b32 v26, a66\n\t" \
"ds_read_b128 v[228:231], v19 offset:4096\n\t" \
"s_waitcnt lgkmcnt(12)\n\t" \
"v_mfma_f32_32x32x16_bf16 v[80:95], v[204:207], v[192:195], 0\n\t" \
"v_accvgpr_read_b32 v27, a67\n\t" \
"ds_read_b64_tr_b16 v[236:237], v41 offset:4096\n\t" \
"v_accvgpr_read_b32 v28, a68\n\t" \
"v_mfma_f32_32x32x16_bf16 v[144:159], v[204:207], v[196:199], 0\n\t" \
"ds_read_b64_tr_b16 v[238:239], v42 offset:5120\n\t" \
"v_accvgpr_read_b32 v29, a69\n\t" \
"ds_read_b64_tr_b16 v[240:241], v43 offset:4096\n\t" \
"v_accvgpr_read_b32 v30, a70\n\t" \
"s_waitcnt lgkmcnt(13)\n\t" \
"v_mfma_f32_32x32x16_bf16 v[96:111], v[208:211], v[192:195], 0\n\t" \
"ds_read_b64_tr_b16 v[242:243], v44 offset:5120\n\t" \
"v_accvgpr_read_b32 v31, a71\n\t" \
"ds_read_b64_tr_b16 v[244:245], v45 offset:4096\n\t" \
"v_mfma_f32_32x32x16_bf16 v[160:175], v[208:211], v[196:199], 0\n\t" \
"v_cvt_pk_bf16_f32 v32, v24, v25\n\t" \
"s_waitcnt lgkmcnt(10)\n\t" \
"ds_read_b64_tr_b16 v[246:247], v46 offset:5120\n\t" \
"v_cvt_pk_bf16_f32 v33, v26, v27\n\t" \
"v_mfma_f32_32x32x16_bf16 v[112:127], v[212:215], v[192:195], 0\n\t" \
"ds_read_b64_tr_b16 v[248:249], v22 offset:2048\n\t" \
"v_cvt_pk_bf16_f32 v34, v28, v29\n\t" \
"ds_read_b64_tr_b16 v[250:251], v22 offset:2560\n\t" \
"v_cvt_pk_bf16_f32 v35, v30, v31\n\t" \
"v_mfma_f32_32x32x16_bf16 v[176:191], v[212:215], v[196:199], 0\n\t" \
"ds_read_b64_tr_b16 v[252:253], v23 offset:2048\n\t" \
"s_add_u32 s59, s81, 16384\n\t" \
"buffer_store_dwordx4 v[32:35], v36, %0, s59 offen sc1\n\t" \
"ds_read_b64_tr_b16 v[254:255], v23 offset:2560\n\t" \
"v_mfma_f32_32x32x16_bf16 a[0:15], v[216:219], v[192:195], 0\n\t" \
"v_accvgpr_read_b32 v24, a72\n\t" \
"v_accvgpr_read_b32 v25, a73\n\t" \
"v_accvgpr_read_b32 v26, a74\n\t" \
"v_mfma_f32_32x32x16_bf16 a[16:31], v[216:219], v[196:199], 0\n\t" \
"v_accvgpr_read_b32 v27, a75\n\t" \
"v_accvgpr_read_b32 v28, a76\n\t" \
"v_accvgpr_read_b32 v29, a77\n\t" \
"v_accvgpr_read_b32 v30, a78\n\t" \
"s_waitcnt lgkmcnt(14)\n\t" \
"v_mfma_f32_32x32x16_bf16 a[32:47], v[192:195], v[220:223], 0\n\t" \
"v_accvgpr_read_b32 v31, a79\n\t" \
"v_cvt_pk_bf16_f32 v32, v24, v25\n\t" \
"v_cvt_pk_bf16_f32 v33, v26, v27\n\t" \
"v_mfma_f32_32x32x16_bf16 a[48:63], v[196:199], v[220:223], 0\n\t" \
"v_cvt_pk_bf16_f32 v34, v28, v29\n\t" \
"v_cvt_pk_bf16_f32 v35, v30, v31\n\t" \
"s_add_u32 s59, s81, 17408\n\t" \
"buffer_store_dwordx4 v[32:35], v36, %0, s59 offen sc1\n\t" \
"s_waitcnt lgkmcnt(11)\n\t" \
"v_mfma_f32_32x32x16_bf16 v[64:79], v[232:235], v[224:227], v[64:79]\n\t" \
"ds_read_b64_tr_b16 v[200:201], v47 offset:0\n\t" \
"v_accvgpr_read_b32 v24, a80\n\t" \
"ds_read_b64_tr_b16 v[202:203], v48 offset:1024\n\t" \
"v_accvgpr_read_b32 v25, a81\n\t" \
"s_waitcnt lgkmcnt(12)\n\t" \
"v_mfma_f32_32x32x16_bf16 v[128:143], v[232:235], v[228:231], v[128:143]\n\t" \
"ds_read_b128 v[192:195], v20 offset:0\n\t" \
"v_accvgpr_read_b32 v26, a82\n\t" \
"ds_read_b128 v[196:199], v20 offset:4096\n\t" \
"s_waitcnt lgkmcnt(12)\n\t" \
"v_mfma_f32_32x32x16_bf16 v[80:95], v[236:239], v[224:227], v[80:95]\n\t" \
"v_accvgpr_read_b32 v27, a83\n\t" \
"ds_read_b64_tr_b16 v[204:205], v49 offset:0\n\t" \
"v_accvgpr_read_b32 v28, a84\n\t" \
"v_mfma_f32_32x32x16_bf16 v[144:159], v[236:239], v[228:231], v[144:159]\n\t" \
"ds_read_b64_tr_b16 v[206:207], v50 offset:1024\n\t" \
"v_accvgpr_read_b32 v29, a85\n\t" \
"ds_read_b64_tr_b16 v[208:209], v51 offset:0\n\t" \
"v_accvgpr_read_b32 v30, a86\n\t" \
"s_waitcnt lgkmcnt(13)\n\t" \
"v_mfma_f32_32x32x16_bf16 v[96:111], v[240:243], v[224:227], v[96:111]\n\t" \
"ds_read_b64_tr_b16 v[210:211], v52 offset:1024\n\t" \
"v_accvgpr_read_b32 v31, a87\n\t" \
"ds_read_b64_tr_b16 v[212:213], v53 offset:0\n\t" \
"v_mfma_f32_32x32x16_bf16 v[160:175], v[240:243], v[228:231], v[160:175]\n\t" \
"v_cvt_pk_bf16_f32 v32, v24, v25\n\t" \
"s_waitcnt lgkmcnt(10)\n\t" \
"ds_read_b64_tr_b16 v[214:215], v54 offset:1024\n\t" \
"v_cvt_pk_bf16_f32 v33, v26, v27\n\t" \
"v_mfma_f32_32x32x16_bf16 v[112:127], v[244:247], v[224:227], v[112:127]\n\t" \
"ds_read_b64_tr_b16 v[216:217], v22 offset:4096\n\t" \
"v_cvt_pk_bf16_f32 v34, v28, v29\n\t" \
"ds_read_b64_tr_b16 v[218:219], v22 offset:4608\n\t" \
"v_cvt_pk_bf16_f32 v35, v30, v31\n\t" \
"v_mfma_f32_32x32x16_bf16 v[176:191], v[244:247], v[228:231], v[176:191]\n\t" \
"ds_read_b64_tr_b16 v[220:221], v23 offset:4096\n\t" \
"s_add_u32 s59, s81, 18432\n\t" \
"buffer_store_dwordx4 v[32:35], v36, %0, s59 offen sc1\n\t" \
"ds_read_b64_tr_b16 v[222:223], v23 offset:4608\n\t" \
"v_mfma_f32_32x32x16_bf16 a[0:15], v[248:251], v[224:227], a[0:15]\n\t" \
"v_accvgpr_read_b32 v24, a88\n\t" \
"v_accvgpr_read_b32 v25, a89\n\t" \
"v_accvgpr_read_b32 v26, a90\n\t" \
"v_mfma_f32_32x32x16_bf16 a[16:31], v[248:251], v[228:231], a[16:31]\n\t" \
"v_accvgpr_read_b32 v27, a91\n\t" \
"v_accvgpr_read_b32 v28, a92\n\t" \
"v_accvgpr_read_b32 v29, a93\n\t" \
"v_accvgpr_read_b32 v30, a94\n\t" \
"s_waitcnt lgkmcnt(14)\n\t" \
"v_mfma_f32_32x32x16_bf16 a[32:47], v[224:227], v[252:255], a[32:47]\n\t" \
"v_accvgpr_read_b32 v31, a95\n\t" \
"v_cvt_pk_bf16_f32 v32, v24, v25\n\t" \
"v_cvt_pk_bf16_f32 v33, v26, v27\n\t" \
"v_mfma_f32_32x32x16_bf16 a[48:63], v[228:231], v[252:255], a[48:63]\n\t" \
"v_cvt_pk_bf16_f32 v34, v28, v29\n\t" \
"v_cvt_pk_bf16_f32 v35, v30, v31\n\t" \
"s_add_u32 s59, s81, 19456\n\t" \
"buffer_store_dwordx4 v[32:35], v36, %0, s59 offen sc1\n\t" \
"s_waitcnt lgkmcnt(11)\n\t" \
"v_mfma_f32_32x32x16_bf16 v[64:79], v[200:203], v[192:195], v[64:79]\n\t" \
"ds_read_b64_tr_b16 v[232:233], v47 offset:4096\n\t" \
"v_accvgpr_read_b32 v24, a96\n\t" \
"ds_read_b64_tr_b16 v[234:235], v48 offset:5120\n\t" \
"v_accvgpr_read_b32 v25, a97\n\t" \
"s_waitcnt lgkmcnt(12)\n\t" \
"v_mfma_f32_32x32x16_bf16 v[128:143], v[200:203], v[196:199], v[128:143]\n\t" \
"ds_read_b128 v[224:227], v21 offset:0\n\t" \
"v_accvgpr_read_b32 v26, a98\n\t" \
"ds_read_b128 v[228:231], v21 offset:4096\n\t" \
"s_waitcnt lgkmcnt(12)\n\t" \
"v_mfma_f32_32x32x16_bf16 v[80:95], v[204:207], v[192:195], v[80:95]\n\t" \
"v_accvgpr_read_b32 v27, a99\n\t" \
"ds_read_b64_tr_b16 v[236:237], v49 offset:4096\n\t" \
"v_accvgpr_read_b32 v28, a100\n\t" \
"v_mfma_f32_32x32x16_bf16 v[144:159], v[204:207], v[196:199], v[144:159]\n\t" \
"ds_read_b64_tr_b16 v[238:239], v50 offset:5120\n\t" \
"v_accvgpr_read_b32 v29, a101\n\t" \
"ds_read_b64_tr_b16 v[240:241], v51 offset:4096\n\t" \
"v_accvgpr_read_b32 v30, a102\n\t" \
"s_waitcnt lgkmcnt(13)\n\t" \
"v_mfma_f32_32x32x16_bf16 v[96:111], v[208:211], v[192:195], v[96:111]\n\t" \
"ds_read_b64_tr_b16 v[242:243], v52 offset:5120\n\t" \
"v_accvgpr_read_b32 v31, a103\n\t" \
"ds_read_b64_tr_b16 v[244:245], v53 offset:4096\n\t" \
"v_mfma_f32_32x32x16_bf16 v[160:175], v[208:211], v[196:199], v[160:175]\n\t" \
"v_cvt_pk_bf16_f32 v32, v24, v25\n\t" \
"s_waitcnt lgkmcnt(10)\n\t" \
"ds_read_b64_tr_b16 v[246:247], v54 offset:5120\n\t" \
"v_cvt_pk_bf16_f32 v33, v26, v27\n\t" \
"v_mfma_f32_32x32x16_bf16 v[112:127], v[212:215], v[192:195], v[112:127]\n\t" \
"ds_read_b64_tr_b16 v[248:249], v22 offset:6144\n\t" \
"v_cvt_pk_bf16_f32 v34, v28, v29\n\t" \
"ds_read_b64_tr_b16 v[250:251], v22 offset:6656\n\t" \
"v_cvt_pk_bf16_f32 v35, v30, v31\n\t" \
"v_mfma_f32_32x32x16_bf16 v[176:191], v[212:215], v[196:199], v[176:191]\n\t" \
"ds_read_b64_tr_b16 v[252:253], v23 offset:6144\n\t" \
"s_add_u32 s59, s81, 24576\n\t" \
"buffer_store_dwordx4 v[32:35], v36, %0, s59 offen sc1\n\t" \
"ds_read_b64_tr_b16 v[254:255], v23 offset:6656\n\t" \
"v_mfma_f32_32x32x16_bf16 a[0:15], v[216:219], v[192:195], a[0:15]\n\t" \
"v_accvgpr_read_b32 v24, a104\n\t" \
"v_accvgpr_read_b32 v25, a105\n\t" \
"v_accvgpr_read_b32 v26, a106\n\t" \
"v_mfma_f32_32x32x16_bf16 a[16:31], v[216:219], v[196:199], a[16:31]\n\t" \
"v_accvgpr_read_b32 v27, a107\n\t" \
"v_accvgpr_read_b32 v28, a108\n\t" \
"v_accvgpr_read_b32 v29, a109\n\t" \
"v_accvgpr_read_b32 v30, a110\n\t" \
"s_waitcnt lgkmcnt(14)\n\t" \
"v_mfma_f32_32x32x16_bf16 a[32:47], v[192:195], v[220:223], a[32:47]\n\t" \
"v_accvgpr_read_b32 v31, a111\n\t" \
"v_cvt_pk_bf16_f32 v32, v24, v25\n\t" \
"v_cvt_pk_bf16_f32 v33, v26, v27\n\t" \
"v_mfma_f32_32x32x16_bf16 a[48:63], v[196:199], v[220:223], a[48:63]\n\t" \
"v_cvt_pk_bf16_f32 v34, v28, v29\n\t" \
"v_cvt_pk_bf16_f32 v35, v30, v31\n\t" \
"s_add_u32 s59, s81, 25600\n\t" \
"buffer_store_dwordx4 v[32:35], v36, %0, s59 offen sc1\n\t" \
"s_waitcnt lgkmcnt(11)\n\t" \
"v_mfma_f32_32x32x16_bf16 v[64:79], v[232:235], v[224:227], v[64:79]\n\t" \
"s_waitcnt lgkmcnt(0)\n\t" \
"s_waitcnt vmcnt(26)\n\t" \
"s_barrier\n\t" \
"v_mfma_f32_32x32x16_bf16 v[128:143], v[232:235], v[228:231], v[128:143]\n\t" \
"s_add_u32 s56, s75, 11\n\t" \
"s_sub_u32 s57, s56, 20\n\t" \
"s_cmp_ge_u32 s56, 20\n\t" \
"s_cselect_b32 s56, s57, s56\n\t" \
"s_lshl_b32 s56, s56, 13\n\t" \
"s_sub_u32 s57, s56, s83\n\t" \
"s_mov_b32 s83, s56\n\t" \
"v_add_u32 v18, s57, v18\n\t" \
"v_add_u32 v19, s57, v19\n\t" \
"v_add_u32 v20, s57, v20\n\t" \
"v_add_u32 v21, s57, v21\n\t" \
"v_add_u32 v39, 0xe000, v39\n\t" \
"v_mfma_f32_32x32x16_bf16 v[80:95], v[236:239], v[224:227], v[80:95]\n\t" \
"v_add_u32 v40, 0xe000, v40\n\t" \
"v_add_u32 v41, 0xe000, v41\n\t" \
"v_add_u32 v42, 0xe000, v42\n\t" \
"v_add_u32 v43, 0xe000, v43\n\t" \
"v_add_u32 v44, 0xe000, v44\n\t" \
"v_add_u32 v45, 0xe000, v45\n\t" \
"v_mfma_f32_32x32x16_bf16 v[144:159], v[236:239], v[228:231], v[144:159]\n\t" \
"v_add_u32 v46, 0xe000, v46\n\t" \
"v_add_u32 v47, 0xe000, v47\n\t" \
"v_add_u32 v48, 0xe000, v48\n\t" \
"v_add_u32 v49, 0xe000, v49\n\t" \
"v_add_u32 v50, 0xe000, v50\n\t" \
"v_add_u32 v51, 0xe000, v51\n\t" \
"v_mfma_f32_32x32x16_bf16 v[96:111], v[240:243], v[224:227], v[96:111]\n\t" \
"v_add_u32 v52, 0xe000, v52\n\t" \
"v_add_u32 v53, 0xe000, v53\n\t" \
"v_add_u32 v54, 0xe000, v54\n\t" \
"v_add_u32 v22, 0xe000, v22\n\t" \
"v_add_u32 v23, 0xe000, v23\n\t" \
"v_mfma_f32_32x32x16_bf16 v[160:175], v[240:243], v[228:231], v[160:175]\n\t" \
"ds_read_b64_tr_b16 v[200:201], v39 offset:0\n\t" \
"v_accvgpr_read_b32 v24, a112\n\t" \
"ds_read_b64_tr_b16 v[202:203], v40 offset:1024\n\t" \
"v_accvgpr_read_b32 v25, a113\n\t" \
"ds_read_b128 v[192:195], v18 offset:0\n\t" \
"v_accvgpr_read_b32 v26, a114\n\t" \
"v_mfma_f32_32x32x16_bf16 v[112:127], v[244:247], v[224:227], v[112:127]\n\t" \
"ds_read_b128 v[196:199], v18 offset:4096\n\t" \
"v_accvgpr_read_b32 v27, a115\n\t" \
"ds_read_b64_tr_b16 v[204:205], v41 offset:0\n\t" \
"v_accvgpr_read_b32 v28, a116\n\t" \
"ds_read_b64_tr_b16 v[206:207], v42 offset:1024\n\t" \
"v_accvgpr_read_b32 v29, a117\n\t" \
"v_mfma_f32_32x32x16_bf16 v[176:191], v[244:247], v[228:231], v[176:191]\n\t" \
"ds_read_b64_tr_b16 v[208:209], v43 offset:0\n\t" \
"v_accvgpr_read_b32 v30, a118\n\t" \
"ds_read_b64_tr_b16 v[210:211], v44 offset:1024\n\t" \
"v_accvgpr_read_b32 v31, a119\n\t" \
"ds_read_b64_tr_b16 v[212:213], v45 offset:0\n\t" \
"v_cvt_pk_bf16_f32 v32, v24, v25\n\t" \
"v_mfma_f32_32x32x16_bf16 a[0:15], v[248:251], v[224:227], a[0:15]\n\t" \
"ds_read_b64_tr_b16 v[214:215], v46 offset:1024\n\t" \
"v_cvt_pk_bf16_f32 v33, v26, v27\n\t" \
"ds_read_b64_tr_b16 v[216:217], v22 offset:0\n\t" \
"v_cvt_pk_bf16_f32 v34, v28, v29\n\t" \
"ds_read_b64_tr_b16 v[218:219], v22 offset:512\n\t" \
"v_mfma_f32_32x32x16_bf16 a[16:31], v[248:251], v[228:231], a[16:31]\n\t" \
"v_cvt_pk_bf16_f32 v35, v30, v31\n\t" \
"ds_read_b64_tr_b16 v[220:221], v23 offset:0\n\t" \
"s_add_u32 s59, s81, 26624\n\t" \
"buffer_store_dwordx4 v[32:35], v36, %0, s59 offen sc1\n\t" \
"ds_read_b64_tr_b16 v[222:223], v23 offset:512\n\t" \
"v_accvgpr_read_b32 v24, a120\n\t" \
"v_accvgpr_read_b32 v25, a121\n\t" \
"v_mfma_f32_32x32x16_bf16 a[32:47], v[224:227], v[252:255], a[32:47]\n\t" \
"v_accvgpr_read_b32 v26, a122\n\t" \
"v_accvgpr_read_b32 v27, a123\n\t" \
"v_accvgpr_read_b32 v28, a124\n\t" \
"v_accvgpr_read_b32 v29, a125\n\t" \
"v_accvgpr_read_b32 v30, a126\n\t" \
"v_accvgpr_read_b32 v31, a127\n\t" \
"v_mfma_f32_32x32x16_bf16 a[48:63], v[228:231], v[252:255], a[48:63]\n\t" \
"v_cvt_pk_bf16_f32 v32, v24, v25\n\t" \
"v_cvt_pk_bf16_f32 v33, v26, v27\n\t" \
"v_cvt_pk_bf16_f32 v34, v28, v29\n\t" \
"v_cvt_pk_bf16_f32 v35, v30, v31\n\t" \
"s_add_u32 s59, s81, 27648\n\t" \
"buffer_store_dwordx4 v[32:35], v36, %0, s59 offen sc1\n\t" \
"s_waitcnt lgkmcnt(11)\n\t" \
"v_mfma_f32_32x32x16_bf16 v[64:79], v[200:203], v[192:195], v[64:79]\n\t" \
"ds_read_b64_tr_b16 v[232:233], v39 offset:4096\n\t" \
"v_accvgpr_read_b32 v24, a128\n\t" \
"s_add_u32 m0, s70, 32768\n\t" \
"s_mov_b32 s54, 393216\n\t" \
"buffer_load_dwordx4 %22, %15, s54 offen lds\n\t" \
"s_waitcnt lgkmcnt(11)\n\t" \
"v_mfma_f32_32x32x16_bf16 v[128:143], v[200:203], v[196:199], v[128:143]\n\t" \
"v_accvgpr_read_b32 v25, a129\n\t" \
"ds_read_b64_tr_b16 v[234:235], v40 offset:5120\n\t" \
"v_accvgpr_read_b32 v26, a130\n\t" \
"s_waitcnt lgkmcnt(10)\n\t" \
"v_mfma_f32_32x32x16_bf16 v[80:95], v[204:207], v[192:195], v[80:95]\n\t" \
"s_add_u32 m0, s70, 33792\n\t" \
"s_mov_b32 s55, 393216\n\t" \
"buffer_load_dwordx4 %23, %15, s55 offen lds\n\t" \
"v_accvgpr_read_b32 v27, a131\n\t" \
"v_mfma_f32_32x32x16_bf16 v[144:159], v[204:207], v[196:199], v[144:159]\n\t" \
"ds_read_b128 v[224:227], v19 offset:0\n\t" \
"v_accvgpr_read_b32 v28, a132\n\t" \
"s_add_u32 m0, s70, 40960\n\t" \
"s_add_u32 s54, s71, 384\n\t" \
"buffer_load_dwordx4 %18, %13, s54 offen lds\n\t" \
"s_waitcnt lgkmcnt(9)\n\t" \
"v_mfma_f32_32x32x16_bf16 v[96:111], v[208:211], v[192:195], v[96:111]\n\t" \
"v_accvgpr_read_b32 v29, a133\n\t" \
"ds_read_b128 v[228:231], v19 offset:4096\n\t" \
"v_accvgpr_read_b32 v30, a134\n\t" \
"v_mfma_f32_32x32x16_bf16 v[160:175], v[208:211], v[196:199], v[160:175]\n\t" \
"s_add_u32 m0, s70, 41984\n\t" \
"s_add_u32 s55, s71, 384\n\t" \
"buffer_load_dwordx4 %19, %13, s55 offen lds\n\t" \
"v_accvgpr_read_b32 v31, a135\n\t" \
"s_waitcnt lgkmcnt(8)\n\t" \
"v_mfma_f32_32x32x16_bf16 v[112:127], v[212:215], v[192:195], v[112:127]\n\t" \
"ds_read_b64_tr_b16 v[236:237], v41 offset:4096\n\t" \
"v_cvt_pk_bf16_f32 v32, v24, v25\n\t" \
"s_add_u32 m0, s70, 49152\n\t" \
"s_add_u32 s54, s72, 384\n\t" \
"buffer_load_dwordx4 %18, %13, s54 offen lds\n\t" \
"v_mfma_f32_32x32x16_bf16 v[176:191], v[212:215], v[196:199], v[176:191]\n\t" \
"v_cvt_pk_bf16_f32 v33, v26, v27\n\t" \
"ds_read_b64_tr_b16 v[238:239], v42 offset:5120\n\t" \
"v_cvt_pk_bf16_f32 v34, v28, v29\n\t" \
"s_waitcnt lgkmcnt(8)\n\t" \
"v_mfma_f32_32x32x16_bf16 a[0:15], v[216:219], v[192:195], a[0:15]\n\t" \
"ds_read_b64_tr_b16 v[240:241], v43 offset:4096\n\t" \
"v_cvt_pk_bf16_f32 v35, v30, v31\n\t" \
"v_mfma_f32_32x32x16_bf16 a[16:31], v[216:219], v[196:199], a[16:31]\n\t" \
"ds_read_b64_tr_b16 v[242:243], v44 offset:5120\n\t" \
"s_add_u32 s59, s82, 0\n\t" \
"buffer_store_dwordx4 v[32:35], v36, %0, s59 offen sc1\n\t" \
"ds_read_b64_tr_b16 v[244:245], v45 offset:4096\n\t" \
"s_waitcnt lgkmcnt(9)\n\t" \
"v_mfma_f32_32x32x16_bf16 a[32:47], v[192:195], v[220:223], a[32:47]\n\t" \
"ds_read_b64_tr_b16 v[246:247], v46 offset:5120\n\t" \
"ds_read_b64_tr_b16 v[248:249], v22 offset:2048\n\t" \
"ds_read_b64_tr_b16 v[250:251], v22 offset:2560\n\t" \
"v_mfma_f32_32x32x16_bf16 a[48:63], v[196:199], v[220:223], a[48:63]\n\t" \
"ds_read_b64_tr_b16 v[252:253], v23 offset:2048\n\t" \
"ds_read_b64_tr_b16 v[254:255], v23 offset:2560\n\t" \
"s_waitcnt lgkmcnt(11)\n\t" \
"v_mfma_f32_32x32x16_bf16 v[64:79], v[232:235], v[224:227], v[64:79]\n\t" \
"ds_read_b64_tr_b16 v[200:201], v47 offset:0\n\t" \
"v_accvgpr_read_b32 v24, a136\n\t" \
"s_add_u32 m0, s70, 50176\n\t" \
"s_add_u32 s55, s72, 384\n\t" \
"buffer_load_dwordx4 %19, %13, s55 offen lds\n\t" \
"s_waitcnt lgkmcnt(11)\n\t" \
"v_mfma_f32_32x32x16_bf16 v[128:143], v[232:235], v[228:231], v[128:143]\n\t" \
"v_accvgpr_read_b32 v25, a137\n\t" \
"ds_read_b64_tr_b16 v[202:203], v48 offset:1024\n\t" \
"v_accvgpr_read_b32 v26, a138\n\t" \
"s_waitcnt lgkmcnt(10)\n\t" \
"v_mfma_f32_32x32x16_bf16 v[80:95], v[236:239], v[224:227], v[80:95]\n\t" \
"s_add_u32 m0, s70, 57344\n\t" \
"s_add_u32 s54, s73, 384\n\t" \
"buffer_load_dwordx4 %18, %13, s54 offen lds\n\t" \
"v_accvgpr_read_b32 v27, a139\n\t" \
"v_mfma_f32_32x32x16_bf16 v[144:159], v[236:239], v[228:231], v[144:159]\n\t" \
"ds_read_b128 v[192:195], v20 offset:0\n\t" \
"v_accvgpr_read_b32 v28, a140\n\t" \
"s_add_u32 m0, s70, 58368\n\t" \
"s_add_u32 s55, s73, 384\n\t" \
"buffer_load_dwordx4 %19, %13, s55 offen lds\n\t" \
"s_waitcnt lgkmcnt(9)\n\t" \
"v_mfma_f32_32x32x16_bf16 v[96:111], v[240:243], v[224:227], v[96:111]\n\t" \
"v_accvgpr_read_b32 v29, a141\n\t" \
"ds_read_b128 v[196:199], v20 offset:4096\n\t" \
"v_accvgpr_read_b32 v30, a142\n\t" \
"v_mfma_f32_32x32x16_bf16 v[160:175], v[240:243], v[228:231], v[160:175]\n\t" \
"s_add_u32 m0, s70, 65536\n\t" \
"s_add_u32 s54, s74, 384\n\t" \
"buffer_load_dwordx4 %18, %13, s54 offen lds\n\t" \
"v_accvgpr_read_b32 v31, a143\n\t" \
"s_waitcnt lgkmcnt(8)\n\t" \
"v_mfma_f32_32x32x16_bf16 v[112:127], v[244:247], v[224:227], v[112:127]\n\t" \
"ds_read_b64_tr_b16 v[204:205], v49 offset:0\n\t" \
"v_cvt_pk_bf16_f32 v32, v24, v25\n\t" \
"s_add_u32 m0, s70, 66560\n\t" \
"s_add_u32 s55, s74, 384\n\t" \
"buffer_load_dwordx4 %19, %13, s55 offen lds\n\t" \
"v_mfma_f32_32x32x16_bf16 v[176:191], v[244:247], v[228:231], v[176:191]\n\t" \
"v_cvt_pk_bf16_f32 v33, v26, v27\n\t" \
"ds_read_b64_tr_b16 v[206:207], v50 offset:1024\n\t" \
"v_cvt_pk_bf16_f32 v34, v28, v29\n\t" \
"s_waitcnt lgkmcnt(8)\n\t" \
"v_mfma_f32_32x32x16_bf16 a[0:15], v[248:251], v[224:227], a[0:15]\n\t" \
"ds_read_b64_tr_b16 v[208:209], v51 offset:0\n\t" \
"v_cvt_pk_bf16_f32 v35, v30, v31\n\t" \
"v_mfma_f32_32x32x16_bf16 a[16:31], v[248:251], v[228:231], a[16:31]\n\t" \
"ds_read_b64_tr_b16 v[210:211], v52 offset:1024\n\t" \
"s_add_u32 s59, s82, 4096\n\t" \
"buffer_store_dwordx4 v[32:35], v36, %0, s59 offen sc1\n\t" \
"ds_read_b64_tr_b16 v[212:213], v53 offset:0\n\t" \
"s_waitcnt lgkmcnt(9)\n\t" \
"v_mfma_f32_32x32x16_bf16 a[32:47], v[224:227], v[252:255], a[32:47]\n\t" \
"ds_read_b64_tr_b16 v[214:215], v54 offset:1024\n\t" \
"ds_read_b64_tr_b16 v[216:217], v22 offset:4096\n\t" \
"ds_read_b64_tr_b16 v[218:219], v22 offset:4608\n\t" \
"v_mfma_f32_32x32x16_bf16 a[48:63], v[228:231], v[252:255], a[48:63]\n\t" \
"ds_read_b64_tr_b16 v[220:221], v23 offset:4096\n\t" \
"ds_read_b64_tr_b16 v[222:223], v23 offset:4608\n\t" \
"s_waitcnt lgkmcnt(11)\n\t" \
"v_mfma_f32_32x32x16_bf16 v[64:79], v[200:203], v[192:195], v[64:79]\n\t" \
"ds_read_b64_tr_b16 v[232:233], v47 offset:4096\n\t" \
"v_accvgpr_read_b32 v24, a144\n\t" \
"s_add_u32 m0, s70, 73728\n\t" \
"s_mov_b32 s54, 1572864\n\t" \
"buffer_load_dwordx4 %20, %14, s54 offen lds\n\t" \
"s_waitcnt lgkmcnt(11)\n\t" \
"v_mfma_f32_32x32x16_bf16 v[128:143], v[200:203], v[196:199], v[128:143]\n\t" \
"v_accvgpr_read_b32 v25, a145\n\t" \
"ds_read_b64_tr_b16 v[234:235], v48 offset:5120\n\t" \
"v_accvgpr_read_b32 v26, a146\n\t" \
"s_waitcnt lgkmcnt(10)\n\t" \
"v_mfma_f32_32x32x16_bf16 v[80:95], v[204:207], v[192:195], v[80:95]\n\t" \
"s_add_u32 m0, s70, 74752\n\t" \
"s_mov_b32 s55, 1572864\n\t" \
"buffer_load_dwordx4 %21, %14, s55 offen lds\n\t" \
"v_accvgpr_read_b32 v27, a147\n\t" \
"v_mfma_f32_32x32x16_bf16 v[144:159], v[204:207], v[196:199], v[144:159]\n\t" \
"ds_read_b128 v[224:227], v21 offset:0\n\t" \
"v_accvgpr_read_b32 v28, a148\n\t" \
"s_add_u32 m0, s70, 81920\n\t" \
"s_mov_b32 s54, 1835008\n\t" \
"buffer_load_dwordx4 %20, %14, s54 offen lds\n\t" \
"s_waitcnt lgkmcnt(9)\n\t" \
"v_mfma_f32_32x32x16_bf16 v[96:111], v[208:211], v[192:195], v[96:111]\n\t" \
"v_accvgpr_read_b32 v29, a149\n\t" \
"ds_read_b128 v[228:231], v21 offset:4096\n\t" \
"v_mfma_f32_32x32x16_bf16 v[160:175], v[208:211], v[196:199], v[160:175]\n\t" \
"v_accvgpr_read_b32 v30, a150\n\t" \
"s_add_u32 m0, s70, 82944\n\t" \
"s_mov_b32 s55, 1835008\n\t" \
"buffer_load_dwordx4 %21, %14, s55 offen lds\n\t" \
"v_accvgpr_read_b32 v31, a151\n\t" \
"s_waitcnt lgkmcnt(8)\n\t" \
"v_mfma_f32_32x32x16_bf16 v[112:127], v[212:215], v[192:195], v[112:127]\n\t" \
"ds_read_b64_tr_b16 v[236:237], v49 offset:4096\n\t" \
"v_cvt_pk_bf16_f32 v32, v24, v25\n\t" \
"ds_read_b64_tr_b16 v[238:239], v50 offset:5120\n\t" \
"v_mfma_f32_32x32x16_bf16 v[176:191], v[212:215], v[196:199], v[176:191]\n\t" \
"v_cvt_pk_bf16_f32 v33, v26, v27\n\t" \
"ds_read_b64_tr_b16 v[240:241], v51 offset:4096\n\t" \
"s_waitcnt lgkmcnt(9)\n\t" \
"v_mfma_f32_32x32x16_bf16 a[0:15], v[216:219], v[192:195], a[0:15]\n\t" \
"v_cvt_pk_bf16_f32 v34, v28, v29\n\t" \
"ds_read_b64_tr_b16 v[242:243], v52 offset:5120\n\t" \
"v_cvt_pk_bf16_f32 v35, v30, v31\n\t" \
"v_mfma_f32_32x32x16_bf16 a[16:31], v[216:219], v[196:199], a[16:31]\n\t" \
"ds_read_b64_tr_b16 v[244:245], v53 offset:4096\n\t" \
"s_add_u32 s59, s82, 1024\n\t" \
"buffer_store_dwordx4 v[32:35], v36, %0, s59 offen sc1\n\t" \
"s_waitcnt lgkmcnt(9)\n\t" \
"v_mfma_f32_32x32x16_bf16 a[32:47], v[192:195], v[220:223], a[32:47]\n\t" \
"ds_read_b64_tr_b16 v[246:247], v54 offset:5120\n\t" \
"ds_read_b64_tr_b16 v[248:249], v22 offset:6144\n\t" \
"ds_read_b64_tr_b16 v[250:251], v22 offset:6656\n\t" \
"v_mfma_f32_32x32x16_bf16 a[48:63], v[196:199], v[220:223], a[48:63]\n\t" \
"ds_read_b64_tr_b16 v[252:253], v23 offset:6144\n\t" \
"ds_read_b64_tr_b16 v[254:255], v23 offset:6656\n\t" \
"s_waitcnt lgkmcnt(11)\n\t" \
"v_mfma_f32_32x32x16_bf16 v[64:79], v[232:235], v[224:227], v[64:79]\n\t" \
"s_waitcnt lgkmcnt(0)\n\t" \
"s_waitcnt vmcnt(15)\n\t" \
"s_barrier\n\t" \
"v_mfma_f32_32x32x16_bf16 v[128:143], v[232:235], v[228:231], v[128:143]\n\t" \
"s_add_u32 s56, s75, 18\n\t" \
"s_sub_u32 s57, s56, 20\n\t" \
"s_cmp_ge_u32 s56, 20\n\t" \
"s_cselect_b32 s56, s57, s56\n\t" \
"s_lshl_b32 s56, s56, 13\n\t" \
"s_sub_u32 s57, s56, s83\n\t" \
"s_mov_b32 s83, s56\n\t" \
"v_add_u32 v18, s57, v18\n\t" \
"v_add_u32 v19, s57, v19\n\t" \
"v_add_u32 v20, s57, v20\n\t" \
"v_add_u32 v21, s57, v21\n\t" \
"v_mfma_f32_32x32x16_bf16 v[80:95], v[236:239], v[224:227], v[80:95]\n\t" \
"v_add_u32 v39, 0xfffe6000, v39\n\t" \
"v_add_u32 v40, 0xfffe6000, v40\n\t" \
"v_add_u32 v41, 0xfffe6000, v41\n\t" \
"v_add_u32 v42, 0xfffe6000, v42\n\t" \
"v_add_u32 v43, 0xfffe6000, v43\n\t" \
"v_mfma_f32_32x32x16_bf16 v[144:159], v[236:239], v[228:231], v[144:159]\n\t" \
"v_add_u32 v44, 0xfffe6000, v44\n\t" \
"v_add_u32 v45, 0xfffe6000, v45\n\t" \
"v_add_u32 v46, 0xfffe6000, v46\n\t" \
"v_add_u32 v47, 0xfffe6000, v47\n\t" \
"v_mfma_f32_32x32x16_bf16 v[96:111], v[240:243], v[224:227], v[96:111]\n\t" \
"v_add_u32 v48, 0xfffe6000, v48\n\t" \
"v_add_u32 v49, 0xfffe6000, v49\n\t" \
"v_add_u32 v50, 0xfffe6000, v50\n\t" \
"v_add_u32 v51, 0xfffe6000, v51\n\t" \
"v_add_u32 v52, 0xfffe6000, v52\n\t" \
"v_mfma_f32_32x32x16_bf16 v[160:175], v[240:243], v[228:231], v[160:175]\n\t" \
"v_add_u32 v53, 0xfffe6000, v53\n\t" \
"v_add_u32 v54, 0xfffe6000, v54\n\t" \
"v_add_u32 v22, 0xfffe6000, v22\n\t" \
"v_add_u32 v23, 0xfffe6000, v23\n\t" \
"v_mfma_f32_32x32x16_bf16 v[112:127], v[244:247], v[224:227], v[112:127]\n\t" \
"ds_read_b64_tr_b16 v[200:201], v39 offset:0\n\t" \
"v_accvgpr_read_b32 v24, a152\n\t" \
"ds_read_b64_tr_b16 v[202:203], v40 offset:1024\n\t" \
"v_accvgpr_read_b32 v25, a153\n\t" \
"ds_read_b128 v[192:195], v18 offset:0\n\t" \
"v_mfma_f32_32x32x16_bf16 v[176:191], v[244:247], v[228:231], v[176:191]\n\t" \
"v_accvgpr_read_b32 v26, a154\n\t" \
"ds_read_b128 v[196:199], v18 offset:4096\n\t" \
"v_accvgpr_read_b32 v27, a155\n\t" \
"ds_read_b64_tr_b16 v[204:205], v41 offset:0\n\t" \
"v_mfma_f32_32x32x16_bf16 a[0:15], v[248:251], v[224:227], a[0:15]\n\t" \
"v_accvgpr_read_b32 v28, a156\n\t" \
"ds_read_b64_tr_b16 v[206:207], v42 offset:1024\n\t" \
"v_accvgpr_read_b32 v29, a157\n\t" \
"ds_read_b64_tr_b16 v[208:209], v43 offset:0\n\t" \
"v_accvgpr_read_b32 v30, a158\n\t" \
"v_mfma_f32_32x32x16_bf16 a[16:31], v[248:251], v[228:231], a[16:31]\n\t" \
"ds_read_b64_tr_b16 v[210:211], v44 offset:1024\n\t" \
"v_accvgpr_read_b32 v31, a159\n\t" \
"ds_read_b64_tr_b16 v[212:213], v45 offset:0\n\t" \
"v_cvt_pk_bf16_f32 v32, v24, v25\n\t" \
"v_mfma_f32_32x32x16_bf16 a[32:47], v[224:227], v[252:255], a[32:47]\n\t" \
"ds_read_b64_tr_b16 v[214:215], v46 offset:1024\n\t" \
"v_cvt_pk_bf16_f32 v33, v26, v27\n\t" \
"ds_read_b64_tr_b16 v[216:217], v22 offset:0\n\t" \
"v_cvt_pk_bf16_f32 v34, v28, v29\n\t" \
"ds_read_b64_tr_b16 v[218:219], v22 offset:512\n\t" \
"v_mfma_f32_32x32x16_bf16 a[48:63], v[228:231], v[252:255], a[48:63]\n\t" \
"v_cvt_pk_bf16_f32 v35, v30, v31\n\t" \
"ds_read_b64_tr_b16 v[220:221], v23 offset:0\n\t" \
"s_add_u32 s59, s82, 5120\n\t" \
"buffer_store_dwordx4 v[32:35], v36, %0, s59 offen sc1\n\t" \
"ds_read_b64_tr_b16 v[222:223], v23 offset:512\n\t" \
"s_waitcnt lgkmcnt(11)\n\t" \
"v_mfma_f32_32x32x16_bf16 v[64:79], v[200:203], v[192:195], v[64:79]\n\t" \
"ds_read_b64_tr_b16 v[232:233], v39 offset:4096\n\t" \
"v_accvgpr_read_b32 v24, a160\n\t" \
"s_add_u32 m0, s70, 90112\n\t" \
"s_mov_b32 s54, 589824\n\t" \
"buffer_load_dwordx4 %22, %15, s54 offen lds\n\t" \
"s_waitcnt lgkmcnt(11)\n\t" \
"v_mfma_f32_32x32x16_bf16 v[128:143], v[200:203], v[196:199], v[128:143]\n\t" \
"v_accvgpr_read_b32 v25, a161\n\t" \
"ds_read_b64_tr_b16 v[234:235], v40 offset:5120\n\t" \
"v_accvgpr_read_b32 v26, a162\n\t" \
"s_waitcnt lgkmcnt(10)\n\t" \
"v_mfma_f32_32x32x16_bf16 v[80:95], v[204:207], v[192:195], v[80:95]\n\t" \
"s_add_u32 m0, s70, 91136\n\t" \
"s_mov_b32 s55, 589824\n\t" \
"buffer_load_dwordx4 %23, %15, s55 offen lds\n\t" \
"v_accvgpr_read_b32 v27, a163\n\t" \
"v_mfma_f32_32x32x16_bf16 v[144:159], v[204:207], v[196:199], v[144:159]\n\t" \
"ds_read_b128 v[224:227], v19 offset:0\n\t" \
"v_accvgpr_read_b32 v28, a164\n\t" \
"s_add_u32 m0, s70, 98304\n\t" \
"s_add_u32 s54, s71, 512\n\t" \
"buffer_load_dwordx4 %18, %13, s54 offen lds\n\t" \
"s_waitcnt lgkmcnt(9)\n\t" \
"v_mfma_f32_32x32x16_bf16 v[96:111], v[208:211], v[192:195], v[96:111]\n\t" \
"v_accvgpr_read_b32 v29, a165\n\t" \
"ds_read_b128 v[228:231], v19 offset:4096\n\t" \
"v_accvgpr_read_b32 v30, a166\n\t" \
"v_mfma_f32_32x32x16_bf16 v[160:175], v[208:211], v[196:199], v[160:175]\n\t" \
"s_add_u32 m0, s70, 99328\n\t" \
"s_add_u32 s55, s71, 512\n\t" \
"buffer_load_dwordx4 %19, %13, s55 offen lds\n\t" \
"v_accvgpr_read_b32 v31, a167\n\t" \
"s_waitcnt lgkmcnt(8)\n\t" \
"v_mfma_f32_32x32x16_bf16 v[112:127], v[212:215], v[192:195], v[112:127]\n\t" \
"ds_read_b64_tr_b16 v[236:237], v41 offset:4096\n\t" \
"v_cvt_pk_bf16_f32 v32, v24, v25\n\t" \
"s_add_u32 m0, s70, 106496\n\t" \
"s_add_u32 s54, s72, 512\n\t" \
"buffer_load_dwordx4 %18, %13, s54 offen lds\n\t" \
"v_mfma_f32_32x32x16_bf16 v[176:191], v[212:215], v[196:199], v[176:191]\n\t" \
"v_cvt_pk_bf16_f32 v33, v26, v27\n\t" \
"ds_read_b64_tr_b16 v[238:239], v42 offset:5120\n\t" \
"v_cvt_pk_bf16_f32 v34, v28, v29\n\t" \
"s_waitcnt lgkmcnt(8)\n\t" \
"v_mfma_f32_32x32x16_bf16 a[0:15], v[216:219], v[192:195], a[0:15]\n\t" \
"ds_read_b64_tr_b16 v[240:241], v43 offset:4096\n\t" \
"v_cvt_pk_bf16_f32 v35, v30, v31\n\t" \
"v_mfma_f32_32x32x16_bf16 a[16:31], v[216:219], v[196:199], a[16:31]\n\t" \
"ds_read_b64_tr_b16 v[242:243], v44 offset:5120\n\t" \
"s_add_u32 s59, s82, 8192\n\t" \
"buffer_store_dwordx4 v[32:35], v36, %0, s59 offen sc1\n\t" \
"ds_read_b64_tr_b16 v[244:245], v45 offset:4096\n\t" \
"s_waitcnt lgkmcnt(9)\n\t" \
"v_mfma_f32_32x32x16_bf16 a[32:47], v[192:195], v[220:223], a[32:47]\n\t" \
"ds_read_b64_tr_b16 v[246:247], v46 offset:5120\n\t" \
"ds_read_b64_tr_b16 v[248:249], v22 offset:2048\n\t" \
"ds_read_b64_tr_b16 v[250:251], v22 offset:2560\n\t" \
"v_mfma_f32_32x32x16_bf16 a[48:63], v[196:199], v[220:223], a[48:63]\n\t" \
"ds_read_b64_tr_b16 v[252:253], v23 offset:2048\n\t" \
"ds_read_b64_tr_b16 v[254:255], v23 offset:2560\n\t" \
"s_waitcnt lgkmcnt(11)\n\t" \
"v_mfma_f32_32x32x16_bf16 v[64:79], v[232:235], v[224:227], v[64:79]\n\t" \
"ds_read_b64_tr_b16 v[200:201], v47 offset:0\n\t" \
"v_accvgpr_read_b32 v24, a168\n\t" \
"s_add_u32 m0, s70, 107520\n\t" \
"s_add_u32 s55, s72, 512\n\t" \
"buffer_load_dwordx4 %19, %13, s55 offen lds\n\t" \
"s_waitcnt lgkmcnt(11)\n\t" \
"v_mfma_f32_32x32x16_bf16 v[128:143], v[232:235], v[228:231], v[128:143]\n\t" \
"v_accvgpr_read_b32 v25, a169\n\t" \
"ds_read_b64_tr_b16 v[202:203], v48 offset:1024\n\t" \
"v_accvgpr_read_b32 v26, a170\n\t" \
"s_waitcnt lgkmcnt(10)\n\t" \
"v_mfma_f32_32x32x16_bf16 v[80:95], v[236:239], v[224:227], v[80:95]\n\t" \
"s_add_u32 m0, s70, 114688\n\t" \
"s_add_u32 s54, s73, 512\n\t" \
"buffer_load_dwordx4 %18, %13, s54 offen lds\n\t" \
"v_accvgpr_read_b32 v27, a171\n\t" \
"v_mfma_f32_32x32x16_bf16 v[144:159], v[236:239], v[228:231], v[144:159]\n\t" \
"ds_read_b128 v[192:195], v20 offset:0\n\t" \
"v_accvgpr_read_b32 v28, a172\n\t" \
"s_add_u32 m0, s70, 115712\n\t" \
"s_add_u32 s55, s73, 512\n\t" \
"buffer_load_dwordx4 %19, %13, s55 offen lds\n\t" \
"s_waitcnt lgkmcnt(9)\n\t" \
"v_mfma_f32_32x32x16_bf16 v[96:111], v[240:243], v[224:227], v[96:111]\n\t" \
"v_accvgpr_read_b32 v29, a173\n\t" \
"ds_read_b128 v[196:199], v20 offset:4096\n\t" \
"v_accvgpr_read_b32 v30, a174\n\t" \
"v_mfma_f32_32x32x16_bf16 v[160:175], v[240:243], v[228:231], v[160:175]\n\t" \
"s_add_u32 m0, s70, 122880\n\t" \
"s_add_u32 s54, s74, 512\n\t" \
"buffer_load_dwordx4 %18, %13, s54 offen lds\n\t" \
"v_accvgpr_read_b32 v31, a175\n\t" \
"s_waitcnt lgkmcnt(8)\n\t" \
"v_mfma_f32_32x32x16_bf16 v[112:127], v[244:247], v[224:227], v[112:127]\n\t" \
"ds_read_b64_tr_b16 v[204:205], v49 offset:0\n\t" \
"v_cvt_pk_bf16_f32 v32, v24, v25\n\t" \
"s_add_u32 m0, s70, 123904\n\t" \
"s_add_u32 s55, s74, 512\n\t" \
"buffer_load_dwordx4 %19, %13, s55 offen lds\n\t" \
"v_mfma_f32_32x32x16_bf16 v[176:191], v[244:247], v[228:231], v[176:191]\n\t" \
"v_cvt_pk_bf16_f32 v33, v26, v27\n\t" \
"ds_read_b64_tr_b16 v[206:207], v50 offset:1024\n\t" \
"v_cvt_pk_bf16_f32 v34, v28, v29\n\t" \
"s_waitcnt lgkmcnt(8)\n\t" \
"v_mfma_f32_32x32x16_bf16 a[0:15], v[248:251], v[224:227], a[0:15]\n\t" \
"ds_read_b64_tr_b16 v[208:209], v51 offset:0\n\t" \
"v_cvt_pk_bf16_f32 v35, v30, v31\n\t" \
"v_mfma_f32_32x32x16_bf16 a[16:31], v[248:251], v[228:231], a[16:31]\n\t" \
"ds_read_b64_tr_b16 v[210:211], v52 offset:1024\n\t" \
"s_add_u32 s59, s82, 12288\n\t" \
"buffer_store_dwordx4 v[32:35], v36, %0, s59 offen sc1\n\t" \
"ds_read_b64_tr_b16 v[212:213], v53 offset:0\n\t" \
"s_waitcnt lgkmcnt(9)\n\t" \
"v_mfma_f32_32x32x16_bf16 a[32:47], v[224:227], v[252:255], a[32:47]\n\t" \
"ds_read_b64_tr_b16 v[214:215], v54 offset:1024\n\t" \
"ds_read_b64_tr_b16 v[216:217], v22 offset:4096\n\t" \
"ds_read_b64_tr_b16 v[218:219], v22 offset:4608\n\t" \
"v_mfma_f32_32x32x16_bf16 a[48:63], v[228:231], v[252:255], a[48:63]\n\t" \
"ds_read_b64_tr_b16 v[220:221], v23 offset:4096\n\t" \
"ds_read_b64_tr_b16 v[222:223], v23 offset:4608\n\t" \
"s_waitcnt lgkmcnt(11)\n\t" \
"v_mfma_f32_32x32x16_bf16 v[64:79], v[200:203], v[192:195], v[64:79]\n\t" \
"ds_read_b64_tr_b16 v[232:233], v47 offset:4096\n\t" \
"v_accvgpr_read_b32 v24, a176\n\t" \
"s_add_u32 m0, s70, 131072\n\t" \
"s_mov_b32 s54, 2097152\n\t" \
"buffer_load_dwordx4 %20, %14, s54 offen lds\n\t" \
"s_waitcnt lgkmcnt(11)\n\t" \
"v_mfma_f32_32x32x16_bf16 v[128:143], v[200:203], v[196:199], v[128:143]\n\t" \
"v_accvgpr_read_b32 v25, a177\n\t" \
"ds_read_b64_tr_b16 v[234:235], v48 offset:5120\n\t" \
"v_accvgpr_read_b32 v26, a178\n\t" \
"s_waitcnt lgkmcnt(10)\n\t" \
"v_mfma_f32_32x32x16_bf16 v[80:95], v[204:207], v[192:195], v[80:95]\n\t" \
"s_add_u32 m0, s70, 132096\n\t" \
"s_mov_b32 s55, 2097152\n\t" \
"buffer_load_dwordx4 %21, %14, s55 offen lds\n\t" \
"v_accvgpr_read_b32 v27, a179\n\t" \
"v_mfma_f32_32x32x16_bf16 v[144:159], v[204:207], v[196:199], v[144:159]\n\t" \
"ds_read_b128 v[224:227], v21 offset:0\n\t" \
"v_accvgpr_read_b32 v28, a180\n\t" \
"s_add_u32 m0, s70, 139264\n\t" \
"s_mov_b32 s54, 2359296\n\t" \
"buffer_load_dwordx4 %20, %14, s54 offen lds\n\t" \
"s_waitcnt lgkmcnt(9)\n\t" \
"v_mfma_f32_32x32x16_bf16 v[96:111], v[208:211], v[192:195], v[96:111]\n\t" \
"v_accvgpr_read_b32 v29, a181\n\t" \
"ds_read_b128 v[228:231], v21 offset:4096\n\t" \
"v_mfma_f32_32x32x16_bf16 v[160:175], v[208:211], v[196:199], v[160:175]\n\t" \
"v_accvgpr_read_b32 v30, a182\n\t" \
"s_add_u32 m0, s70, 140288\n\t" \
"s_mov_b32 s55, 2359296\n\t" \
"buffer_load_dwordx4 %21, %14, s55 offen lds\n\t" \
"v_accvgpr_read_b32 v31, a183\n\t" \
"s_waitcnt lgkmcnt(8)\n\t" \
"v_mfma_f32_32x32x16_bf16 v[112:127], v[212:215], v[192:195], v[112:127]\n\t" \
"ds_read_b64_tr_b16 v[236:237], v49 offset:4096\n\t" \
"v_cvt_pk_bf16_f32 v32, v24, v25\n\t" \
"ds_read_b64_tr_b16 v[238:239], v50 offset:5120\n\t" \
"v_mfma_f32_32x32x16_bf16 v[176:191], v[212:215], v[196:199], v[176:191]\n\t" \
"v_cvt_pk_bf16_f32 v33, v26, v27\n\t" \
"ds_read_b64_tr_b16 v[240:241], v51 offset:4096\n\t" \
"s_waitcnt lgkmcnt(9)\n\t" \
"v_mfma_f32_32x32x16_bf16 a[0:15], v[216:219], v[192:195], a[0:15]\n\t" \
"v_cvt_pk_bf16_f32 v34, v28, v29\n\t" \
"ds_read_b64_tr_b16 v[242:243], v52 offset:5120\n\t" \
"v_cvt_pk_bf16_f32 v35, v30, v31\n\t" \
"v_mfma_f32_32x32x16_bf16 a[16:31], v[216:219], v[196:199], a[16:31]\n\t" \
"ds_read_b64_tr_b16 v[244:245], v53 offset:4096\n\t" \
"s_add_u32 s59, s82, 9216\n\t" \
"buffer_store_dwordx4 v[32:35], v36, %0, s59 offen sc1\n\t" \
"s_waitcnt lgkmcnt(9)\n\t" \
"v_mfma_f32_32x32x16_bf16 a[32:47], v[192:195], v[220:223], a[32:47]\n\t" \
"ds_read_b64_tr_b16 v[246:247], v54 offset:5120\n\t" \
"ds_read_b64_tr_b16 v[248:249], v22 offset:6144\n\t" \
"ds_read_b64_tr_b16 v[250:251], v22 offset:6656\n\t" \
"v_mfma_f32_32x32x16_bf16 a[48:63], v[196:199], v[220:223], a[48:63]\n\t" \
"ds_read_b64_tr_b16 v[252:253], v23 offset:6144\n\t" \
"ds_read_b64_tr_b16 v[254:255], v23 offset:6656\n\t" \
"s_waitcnt lgkmcnt(11)\n\t" \
"v_mfma_f32_32x32x16_bf16 v[64:79], v[232:235], v[224:227], v[64:79]\n\t" \
"s_waitcnt lgkmcnt(0)\n\t" \
"s_waitcnt vmcnt(15)\n\t" \
"s_barrier\n\t" \
"v_mfma_f32_32x32x16_bf16 v[128:143], v[232:235], v[228:231], v[128:143]\n\t" \
"s_add_u32 s56, s75, 5\n\t" \
"s_sub_u32 s57, s56, 20\n\t" \
"s_cmp_ge_u32 s56, 20\n\t" \
"s_cselect_b32 s56, s57, s56\n\t" \
"s_lshl_b32 s56, s56, 13\n\t" \
"s_sub_u32 s57, s56, s83\n\t" \
"s_mov_b32 s83, s56\n\t" \
"v_add_u32 v18, s57, v18\n\t" \
"v_add_u32 v19, s57, v19\n\t" \
"v_add_u32 v20, s57, v20\n\t" \
"v_add_u32 v21, s57, v21\n\t" \
"v_mfma_f32_32x32x16_bf16 v[80:95], v[236:239], v[224:227], v[80:95]\n\t" \
"v_add_u32 v39, 0xe000, v39\n\t" \
"v_add_u32 v40, 0xe000, v40\n\t" \
"v_add_u32 v41, 0xe000, v41\n\t" \
"v_add_u32 v42, 0xe000, v42\n\t" \
"v_add_u32 v43, 0xe000, v43\n\t" \
"v_mfma_f32_32x32x16_bf16 v[144:159], v[236:239], v[228:231], v[144:159]\n\t" \
"v_add_u32 v44, 0xe000, v44\n\t" \
"v_add_u32 v45, 0xe000, v45\n\t" \
"v_add_u32 v46, 0xe000, v46\n\t" \
"v_add_u32 v47, 0xe000, v47\n\t" \
"v_mfma_f32_32x32x16_bf16 v[96:111], v[240:243], v[224:227], v[96:111]\n\t" \
"v_add_u32 v48, 0xe000, v48\n\t" \
"v_add_u32 v49, 0xe000, v49\n\t" \
"v_add_u32 v50, 0xe000, v50\n\t" \
"v_add_u32 v51, 0xe000, v51\n\t" \
"v_add_u32 v52, 0xe000, v52\n\t" \
"v_mfma_f32_32x32x16_bf16 v[160:175], v[240:243], v[228:231], v[160:175]\n\t" \
"v_add_u32 v53, 0xe000, v53\n\t" \
"v_add_u32 v54, 0xe000, v54\n\t" \
"v_add_u32 v22, 0xe000, v22\n\t" \
"v_add_u32 v23, 0xe000, v23\n\t" \
"v_mfma_f32_32x32x16_bf16 v[112:127], v[244:247], v[224:227], v[112:127]\n\t" \
"ds_read_b64_tr_b16 v[200:201], v39 offset:0\n\t" \
"v_accvgpr_read_b32 v24, a184\n\t" \
"ds_read_b64_tr_b16 v[202:203], v40 offset:1024\n\t" \
"v_accvgpr_read_b32 v25, a185\n\t" \
"ds_read_b128 v[192:195], v18 offset:0\n\t" \
"v_mfma_f32_32x32x16_bf16 v[176:191], v[244:247], v[228:231], v[176:191]\n\t" \
"v_accvgpr_read_b32 v26, a186\n\t" \
"ds_read_b128 v[196:199], v18 offset:4096\n\t" \
"v_accvgpr_read_b32 v27, a187\n\t" \
"ds_read_b64_tr_b16 v[204:205], v41 offset:0\n\t" \
"v_mfma_f32_32x32x16_bf16 a[0:15], v[248:251], v[224:227], a[0:15]\n\t" \
"v_accvgpr_read_b32 v28, a188\n\t" \
"ds_read_b64_tr_b16 v[206:207], v42 offset:1024\n\t" \
"v_accvgpr_read_b32 v29, a189\n\t" \
"ds_read_b64_tr_b16 v[208:209], v43 offset:0\n\t" \
"v_accvgpr_read_b32 v30, a190\n\t" \
"v_mfma_f32_32x32x16_bf16 a[16:31], v[248:251], v[228:231], a[16:31]\n\t" \
"ds_read_b64_tr_b16 v[210:211], v44 offset:1024\n\t" \
"v_accvgpr_read_b32 v31, a191\n\t" \
"ds_read_b64_tr_b16 v[212:213], v45 offset:0\n\t" \
"v_cvt_pk_bf16_f32 v32, v24, v25\n\t" \
"v_mfma_f32_32x32x16_bf16 a[32:47], v[224:227], v[252:255], a[32:47]\n\t" \
"ds_read_b64_tr_b16 v[214:215], v46 offset:1024\n\t" \
"v_cvt_pk_bf16_f32 v33, v26, v27\n\t" \
"ds_read_b64_tr_b16 v[216:217], v22 offset:0\n\t" \
"v_cvt_pk_bf16_f32 v34, v28, v29\n\t" \
"ds_read_b64_tr_b16 v[218:219], v22 offset:512\n\t" \
"v_mfma_f32_32x32x16_bf16 a[48:63], v[228:231], v[252:255], a[48:63]\n\t" \
"v_cvt_pk_bf16_f32 v35, v30, v31\n\t" \
"ds_read_b64_tr_b16 v[220:221], v23 offset:0\n\t" \
"s_add_u32 s59, s82, 13312\n\t" \
"buffer_store_dwordx4 v[32:35], v36, %0, s59 offen sc1\n\t" \
"ds_read_b64_tr_b16 v[222:223], v23 offset:512\n\t" \
"s_waitcnt lgkmcnt(11)\n\t" \
"v_mfma_f32_32x32x16_bf16 v[64:79], v[200:203], v[192:195], v[64:79]\n\t" \
"ds_read_b64_tr_b16 v[232:233], v39 offset:4096\n\t" \
"v_accvgpr_read_b32 v24, a192\n\t" \
"s_add_u32 m0, s70, 147456\n\t" \
"s_mov_b32 s54, 786432\n\t" \
"buffer_load_dwordx4 %22, %15, s54 offen lds\n\t" \
"s_waitcnt lgkmcnt(11)\n\t" \
"v_mfma_f32_32x32x16_bf16 v[128:143], v[200:203], v[196:199], v[128:143]\n\t" \
"v_accvgpr_read_b32 v25, a193\n\t" \
"ds_read_b64_tr_b16 v[234:235], v40 offset:5120\n\t" \
"v_accvgpr_read_b32 v26, a194\n\t" \
"s_waitcnt lgkmcnt(10)\n\t" \
"v_mfma_f32_32x32x16_bf16 v[80:95], v[204:207], v[192:195], v[80:95]\n\t" \
"s_add_u32 m0, s70, 148480\n\t" \
"s_mov_b32 s55, 786432\n\t" \
"buffer_load_dwordx4 %23, %15, s55 offen lds\n\t" \
"v_accvgpr_read_b32 v27, a195\n\t" \
"v_mfma_f32_32x32x16_bf16 v[144:159], v[204:207], v[196:199], v[144:159]\n\t" \
"ds_read_b128 v[224:227], v19 offset:0\n\t" \
"v_accvgpr_read_b32 v28, a196\n\t" \
"s_add_u32 m0, s70, 155648\n\t" \
"s_add_u32 s54, s71, 640\n\t" \
"buffer_load_dwordx4 %18, %13, s54 offen lds\n\t" \
"s_waitcnt lgkmcnt(9)\n\t" \
"v_mfma_f32_32x32x16_bf16 v[96:111], v[208:211], v[192:195], v[96:111]\n\t" \
"v_accvgpr_read_b32 v29, a197\n\t" \
"ds_read_b128 v[228:231], v19 offset:4096\n\t" \
"v_accvgpr_read_b32 v30, a198\n\t" \
"v_mfma_f32_32x32x16_bf16 v[160:175], v[208:211], v[196:199], v[160:175]\n\t" \
"s_add_u32 m0, s70, 156672\n\t" \
"s_add_u32 s55, s71, 640\n\t" \
"buffer_load_dwordx4 %19, %13, s55 offen lds\n\t" \
"v_accvgpr_read_b32 v31, a199\n\t" \
"s_waitcnt lgkmcnt(8)\n\t" \
"v_mfma_f32_32x32x16_bf16 v[112:127], v[212:215], v[192:195], v[112:127]\n\t" \
"ds_read_b64_tr_b16 v[236:237], v41 offset:4096\n\t" \
"v_cvt_pk_bf16_f32 v32, v24, v25\n\t" \
"s_add_u32 m0, s70, 0\n\t" \
"s_add_u32 s54, s72, 640\n\t" \
"buffer_load_dwordx4 %18, %13, s54 offen lds\n\t" \
"v_mfma_f32_32x32x16_bf16 v[176:191], v[212:215], v[196:199], v[176:191]\n\t" \
"v_cvt_pk_bf16_f32 v33, v26, v27\n\t" \
"ds_read_b64_tr_b16 v[238:239], v42 offset:5120\n\t" \
"v_cvt_pk_bf16_f32 v34, v28, v29\n\t" \
"s_waitcnt lgkmcnt(8)\n\t" \
"v_mfma_f32_32x32x16_bf16 a[0:15], v[216:219], v[192:195], a[0:15]\n\t" \
"ds_read_b64_tr_b16 v[240:241], v43 offset:4096\n\t" \
"v_cvt_pk_bf16_f32 v35, v30, v31\n\t" \
"v_mfma_f32_32x32x16_bf16 a[16:31], v[216:219], v[196:199], a[16:31]\n\t" \
"ds_read_b64_tr_b16 v[242:243], v44 offset:5120\n\t" \
"s_add_u32 s59, s82, 16384\n\t" \
"buffer_store_dwordx4 v[32:35], v36, %0, s59 offen sc1\n\t" \
"ds_read_b64_tr_b16 v[244:245], v45 offset:4096\n\t" \
"s_waitcnt lgkmcnt(9)\n\t" \
"v_mfma_f32_32x32x16_bf16 a[32:47], v[192:195], v[220:223], a[32:47]\n\t" \
"ds_read_b64_tr_b16 v[246:247], v46 offset:5120\n\t" \
"ds_read_b64_tr_b16 v[248:249], v22 offset:2048\n\t" \
"ds_read_b64_tr_b16 v[250:251], v22 offset:2560\n\t" \
"v_mfma_f32_32x32x16_bf16 a[48:63], v[196:199], v[220:223], a[48:63]\n\t" \
"ds_read_b64_tr_b16 v[252:253], v23 offset:2048\n\t" \
"ds_read_b64_tr_b16 v[254:255], v23 offset:2560\n\t" \
"s_waitcnt lgkmcnt(11)\n\t" \
"v_mfma_f32_32x32x16_bf16 v[64:79], v[232:235], v[224:227], v[64:79]\n\t" \
"ds_read_b64_tr_b16 v[200:201], v47 offset:0\n\t" \
"v_accvgpr_read_b32 v24, a200\n\t" \
"s_add_u32 m0, s70, 1024\n\t" \
"s_add_u32 s55, s72, 640\n\t" \
"buffer_load_dwordx4 %19, %13, s55 offen lds\n\t" \
"s_waitcnt lgkmcnt(11)\n\t" \
"v_mfma_f32_32x32x16_bf16 v[128:143], v[232:235], v[228:231], v[128:143]\n\t" \
"v_accvgpr_read_b32 v25, a201\n\t" \
"ds_read_b64_tr_b16 v[202:203], v48 offset:1024\n\t" \
"v_accvgpr_read_b32 v26, a202\n\t" \
"s_waitcnt lgkmcnt(10)\n\t" \
"v_mfma_f32_32x32x16_bf16 v[80:95], v[236:239], v[224:227], v[80:95]\n\t" \
"s_add_u32 m0, s70, 8192\n\t" \
"s_add_u32 s54, s73, 640\n\t" \
"buffer_load_dwordx4 %18, %13, s54 offen lds\n\t" \
"v_accvgpr_read_b32 v27, a203\n\t" \
"v_mfma_f32_32x32x16_bf16 v[144:159], v[236:239], v[228:231], v[144:159]\n\t" \
"ds_read_b128 v[192:195], v20 offset:0\n\t" \
"v_accvgpr_read_b32 v28, a204\n\t" \
"s_add_u32 m0, s70, 9216\n\t" \
"s_add_u32 s55, s73, 640\n\t" \
"buffer_load_dwordx4 %19, %13, s55 offen lds\n\t" \
"s_waitcnt lgkmcnt(9)\n\t" \
"v_mfma_f32_32x32x16_bf16 v[96:111], v[240:243], v[224:227], v[96:111]\n\t" \
"v_accvgpr_read_b32 v29, a205\n\t" \
"ds_read_b128 v[196:199], v20 offset:4096\n\t" \
"v_accvgpr_read_b32 v30, a206\n\t" \
"v_mfma_f32_32x32x16_bf16 v[160:175], v[240:243], v[228:231], v[160:175]\n\t" \
"s_add_u32 m0, s70, 16384\n\t" \
"s_add_u32 s54, s74, 640\n\t" \
"buffer_load_dwordx4 %18, %13, s54 offen lds\n\t" \
"v_accvgpr_read_b32 v31, a207\n\t" \
"s_waitcnt lgkmcnt(8)\n\t" \
"v_mfma_f32_32x32x16_bf16 v[112:127], v[244:247], v[224:227], v[112:127]\n\t" \
"ds_read_b64_tr_b16 v[204:205], v49 offset:0\n\t" \
"v_cvt_pk_bf16_f32 v32, v24, v25\n\t" \
"s_add_u32 m0, s70, 17408\n\t" \
"s_add_u32 s55, s74, 640\n\t" \
"buffer_load_dwordx4 %19, %13, s55 offen lds\n\t" \
"v_mfma_f32_32x32x16_bf16 v[176:191], v[244:247], v[228:231], v[176:191]\n\t" \
"v_cvt_pk_bf16_f32 v33, v26, v27\n\t" \
"ds_read_b64_tr_b16 v[206:207], v50 offset:1024\n\t" \
"v_cvt_pk_bf16_f32 v34, v28, v29\n\t" \
"s_waitcnt lgkmcnt(8)\n\t" \
"v_mfma_f32_32x32x16_bf16 a[0:15], v[248:251], v[224:227], a[0:15]\n\t" \
"ds_read_b64_tr_b16 v[208:209], v51 offset:0\n\t" \
"v_cvt_pk_bf16_f32 v35, v30, v31\n\t" \
"v_mfma_f32_32x32x16_bf16 a[16:31], v[248:251], v[228:231], a[16:31]\n\t" \
"ds_read_b64_tr_b16 v[210:211], v52 offset:1024\n\t" \
"s_add_u32 s59, s82, 20480\n\t" \
"buffer_store_dwordx4 v[32:35], v36, %0, s59 offen sc1\n\t" \
"ds_read_b64_tr_b16 v[212:213], v53 offset:0\n\t" \
"s_waitcnt lgkmcnt(9)\n\t" \
"v_mfma_f32_32x32x16_bf16 a[32:47], v[224:227], v[252:255], a[32:47]\n\t" \
"ds_read_b64_tr_b16 v[214:215], v54 offset:1024\n\t" \
"ds_read_b64_tr_b16 v[216:217], v22 offset:4096\n\t" \
"ds_read_b64_tr_b16 v[218:219], v22 offset:4608\n\t" \
"v_mfma_f32_32x32x16_bf16 a[48:63], v[228:231], v[252:255], a[48:63]\n\t" \
"ds_read_b64_tr_b16 v[220:221], v23 offset:4096\n\t" \
"ds_read_b64_tr_b16 v[222:223], v23 offset:4608\n\t" \
"s_waitcnt lgkmcnt(11)\n\t" \
"v_mfma_f32_32x32x16_bf16 v[64:79], v[200:203], v[192:195], v[64:79]\n\t" \
"ds_read_b64_tr_b16 v[232:233], v47 offset:4096\n\t" \
"v_accvgpr_read_b32 v24, a208\n\t" \
"s_add_u32 m0, s70, 24576\n\t" \
"s_mov_b32 s54, 2621440\n\t" \
"buffer_load_dwordx4 %20, %14, s54 offen lds\n\t" \
"s_waitcnt lgkmcnt(11)\n\t" \
"v_mfma_f32_32x32x16_bf16 v[128:143], v[200:203], v[196:199], v[128:143]\n\t" \
"v_accvgpr_read_b32 v25, a209\n\t" \
"ds_read_b64_tr_b16 v[234:235], v48 offset:5120\n\t" \
"v_accvgpr_read_b32 v26, a210\n\t" \
"s_waitcnt lgkmcnt(10)\n\t" \
"v_mfma_f32_32x32x16_bf16 v[80:95], v[204:207], v[192:195], v[80:95]\n\t" \
"s_add_u32 m0, s70, 25600\n\t" \
"s_mov_b32 s55, 2621440\n\t" \
"buffer_load_dwordx4 %21, %14, s55 offen lds\n\t" \
"v_accvgpr_read_b32 v27, a211\n\t" \
"v_mfma_f32_32x32x16_bf16 v[144:159], v[204:207], v[196:199], v[144:159]\n\t" \
"ds_read_b128 v[224:227], v21 offset:0\n\t" \
"v_accvgpr_read_b32 v28, a212\n\t" \
"s_add_u32 m0, s70, 32768\n\t" \
"s_mov_b32 s54, 2883584\n\t" \
"buffer_load_dwordx4 %20, %14, s54 offen lds\n\t" \
"s_waitcnt lgkmcnt(9)\n\t" \
"v_mfma_f32_32x32x16_bf16 v[96:111], v[208:211], v[192:195], v[96:111]\n\t" \
"v_accvgpr_read_b32 v29, a213\n\t" \
"ds_read_b128 v[228:231], v21 offset:4096\n\t" \
"v_mfma_f32_32x32x16_bf16 v[160:175], v[208:211], v[196:199], v[160:175]\n\t" \
"v_accvgpr_read_b32 v30, a214\n\t" \
"s_add_u32 m0, s70, 33792\n\t" \
"s_mov_b32 s55, 2883584\n\t" \
"buffer_load_dwordx4 %21, %14, s55 offen lds\n\t" \
"v_accvgpr_read_b32 v31, a215\n\t" \
"s_waitcnt lgkmcnt(8)\n\t" \
"v_mfma_f32_32x32x16_bf16 v[112:127], v[212:215], v[192:195], v[112:127]\n\t" \
"ds_read_b64_tr_b16 v[236:237], v49 offset:4096\n\t" \
"v_cvt_pk_bf16_f32 v32, v24, v25\n\t" \
"ds_read_b64_tr_b16 v[238:239], v50 offset:5120\n\t" \
"v_mfma_f32_32x32x16_bf16 v[176:191], v[212:215], v[196:199], v[176:191]\n\t" \
"v_cvt_pk_bf16_f32 v33, v26, v27\n\t" \
"ds_read_b64_tr_b16 v[240:241], v51 offset:4096\n\t" \
"s_waitcnt lgkmcnt(9)\n\t" \
"v_mfma_f32_32x32x16_bf16 a[0:15], v[216:219], v[192:195], a[0:15]\n\t" \
"v_cvt_pk_bf16_f32 v34, v28, v29\n\t" \
"ds_read_b64_tr_b16 v[242:243], v52 offset:5120\n\t" \
"v_cvt_pk_bf16_f32 v35, v30, v31\n\t" \
"v_mfma_f32_32x32x16_bf16 a[16:31], v[216:219], v[196:199], a[16:31]\n\t" \
"ds_read_b64_tr_b16 v[244:245], v53 offset:4096\n\t" \
"s_add_u32 s59, s82, 17408\n\t" \
"buffer_store_dwordx4 v[32:35], v36, %0, s59 offen sc1\n\t" \
"s_waitcnt lgkmcnt(9)\n\t" \
"v_mfma_f32_32x32x16_bf16 a[32:47], v[192:195], v[220:223], a[32:47]\n\t" \
"ds_read_b64_tr_b16 v[246:247], v54 offset:5120\n\t" \
"ds_read_b64_tr_b16 v[248:249], v22 offset:6144\n\t" \
"ds_read_b64_tr_b16 v[250:251], v22 offset:6656\n\t" \
"v_mfma_f32_32x32x16_bf16 a[48:63], v[196:199], v[220:223], a[48:63]\n\t" \
"ds_read_b64_tr_b16 v[252:253], v23 offset:6144\n\t" \
"ds_read_b64_tr_b16 v[254:255], v23 offset:6656\n\t" \
"s_waitcnt lgkmcnt(11)\n\t" \
"v_mfma_f32_32x32x16_bf16 v[64:79], v[232:235], v[224:227], v[64:79]\n\t" \
"s_waitcnt lgkmcnt(0)\n\t" \
"s_waitcnt vmcnt(15)\n\t" \
"s_barrier\n\t" \
"v_mfma_f32_32x32x16_bf16 v[128:143], v[232:235], v[228:231], v[128:143]\n\t" \
"s_add_u32 s56, s75, 12\n\t" \
"s_sub_u32 s57, s56, 20\n\t" \
"s_cmp_ge_u32 s56, 20\n\t" \
"s_cselect_b32 s56, s57, s56\n\t" \
"s_lshl_b32 s56, s56, 13\n\t" \
"s_sub_u32 s57, s56, s83\n\t" \
"s_mov_b32 s83, s56\n\t" \
"v_add_u32 v18, s57, v18\n\t" \
"v_add_u32 v19, s57, v19\n\t" \
"v_add_u32 v20, s57, v20\n\t" \
"v_add_u32 v21, s57, v21\n\t" \
"v_mfma_f32_32x32x16_bf16 v[80:95], v[236:239], v[224:227], v[80:95]\n\t" \
"v_add_u32 v39, 0xe000, v39\n\t" \
"v_add_u32 v40, 0xe000, v40\n\t" \
"v_add_u32 v41, 0xe000, v41\n\t" \
"v_add_u32 v42, 0xe000, v42\n\t" \
"v_add_u32 v43, 0xe000, v43\n\t" \
"v_mfma_f32_32x32x16_bf16 v[144:159], v[236:239], v[228:231], v[144:159]\n\t" \
"v_add_u32 v44, 0xe000, v44\n\t" \
"v_add_u32 v45, 0xe000, v45\n\t" \
"v_add_u32 v46, 0xe000, v46\n\t" \
"v_add_u32 v47, 0xe000, v47\n\t" \
"v_mfma_f32_32x32x16_bf16 v[96:111], v[240:243], v[224:227], v[96:111]\n\t" \
"v_add_u32 v48, 0xe000, v48\n\t" \
"v_add_u32 v49, 0xe000, v49\n\t" \
"v_add_u32 v50, 0xe000, v50\n\t" \
"v_add_u32 v51, 0xe000, v51\n\t" \
"v_add_u32 v52, 0xe000, v52\n\t" \
"v_mfma_f32_32x32x16_bf16 v[160:175], v[240:243], v[228:231], v[160:175]\n\t" \
"v_add_u32 v53, 0xe000, v53\n\t" \
"v_add_u32 v54, 0xe000, v54\n\t" \
"v_add_u32 v22, 0xe000, v22\n\t" \
"v_add_u32 v23, 0xe000, v23\n\t" \
"v_mfma_f32_32x32x16_bf16 v[112:127], v[244:247], v[224:227], v[112:127]\n\t" \
"ds_read_b64_tr_b16 v[200:201], v39 offset:0\n\t" \
"v_accvgpr_read_b32 v24, a216\n\t" \
"ds_read_b64_tr_b16 v[202:203], v40 offset:1024\n\t" \
"v_accvgpr_read_b32 v25, a217\n\t" \
"ds_read_b128 v[192:195], v18 offset:0\n\t" \
"v_mfma_f32_32x32x16_bf16 v[176:191], v[244:247], v[228:231], v[176:191]\n\t" \
"v_accvgpr_read_b32 v26, a218\n\t" \
"ds_read_b128 v[196:199], v18 offset:4096\n\t" \
"v_accvgpr_read_b32 v27, a219\n\t" \
"ds_read_b64_tr_b16 v[204:205], v41 offset:0\n\t" \
"v_mfma_f32_32x32x16_bf16 a[0:15], v[248:251], v[224:227], a[0:15]\n\t" \
"v_accvgpr_read_b32 v28, a220\n\t" \
"ds_read_b64_tr_b16 v[206:207], v42 offset:1024\n\t" \
"v_accvgpr_read_b32 v29, a221\n\t" \
"ds_read_b64_tr_b16 v[208:209], v43 offset:0\n\t" \
"v_accvgpr_read_b32 v30, a222\n\t" \
"v_mfma_f32_32x32x16_bf16 a[16:31], v[248:251], v[228:231], a[16:31]\n\t" \
"ds_read_b64_tr_b16 v[210:211], v44 offset:1024\n\t" \
"v_accvgpr_read_b32 v31, a223\n\t" \
"ds_read_b64_tr_b16 v[212:213], v45 offset:0\n\t" \
"v_cvt_pk_bf16_f32 v32, v24, v25\n\t" \
"v_mfma_f32_32x32x16_bf16 a[32:47], v[224:227], v[252:255], a[32:47]\n\t" \
"ds_read_b64_tr_b16 v[214:215], v46 offset:1024\n\t" \
"v_cvt_pk_bf16_f32 v33, v26, v27\n\t" \
"ds_read_b64_tr_b16 v[216:217], v22 offset:0\n\t" \
"v_cvt_pk_bf16_f32 v34, v28, v29\n\t" \
"ds_read_b64_tr_b16 v[218:219], v22 offset:512\n\t" \
"v_mfma_f32_32x32x16_bf16 a[48:63], v[228:231], v[252:255], a[48:63]\n\t" \
"v_cvt_pk_bf16_f32 v35, v30, v31\n\t" \
"ds_read_b64_tr_b16 v[220:221], v23 offset:0\n\t" \
"s_add_u32 s59, s82, 21504\n\t" \
"buffer_store_dwordx4 v[32:35], v36, %0, s59 offen sc1\n\t" \
"ds_read_b64_tr_b16 v[222:223], v23 offset:512\n\t" \
"s_waitcnt lgkmcnt(11)\n\t" \
"v_mfma_f32_32x32x16_bf16 v[64:79], v[200:203], v[192:195], v[64:79]\n\t" \
"ds_read_b64_tr_b16 v[232:233], v39 offset:4096\n\t" \
"v_accvgpr_read_b32 v24, a224\n\t" \
"s_add_u32 m0, s70, 40960\n\t" \
"s_mov_b32 s54, 983040\n\t" \
"buffer_load_dwordx4 %22, %15, s54 offen lds\n\t" \
"s_waitcnt lgkmcnt(11)\n\t" \
"v_mfma_f32_32x32x16_bf16 v[128:143], v[200:203], v[196:199], v[128:143]\n\t" \
"v_accvgpr_read_b32 v25, a225\n\t" \
"ds_read_b64_tr_b16 v[234:235], v40 offset:5120\n\t" \
"v_accvgpr_read_b32 v26, a226\n\t" \
"s_waitcnt lgkmcnt(10)\n\t" \
"v_mfma_f32_32x32x16_bf16 v[80:95], v[204:207], v[192:195], v[80:95]\n\t" \
"s_add_u32 m0, s70, 41984\n\t" \
"s_mov_b32 s55, 983040\n\t" \
"buffer_load_dwordx4 %23, %15, s55 offen lds\n\t" \
"v_accvgpr_read_b32 v27, a227\n\t" \
"v_mfma_f32_32x32x16_bf16 v[144:159], v[204:207], v[196:199], v[144:159]\n\t" \
"ds_read_b128 v[224:227], v19 offset:0\n\t" \
"v_accvgpr_read_b32 v28, a228\n\t" \
"s_add_u32 m0, s70, 49152\n\t" \
"s_add_u32 s54, s71, 768\n\t" \
"buffer_load_dwordx4 %18, %13, s54 offen lds\n\t" \
"s_waitcnt lgkmcnt(9)\n\t" \
"v_mfma_f32_32x32x16_bf16 v[96:111], v[208:211], v[192:195], v[96:111]\n\t" \
"v_accvgpr_read_b32 v29, a229\n\t" \
"ds_read_b128 v[228:231], v19 offset:4096\n\t" \
"v_accvgpr_read_b32 v30, a230\n\t" \
"v_mfma_f32_32x32x16_bf16 v[160:175], v[208:211], v[196:199], v[160:175]\n\t" \
"s_add_u32 m0, s70, 50176\n\t" \
"s_add_u32 s55, s71, 768\n\t" \
"buffer_load_dwordx4 %19, %13, s55 offen lds\n\t" \
"v_accvgpr_read_b32 v31, a231\n\t" \
"s_waitcnt lgkmcnt(8)\n\t" \
"v_mfma_f32_32x32x16_bf16 v[112:127], v[212:215], v[192:195], v[112:127]\n\t" \
"ds_read_b64_tr_b16 v[236:237], v41 offset:4096\n\t" \
"v_cvt_pk_bf16_f32 v32, v24, v25\n\t" \
"s_add_u32 m0, s70, 57344\n\t" \
"s_add_u32 s54, s72, 768\n\t" \
"buffer_load_dwordx4 %18, %13, s54 offen lds\n\t" \
"v_mfma_f32_32x32x16_bf16 v[176:191], v[212:215], v[196:199], v[176:191]\n\t" \
"v_cvt_pk_bf16_f32 v33, v26, v27\n\t" \
"ds_read_b64_tr_b16 v[238:239], v42 offset:5120\n\t" \
"v_cvt_pk_bf16_f32 v34, v28, v29\n\t" \
"s_waitcnt lgkmcnt(8)\n\t" \
"v_mfma_f32_32x32x16_bf16 a[0:15], v[216:219], v[192:195], a[0:15]\n\t" \
"ds_read_b64_tr_b16 v[240:241], v43 offset:4096\n\t" \
"v_cvt_pk_bf16_f32 v35, v30, v31\n\t" \
"v_mfma_f32_32x32x16_bf16 a[16:31], v[216:219], v[196:199], a[16:31]\n\t" \
"ds_read_b64_tr_b16 v[242:243], v44 offset:5120\n\t" \
"s_add_u32 s59, s82, 24576\n\t" \
"buffer_store_dwordx4 v[32:35], v36, %0, s59 offen sc1\n\t" \
"ds_read_b64_tr_b16 v[244:245], v45 offset:4096\n\t" \
"s_waitcnt lgkmcnt(9)\n\t" \
"v_mfma_f32_32x32x16_bf16 a[32:47], v[192:195], v[220:223], a[32:47]\n\t" \
"ds_read_b64_tr_b16 v[246:247], v46 offset:5120\n\t" \
"ds_read_b64_tr_b16 v[248:249], v22 offset:2048\n\t" \
"ds_read_b64_tr_b16 v[250:251], v22 offset:2560\n\t" \
"v_mfma_f32_32x32x16_bf16 a[48:63], v[196:199], v[220:223], a[48:63]\n\t" \
"ds_read_b64_tr_b16 v[252:253], v23 offset:2048\n\t" \
"ds_read_b64_tr_b16 v[254:255], v23 offset:2560\n\t" \
"s_waitcnt lgkmcnt(11)\n\t" \
"v_mfma_f32_32x32x16_bf16 v[64:79], v[232:235], v[224:227], v[64:79]\n\t" \
"ds_read_b64_tr_b16 v[200:201], v47 offset:0\n\t" \
"v_accvgpr_read_b32 v24, a232\n\t" \
"s_add_u32 m0, s70, 58368\n\t" \
"s_add_u32 s55, s72, 768\n\t" \
"buffer_load_dwordx4 %19, %13, s55 offen lds\n\t" \
"s_waitcnt lgkmcnt(11)\n\t" \
"v_mfma_f32_32x32x16_bf16 v[128:143], v[232:235], v[228:231], v[128:143]\n\t" \
"v_accvgpr_read_b32 v25, a233\n\t" \
"ds_read_b64_tr_b16 v[202:203], v48 offset:1024\n\t" \
"v_accvgpr_read_b32 v26, a234\n\t" \
"s_waitcnt lgkmcnt(10)\n\t" \
"v_mfma_f32_32x32x16_bf16 v[80:95], v[236:239], v[224:227], v[80:95]\n\t" \
"s_add_u32 m0, s70, 65536\n\t" \
"s_add_u32 s54, s73, 768\n\t" \
"buffer_load_dwordx4 %18, %13, s54 offen lds\n\t" \
"v_accvgpr_read_b32 v27, a235\n\t" \
"v_mfma_f32_32x32x16_bf16 v[144:159], v[236:239], v[228:231], v[144:159]\n\t" \
"ds_read_b128 v[192:195], v20 offset:0\n\t" \
"v_accvgpr_read_b32 v28, a236\n\t" \
"s_add_u32 m0, s70, 66560\n\t" \
"s_add_u32 s55, s73, 768\n\t" \
"buffer_load_dwordx4 %19, %13, s55 offen lds\n\t" \
"s_waitcnt lgkmcnt(9)\n\t" \
"v_mfma_f32_32x32x16_bf16 v[96:111], v[240:243], v[224:227], v[96:111]\n\t" \
"v_accvgpr_read_b32 v29, a237\n\t" \
"ds_read_b128 v[196:199], v20 offset:4096\n\t" \
"v_accvgpr_read_b32 v30, a238\n\t" \
"v_mfma_f32_32x32x16_bf16 v[160:175], v[240:243], v[228:231], v[160:175]\n\t" \
"s_add_u32 m0, s70, 73728\n\t" \
"s_add_u32 s54, s74, 768\n\t" \
"buffer_load_dwordx4 %18, %13, s54 offen lds\n\t" \
"v_accvgpr_read_b32 v31, a239\n\t" \
"s_waitcnt lgkmcnt(8)\n\t" \
"v_mfma_f32_32x32x16_bf16 v[112:127], v[244:247], v[224:227], v[112:127]\n\t" \
"ds_read_b64_tr_b16 v[204:205], v49 offset:0\n\t" \
"v_cvt_pk_bf16_f32 v32, v24, v25\n\t" \
"s_add_u32 m0, s70, 74752\n\t" \
"s_add_u32 s55, s74, 768\n\t" \
"buffer_load_dwordx4 %19, %13, s55 offen lds\n\t" \
"v_mfma_f32_32x32x16_bf16 v[176:191], v[244:247], v[228:231], v[176:191]\n\t" \
"v_cvt_pk_bf16_f32 v33, v26, v27\n\t" \
"ds_read_b64_tr_b16 v[206:207], v50 offset:1024\n\t" \
"v_cvt_pk_bf16_f32 v34, v28, v29\n\t" \
"s_waitcnt lgkmcnt(8)\n\t" \
"v_mfma_f32_32x32x16_bf16 a[0:15], v[248:251], v[224:227], a[0:15]\n\t" \
"ds_read_b64_tr_b16 v[208:209], v51 offset:0\n\t" \
"v_cvt_pk_bf16_f32 v35, v30, v31\n\t" \
"v_mfma_f32_32x32x16_bf16 a[16:31], v[248:251], v[228:231], a[16:31]\n\t" \
"ds_read_b64_tr_b16 v[210:211], v52 offset:1024\n\t" \
"s_add_u32 s59, s82, 28672\n\t" \
"buffer_store_dwordx4 v[32:35], v36, %0, s59 offen sc1\n\t" \
"ds_read_b64_tr_b16 v[212:213], v53 offset:0\n\t" \
"s_waitcnt lgkmcnt(9)\n\t" \
"v_mfma_f32_32x32x16_bf16 a[32:47], v[224:227], v[252:255], a[32:47]\n\t" \
"ds_read_b64_tr_b16 v[214:215], v54 offset:1024\n\t" \
"ds_read_b64_tr_b16 v[216:217], v22 offset:4096\n\t" \
"ds_read_b64_tr_b16 v[218:219], v22 offset:4608\n\t" \
"v_mfma_f32_32x32x16_bf16 a[48:63], v[228:231], v[252:255], a[48:63]\n\t" \
"ds_read_b64_tr_b16 v[220:221], v23 offset:4096\n\t" \
"ds_read_b64_tr_b16 v[222:223], v23 offset:4608\n\t" \
"s_waitcnt lgkmcnt(11)\n\t" \
"v_mfma_f32_32x32x16_bf16 v[64:79], v[200:203], v[192:195], v[64:79]\n\t" \
"ds_read_b64_tr_b16 v[232:233], v47 offset:4096\n\t" \
"v_accvgpr_read_b32 v24, a240\n\t" \
"s_add_u32 m0, s70, 81920\n\t" \
"s_mov_b32 s54, 3145728\n\t" \
"buffer_load_dwordx4 %20, %14, s54 offen lds\n\t" \
"s_waitcnt lgkmcnt(11)\n\t" \
"v_mfma_f32_32x32x16_bf16 v[128:143], v[200:203], v[196:199], v[128:143]\n\t" \
"v_accvgpr_read_b32 v25, a241\n\t" \
"ds_read_b64_tr_b16 v[234:235], v48 offset:5120\n\t" \
"v_accvgpr_read_b32 v26, a242\n\t" \
"s_waitcnt lgkmcnt(10)\n\t" \
"v_mfma_f32_32x32x16_bf16 v[80:95], v[204:207], v[192:195], v[80:95]\n\t" \
"s_add_u32 m0, s70, 82944\n\t" \
"s_mov_b32 s55, 3145728\n\t" \
"buffer_load_dwordx4 %21, %14, s55 offen lds\n\t" \
"v_accvgpr_read_b32 v27, a243\n\t" \
"v_mfma_f32_32x32x16_bf16 v[144:159], v[204:207], v[196:199], v[144:159]\n\t" \
"ds_read_b128 v[224:227], v21 offset:0\n\t" \
"v_accvgpr_read_b32 v28, a244\n\t" \
"s_add_u32 m0, s70, 90112\n\t" \
"s_mov_b32 s54, 3407872\n\t" \
"buffer_load_dwordx4 %20, %14, s54 offen lds\n\t" \
"s_waitcnt lgkmcnt(9)\n\t" \
"v_mfma_f32_32x32x16_bf16 v[96:111], v[208:211], v[192:195], v[96:111]\n\t" \
"v_accvgpr_read_b32 v29, a245\n\t" \
"ds_read_b128 v[228:231], v21 offset:4096\n\t" \
"v_mfma_f32_32x32x16_bf16 v[160:175], v[208:211], v[196:199], v[160:175]\n\t" \
"v_accvgpr_read_b32 v30, a246\n\t" \
"s_add_u32 m0, s70, 91136\n\t" \
"s_mov_b32 s55, 3407872\n\t" \
"buffer_load_dwordx4 %21, %14, s55 offen lds\n\t" \
"v_accvgpr_read_b32 v31, a247\n\t" \
"s_waitcnt lgkmcnt(8)\n\t" \
"v_mfma_f32_32x32x16_bf16 v[112:127], v[212:215], v[192:195], v[112:127]\n\t" \
"ds_read_b64_tr_b16 v[236:237], v49 offset:4096\n\t" \
"v_cvt_pk_bf16_f32 v32, v24, v25\n\t" \
"ds_read_b64_tr_b16 v[238:239], v50 offset:5120\n\t" \
"v_mfma_f32_32x32x16_bf16 v[176:191], v[212:215], v[196:199], v[176:191]\n\t" \
"v_cvt_pk_bf16_f32 v33, v26, v27\n\t" \
"ds_read_b64_tr_b16 v[240:241], v51 offset:4096\n\t" \
"s_waitcnt lgkmcnt(9)\n\t" \
"v_mfma_f32_32x32x16_bf16 a[0:15], v[216:219], v[192:195], a[0:15]\n\t" \
"v_cvt_pk_bf16_f32 v34, v28, v29\n\t" \
"ds_read_b64_tr_b16 v[242:243], v52 offset:5120\n\t" \
"v_cvt_pk_bf16_f32 v35, v30, v31\n\t" \
"v_mfma_f32_32x32x16_bf16 a[16:31], v[216:219], v[196:199], a[16:31]\n\t" \
"ds_read_b64_tr_b16 v[244:245], v53 offset:4096\n\t" \
"s_add_u32 s59, s82, 25600\n\t" \
"buffer_store_dwordx4 v[32:35], v36, %0, s59 offen sc1\n\t" \
"s_waitcnt lgkmcnt(9)\n\t" \
"v_mfma_f32_32x32x16_bf16 a[32:47], v[192:195], v[220:223], a[32:47]\n\t" \
"ds_read_b64_tr_b16 v[246:247], v54 offset:5120\n\t" \
"ds_read_b64_tr_b16 v[248:249], v22 offset:6144\n\t" \
"ds_read_b64_tr_b16 v[250:251], v22 offset:6656\n\t" \
"v_mfma_f32_32x32x16_bf16 a[48:63], v[196:199], v[220:223], a[48:63]\n\t" \
"ds_read_b64_tr_b16 v[252:253], v23 offset:6144\n\t" \
"ds_read_b64_tr_b16 v[254:255], v23 offset:6656\n\t" \
"s_waitcnt lgkmcnt(11)\n\t" \
"v_mfma_f32_32x32x16_bf16 v[64:79], v[232:235], v[224:227], v[64:79]\n\t" \
"s_waitcnt lgkmcnt(0)\n\t" \
"s_waitcnt vmcnt(15)\n\t" \
"s_barrier\n\t" \
"v_mfma_f32_32x32x16_bf16 v[128:143], v[232:235], v[228:231], v[128:143]\n\t" \
"s_add_u32 s56, s75, 19\n\t" \
"s_sub_u32 s57, s56, 20\n\t" \
"s_cmp_ge_u32 s56, 20\n\t" \
"s_cselect_b32 s56, s57, s56\n\t" \
"s_lshl_b32 s56, s56, 13\n\t" \
"s_sub_u32 s57, s56, s83\n\t" \
"s_mov_b32 s83, s56\n\t" \
"v_add_u32 v18, s57, v18\n\t" \
"v_add_u32 v19, s57, v19\n\t" \
"v_add_u32 v20, s57, v20\n\t" \
"v_add_u32 v21, s57, v21\n\t" \
"v_mfma_f32_32x32x16_bf16 v[80:95], v[236:239], v[224:227], v[80:95]\n\t" \
"v_add_u32 v39, 0xfffe6000, v39\n\t" \
"v_add_u32 v40, 0xfffe6000, v40\n\t" \
"v_add_u32 v41, 0xfffe6000, v41\n\t" \
"v_add_u32 v42, 0xfffe6000, v42\n\t" \
"v_add_u32 v43, 0xfffe6000, v43\n\t" \
"v_mfma_f32_32x32x16_bf16 v[144:159], v[236:239], v[228:231], v[144:159]\n\t" \
"v_add_u32 v44, 0xfffe6000, v44\n\t" \
"v_add_u32 v45, 0xfffe6000, v45\n\t" \
"v_add_u32 v46, 0xfffe6000, v46\n\t" \
"v_add_u32 v47, 0xfffe6000, v47\n\t" \
"v_mfma_f32_32x32x16_bf16 v[96:111], v[240:243], v[224:227], v[96:111]\n\t" \
"v_add_u32 v48, 0xfffe6000, v48\n\t" \
"v_add_u32 v49, 0xfffe6000, v49\n\t" \
"v_add_u32 v50, 0xfffe6000, v50\n\t" \
"v_add_u32 v51, 0xfffe6000, v51\n\t" \
"v_add_u32 v52, 0xfffe6000, v52\n\t" \
"v_mfma_f32_32x32x16_bf16 v[160:175], v[240:243], v[228:231], v[160:175]\n\t" \
"v_add_u32 v53, 0xfffe6000, v53\n\t" \
"v_add_u32 v54, 0xfffe6000, v54\n\t" \
"v_add_u32 v22, 0xfffe6000, v22\n\t" \
"v_add_u32 v23, 0xfffe6000, v23\n\t" \
"v_mfma_f32_32x32x16_bf16 v[112:127], v[244:247], v[224:227], v[112:127]\n\t" \
"ds_read_b64_tr_b16 v[200:201], v39 offset:0\n\t" \
"v_accvgpr_read_b32 v24, a248\n\t" \
"ds_read_b64_tr_b16 v[202:203], v40 offset:1024\n\t" \
"v_accvgpr_read_b32 v25, a249\n\t" \
"ds_read_b128 v[192:195], v18 offset:0\n\t" \
"v_mfma_f32_32x32x16_bf16 v[176:191], v[244:247], v[228:231], v[176:191]\n\t" \
"v_accvgpr_read_b32 v26, a250\n\t" \
"ds_read_b128 v[196:199], v18 offset:4096\n\t" \
"v_accvgpr_read_b32 v27, a251\n\t" \
"ds_read_b64_tr_b16 v[204:205], v41 offset:0\n\t" \
"v_mfma_f32_32x32x16_bf16 a[0:15], v[248:251], v[224:227], a[0:15]\n\t" \
"v_accvgpr_read_b32 v28, a252\n\t" \
"ds_read_b64_tr_b16 v[206:207], v42 offset:1024\n\t" \
"v_accvgpr_read_b32 v29, a253\n\t" \
"ds_read_b64_tr_b16 v[208:209], v43 offset:0\n\t" \
"v_accvgpr_read_b32 v30, a254\n\t" \
"v_mfma_f32_32x32x16_bf16 a[16:31], v[248:251], v[228:231], a[16:31]\n\t" \
"ds_read_b64_tr_b16 v[210:211], v44 offset:1024\n\t" \
"v_accvgpr_read_b32 v31, a255\n\t" \
"ds_read_b64_tr_b16 v[212:213], v45 offset:0\n\t" \
"v_cvt_pk_bf16_f32 v32, v24, v25\n\t" \
"v_mfma_f32_32x32x16_bf16 a[32:47], v[224:227], v[252:255], a[32:47]\n\t" \
"ds_read_b64_tr_b16 v[214:215], v46 offset:1024\n\t" \
"v_cvt_pk_bf16_f32 v33, v26, v27\n\t" \
"ds_read_b64_tr_b16 v[216:217], v22 offset:0\n\t" \
"v_cvt_pk_bf16_f32 v34, v28, v29\n\t" \
"ds_read_b64_tr_b16 v[218:219], v22 offset:512\n\t" \
"v_mfma_f32_32x32x16_bf16 a[48:63], v[228:231], v[252:255], a[48:63]\n\t" \
"v_cvt_pk_bf16_f32 v35, v30, v31\n\t" \
"ds_read_b64_tr_b16 v[220:221], v23 offset:0\n\t" \
"s_add_u32 s59, s82, 29696\n\t" \
"buffer_store_dwordx4 v[32:35], v36, %0, s59 offen sc1\n\t" \
"ds_read_b64_tr_b16 v[222:223], v23 offset:512\n\t" \
"s_waitcnt lgkmcnt(11)\n\t" \
"v_mfma_f32_32x32x16_bf16 v[64:79], v[200:203], v[192:195], v[64:79]\n\t" \
"ds_read_b64_tr_b16 v[232:233], v39 offset:4096\n\t" \
"s_add_u32 m0, s70, 98304\n\t" \
"s_mov_b32 s54, 1179648\n\t" \
"buffer_load_dwordx4 %22, %15, s54 offen lds\n\t" \
"s_waitcnt lgkmcnt(11)\n\t" \
"v_mfma_f32_32x32x16_bf16 v[128:143], v[200:203], v[196:199], v[128:143]\n\t" \
"ds_read_b64_tr_b16 v[234:235], v40 offset:5120\n\t" \
"s_add_u32 m0, s70, 99328\n\t" \
"s_mov_b32 s55, 1179648\n\t" \
"buffer_load_dwordx4 %23, %15, s55 offen lds\n\t" \
"s_waitcnt lgkmcnt(10)\n\t" \
"v_mfma_f32_32x32x16_bf16 v[80:95], v[204:207], v[192:195], v[80:95]\n\t" \
"ds_read_b128 v[224:227], v19 offset:0\n\t" \
"v_mfma_f32_32x32x16_bf16 v[144:159], v[204:207], v[196:199], v[144:159]\n\t" \
"s_add_u32 m0, s70, 106496\n\t" \
"s_add_u32 s54, s71, 896\n\t" \
"buffer_load_dwordx4 %18, %13, s54 offen lds\n\t" \
"ds_read_b128 v[228:231], v19 offset:4096\n\t" \
"s_waitcnt lgkmcnt(10)\n\t" \
"v_mfma_f32_32x32x16_bf16 v[96:111], v[208:211], v[192:195], v[96:111]\n\t" \
"s_add_u32 m0, s70, 107520\n\t" \
"s_add_u32 s55, s71, 896\n\t" \
"buffer_load_dwordx4 %19, %13, s55 offen lds\n\t" \
"v_mfma_f32_32x32x16_bf16 v[160:175], v[208:211], v[196:199], v[160:175]\n\t" \
"ds_read_b64_tr_b16 v[236:237], v41 offset:4096\n\t" \
"s_add_u32 m0, s70, 114688\n\t" \
"s_add_u32 s54, s72, 896\n\t" \
"buffer_load_dwordx4 %18, %13, s54 offen lds\n\t" \
"s_waitcnt lgkmcnt(9)\n\t" \
"v_mfma_f32_32x32x16_bf16 v[112:127], v[212:215], v[192:195], v[112:127]\n\t" \
"ds_read_b64_tr_b16 v[238:239], v42 offset:5120\n\t" \
"ds_read_b64_tr_b16 v[240:241], v43 offset:4096\n\t" \
"v_mfma_f32_32x32x16_bf16 v[176:191], v[212:215], v[196:199], v[176:191]\n\t" \
"ds_read_b64_tr_b16 v[242:243], v44 offset:5120\n\t" \
"s_waitcnt lgkmcnt(10)\n\t" \
"v_mfma_f32_32x32x16_bf16 a[0:15], v[216:219], v[192:195], a[0:15]\n\t" \
"ds_read_b64_tr_b16 v[244:245], v45 offset:4096\n\t" \
"ds_read_b64_tr_b16 v[246:247], v46 offset:5120\n\t" \
"v_mfma_f32_32x32x16_bf16 a[16:31], v[216:219], v[196:199], a[16:31]\n\t" \
"ds_read_b64_tr_b16 v[248:249], v22 offset:2048\n\t" \
"s_waitcnt lgkmcnt(11)\n\t" \
"v_mfma_f32_32x32x16_bf16 a[32:47], v[192:195], v[220:223], a[32:47]\n\t" \
"ds_read_b64_tr_b16 v[250:251], v22 offset:2560\n\t" \
"ds_read_b64_tr_b16 v[252:253], v23 offset:2048\n\t" \
"v_mfma_f32_32x32x16_bf16 a[48:63], v[196:199], v[220:223], a[48:63]\n\t" \
"ds_read_b64_tr_b16 v[254:255], v23 offset:2560\n\t" \
"s_waitcnt lgkmcnt(11)\n\t" \
"v_mfma_f32_32x32x16_bf16 v[64:79], v[232:235], v[224:227], v[64:79]\n\t" \
"ds_read_b64_tr_b16 v[200:201], v47 offset:0\n\t" \
"s_add_u32 m0, s70, 115712\n\t" \
"s_add_u32 s55, s72, 896\n\t" \
"buffer_load_dwordx4 %19, %13, s55 offen lds\n\t" \
"s_waitcnt lgkmcnt(11)\n\t" \
"v_mfma_f32_32x32x16_bf16 v[128:143], v[232:235], v[228:231], v[128:143]\n\t" \
"ds_read_b64_tr_b16 v[202:203], v48 offset:1024\n\t" \
"s_add_u32 m0, s70, 122880\n\t" \
"s_add_u32 s54, s73, 896\n\t" \
"buffer_load_dwordx4 %18, %13, s54 offen lds\n\t" \
"s_waitcnt lgkmcnt(10)\n\t" \
"v_mfma_f32_32x32x16_bf16 v[80:95], v[236:239], v[224:227], v[80:95]\n\t" \
"ds_read_b128 v[192:195], v20 offset:0\n\t" \
"v_mfma_f32_32x32x16_bf16 v[144:159], v[236:239], v[228:231], v[144:159]\n\t" \
"s_add_u32 m0, s70, 123904\n\t" \
"s_add_u32 s55, s73, 896\n\t" \
"buffer_load_dwordx4 %19, %13, s55 offen lds\n\t" \
"ds_read_b128 v[196:199], v20 offset:4096\n\t" \
"s_waitcnt lgkmcnt(10)\n\t" \
"v_mfma_f32_32x32x16_bf16 v[96:111], v[240:243], v[224:227], v[96:111]\n\t" \
"s_add_u32 m0, s70, 131072\n\t" \
"s_add_u32 s54, s74, 896\n\t" \
"buffer_load_dwordx4 %18, %13, s54 offen lds\n\t" \
"v_mfma_f32_32x32x16_bf16 v[160:175], v[240:243], v[228:231], v[160:175]\n\t" \
"ds_read_b64_tr_b16 v[204:205], v49 offset:0\n\t" \
"s_add_u32 m0, s70, 132096\n\t" \
"s_add_u32 s55, s74, 896\n\t" \
"buffer_load_dwordx4 %19, %13, s55 offen lds\n\t" \
"s_waitcnt lgkmcnt(9)\n\t" \
"v_mfma_f32_32x32x16_bf16 v[112:127], v[244:247], v[224:227], v[112:127]\n\t" \
"ds_read_b64_tr_b16 v[206:207], v50 offset:1024\n\t" \
"ds_read_b64_tr_b16 v[208:209], v51 offset:0\n\t" \
"v_mfma_f32_32x32x16_bf16 v[176:191], v[244:247], v[228:231], v[176:191]\n\t" \
"ds_read_b64_tr_b16 v[210:211], v52 offset:1024\n\t" \
"s_waitcnt lgkmcnt(10)\n\t" \
"v_mfma_f32_32x32x16_bf16 a[0:15], v[248:251], v[224:227], a[0:15]\n\t" \
"ds_read_b64_tr_b16 v[212:213], v53 offset:0\n\t" \
"ds_read_b64_tr_b16 v[214:215], v54 offset:1024\n\t" \
"v_mfma_f32_32x32x16_bf16 a[16:31], v[248:251], v[228:231], a[16:31]\n\t" \
"ds_read_b64_tr_b16 v[216:217], v22 offset:4096\n\t" \
"s_waitcnt lgkmcnt(11)\n\t" \
"v_mfma_f32_32x32x16_bf16 a[32:47], v[224:227], v[252:255], a[32:47]\n\t" \
"ds_read_b64_tr_b16 v[218:219], v22 offset:4608\n\t" \
"ds_read_b64_tr_b16 v[220:221], v23 offset:4096\n\t" \
"v_mfma_f32_32x32x16_bf16 a[48:63], v[228:231], v[252:255], a[48:63]\n\t" \
"ds_read_b64_tr_b16 v[222:223], v23 offset:4608\n\t" \
"s_waitcnt lgkmcnt(11)\n\t" \
"v_mfma_f32_32x32x16_bf16 v[64:79], v[200:203], v[192:195], v[64:79]\n\t" \
"ds_read_b64_tr_b16 v[232:233], v47 offset:4096\n\t" \
"s_add_u32 m0, s70, 139264\n\t" \
"s_mov_b32 s54, 3670016\n\t" \
"buffer_load_dwordx4 %20, %14, s54 offen lds\n\t" \
"s_waitcnt lgkmcnt(11)\n\t" \
"v_mfma_f32_32x32x16_bf16 v[128:143], v[200:203], v[196:199], v[128:143]\n\t" \
"ds_read_b64_tr_b16 v[234:235], v48 offset:5120\n\t" \
"s_waitcnt lgkmcnt(10)\n\t" \
"v_mfma_f32_32x32x16_bf16 v[80:95], v[204:207], v[192:195], v[80:95]\n\t" \
"s_add_u32 m0, s70, 140288\n\t" \
"s_mov_b32 s55, 3670016\n\t" \
"buffer_load_dwordx4 %21, %14, s55 offen lds\n\t" \
"ds_read_b128 v[224:227], v21 offset:0\n\t" \
"v_mfma_f32_32x32x16_bf16 v[144:159], v[204:207], v[196:199], v[144:159]\n\t" \
"s_add_u32 m0, s70, 147456\n\t" \
"s_mov_b32 s54, 3932160\n\t" \
"buffer_load_dwordx4 %20, %14, s54 offen lds\n\t" \
"s_waitcnt lgkmcnt(9)\n\t" \
"v_mfma_f32_32x32x16_bf16 v[96:111], v[208:211], v[192:195], v[96:111]\n\t" \
"ds_read_b128 v[228:231], v21 offset:4096\n\t" \
"s_add_u32 m0, s70, 148480\n\t" \
"s_mov_b32 s55, 3932160\n\t" \
"buffer_load_dwordx4 %21, %14, s55 offen lds\n\t" \
"v_mfma_f32_32x32x16_bf16 v[160:175], v[208:211], v[196:199], v[160:175]\n\t" \
"ds_read_b64_tr_b16 v[236:237], v49 offset:4096\n\t" \
"s_waitcnt lgkmcnt(9)\n\t" \
"v_mfma_f32_32x32x16_bf16 v[112:127], v[212:215], v[192:195], v[112:127]\n\t" \
"ds_read_b64_tr_b16 v[238:239], v50 offset:5120\n\t" \
"ds_read_b64_tr_b16 v[240:241], v51 offset:4096\n\t" \
"v_mfma_f32_32x32x16_bf16 v[176:191], v[212:215], v[196:199], v[176:191]\n\t" \
"ds_read_b64_tr_b16 v[242:243], v52 offset:5120\n\t" \
"s_waitcnt lgkmcnt(10)\n\t" \
"v_mfma_f32_32x32x16_bf16 a[0:15], v[216:219], v[192:195], a[0:15]\n\t" \
"ds_read_b64_tr_b16 v[244:245], v53 offset:4096\n\t" \
"ds_read_b64_tr_b16 v[246:247], v54 offset:5120\n\t" \
"v_mfma_f32_32x32x16_bf16 a[16:31], v[216:219], v[196:199], a[16:31]\n\t" \
"ds_read_b64_tr_b16 v[248:249], v22 offset:6144\n\t" \
"s_waitcnt lgkmcnt(11)\n\t" \
"v_mfma_f32_32x32x16_bf16 a[32:47], v[192:195], v[220:223], a[32:47]\n\t" \
"ds_read_b64_tr_b16 v[250:251], v22 offset:6656\n\t" \
"ds_read_b64_tr_b16 v[252:253], v23 offset:6144\n\t" \
"v_mfma_f32_32x32x16_bf16 a[48:63], v[196:199], v[220:223], a[48:63]\n\t" \
"ds_read_b64_tr_b16 v[254:255], v23 offset:6656\n\t" \
"s_waitcnt lgkmcnt(11)\n\t" \
"v_mfma_f32_32x32x16_bf16 v[64:79], v[232:235], v[224:227], v[64:79]\n\t" \
"s_waitcnt lgkmcnt(0)\n\t" \
"s_waitcnt vmcnt(12)\n\t" \
"s_barrier\n\t" \
"s_cmp_lg_u32 s75, 0\n\t" \
"s_cbranch_scc1 NOARR_%=\n\t" \
"v_mov_b32 v33, 64\n\t" \
"v_mov_b32 v34, 1\n\t" \
"s_mov_b64 exec, 1\n\t" \
"buffer_atomic_add v34, v33, %0, s79 offen\n\t" \
"s_mov_b64 exec, -1\n\t" \
"NOARR_%=:\n\t" \
"v_mfma_f32_32x32x16_bf16 v[128:143], v[232:235], v[228:231], v[128:143]\n\t" \
"s_add_u32 s56, s75, 6\n\t" \
"s_sub_u32 s57, s56, 20\n\t" \
"s_cmp_ge_u32 s56, 20\n\t" \
"s_cselect_b32 s56, s57, s56\n\t" \
"s_lshl_b32 s56, s56, 13\n\t" \
"s_sub_u32 s57, s56, s83\n\t" \
"s_mov_b32 s83, s56\n\t" \
"v_add_u32 v18, s57, v18\n\t" \
"v_add_u32 v19, s57, v19\n\t" \
"v_add_u32 v20, s57, v20\n\t" \
"v_mfma_f32_32x32x16_bf16 v[80:95], v[236:239], v[224:227], v[80:95]\n\t" \
"v_add_u32 v21, s57, v21\n\t" \
"v_add_u32 v39, 0xe000, v39\n\t" \
"v_add_u32 v40, 0xe000, v40\n\t" \
"v_mfma_f32_32x32x16_bf16 v[144:159], v[236:239], v[228:231], v[144:159]\n\t" \
"v_add_u32 v41, 0xe000, v41\n\t" \
"v_add_u32 v42, 0xe000, v42\n\t" \
"v_add_u32 v43, 0xe000, v43\n\t" \
"v_add_u32 v44, 0xe000, v44\n\t" \
"v_mfma_f32_32x32x16_bf16 v[96:111], v[240:243], v[224:227], v[96:111]\n\t" \
"v_add_u32 v45, 0xe000, v45\n\t" \
"v_add_u32 v46, 0xe000, v46\n\t" \
"v_add_u32 v47, 0xe000, v47\n\t" \
"v_mfma_f32_32x32x16_bf16 v[160:175], v[240:243], v[228:231], v[160:175]\n\t" \
"v_add_u32 v48, 0xe000, v48\n\t" \
"v_add_u32 v49, 0xe000, v49\n\t" \
"v_add_u32 v50, 0xe000, v50\n\t" \
"v_mfma_f32_32x32x16_bf16 v[112:127], v[244:247], v[224:227], v[112:127]\n\t" \
"v_add_u32 v51, 0xe000, v51\n\t" \
"v_add_u32 v52, 0xe000, v52\n\t" \
"v_add_u32 v53, 0xe000, v53\n\t" \
"v_add_u32 v54, 0xe000, v54\n\t" \
"v_mfma_f32_32x32x16_bf16 v[176:191], v[244:247], v[228:231], v[176:191]\n\t" \
"v_add_u32 v22, 0xe000, v22\n\t" \
"v_add_u32 v23, 0xe000, v23\n\t" \
"ds_read_b64_tr_b16 v[200:201], v39 offset:0\n\t" \
"v_mfma_f32_32x32x16_bf16 a[0:15], v[248:251], v[224:227], a[0:15]\n\t" \
"ds_read_b64_tr_b16 v[202:203], v40 offset:1024\n\t" \
"ds_read_b128 v[192:195], v18 offset:0\n\t" \
"ds_read_b128 v[196:199], v18 offset:4096\n\t" \
"v_mfma_f32_32x32x16_bf16 a[16:31], v[248:251], v[228:231], a[16:31]\n\t" \
"ds_read_b64_tr_b16 v[204:205], v41 offset:0\n\t" \
"ds_read_b64_tr_b16 v[206:207], v42 offset:1024\n\t" \
"ds_read_b64_tr_b16 v[208:209], v43 offset:0\n\t" \
"ds_read_b64_tr_b16 v[210:211], v44 offset:1024\n\t" \
"v_mfma_f32_32x32x16_bf16 a[32:47], v[224:227], v[252:255], a[32:47]\n\t" \
"ds_read_b64_tr_b16 v[212:213], v45 offset:0\n\t" \
"ds_read_b64_tr_b16 v[214:215], v46 offset:1024\n\t" \
"ds_read_b64_tr_b16 v[216:217], v22 offset:0\n\t" \
"v_mfma_f32_32x32x16_bf16 a[48:63], v[228:231], v[252:255], a[48:63]\n\t" \
"ds_read_b64_tr_b16 v[218:219], v22 offset:512\n\t" \
"ds_read_b64_tr_b16 v[220:221], v23 offset:0\n\t" \
"ds_read_b64_tr_b16 v[222:223], v23 offset:512\n\t" \
"s_waitcnt lgkmcnt(11)\n\t" \
"v_mfma_f32_32x32x16_bf16 v[64:79], v[200:203], v[192:195], v[64:79]\n\t" \
"ds_read_b64_tr_b16 v[232:233], v39 offset:4096\n\t" \
"s_add_u32 m0, s70, 155648\n\t" \
"s_mov_b32 s54, 1376256\n\t" \
"buffer_load_dwordx4 %22, %15, s54 offen lds\n\t" \
"s_waitcnt lgkmcnt(11)\n\t" \
"v_mfma_f32_32x32x16_bf16 v[128:143], v[200:203], v[196:199], v[128:143]\n\t" \
"ds_read_b64_tr_b16 v[234:235], v40 offset:5120\n\t" \
"s_waitcnt lgkmcnt(10)\n\t" \
"v_mfma_f32_32x32x16_bf16 v[80:95], v[204:207], v[192:195], v[80:95]\n\t" \
"ds_read_b128 v[224:227], v19 offset:0\n\t" \
"v_mfma_f32_32x32x16_bf16 v[144:159], v[204:207], v[196:199], v[144:159]\n\t" \
"ds_read_b128 v[228:231], v19 offset:4096\n\t" \
"s_waitcnt lgkmcnt(10)\n\t" \
"v_mfma_f32_32x32x16_bf16 v[96:111], v[208:211], v[192:195], v[96:111]\n\t" \
"ds_read_b64_tr_b16 v[236:237], v41 offset:4096\n\t" \
"ds_read_b64_tr_b16 v[238:239], v42 offset:5120\n\t" \
"v_mfma_f32_32x32x16_bf16 v[160:175], v[208:211], v[196:199], v[160:175]\n\t" \
"ds_read_b64_tr_b16 v[240:241], v43 offset:4096\n\t" \
"s_waitcnt lgkmcnt(11)\n\t" \
"v_mfma_f32_32x32x16_bf16 v[112:127], v[212:215], v[192:195], v[112:127]\n\t" \
"ds_read_b64_tr_b16 v[242:243], v44 offset:5120\n\t" \
"v_mfma_f32_32x32x16_bf16 v[176:191], v[212:215], v[196:199], v[176:191]\n\t" \
"ds_read_b64_tr_b16 v[244:245], v45 offset:4096\n\t" \
"s_waitcnt lgkmcnt(11)\n\t" \
"v_mfma_f32_32x32x16_bf16 a[0:15], v[216:219], v[192:195], a[0:15]\n\t" \
"ds_read_b64_tr_b16 v[246:247], v46 offset:5120\n\t" \
"ds_read_b64_tr_b16 v[248:249], v22 offset:2048\n\t" \
"v_mfma_f32_32x32x16_bf16 a[16:31], v[216:219], v[196:199], a[16:31]\n\t" \
"ds_read_b64_tr_b16 v[250:251], v22 offset:2560\n\t" \
"s_waitcnt lgkmcnt(12)\n\t" \
"v_mfma_f32_32x32x16_bf16 a[32:47], v[192:195], v[220:223], a[32:47]\n\t" \
"ds_read_b64_tr_b16 v[252:253], v23 offset:2048\n\t" \
"v_mfma_f32_32x32x16_bf16 a[48:63], v[196:199], v[220:223], a[48:63]\n\t" \
"ds_read_b64_tr_b16 v[254:255], v23 offset:2560\n\t" \
"s_waitcnt lgkmcnt(11)\n\t" \
"v_mfma_f32_32x32x16_bf16 v[64:79], v[232:235], v[224:227], v[64:79]\n\t" \
"ds_read_b64_tr_b16 v[200:201], v47 offset:0\n\t" \
"s_add_u32 m0, s70, 156672\n\t" \
"s_mov_b32 s55, 1376256\n\t" \
"buffer_load_dwordx4 %23, %15, s55 offen lds\n\t" \
"s_waitcnt lgkmcnt(11)\n\t" \
"v_mfma_f32_32x32x16_bf16 v[128:143], v[232:235], v[228:231], v[128:143]\n\t" \
"ds_read_b64_tr_b16 v[202:203], v48 offset:1024\n\t" \
"s_waitcnt lgkmcnt(10)\n\t" \
"v_mfma_f32_32x32x16_bf16 v[80:95], v[236:239], v[224:227], v[80:95]\n\t" \
"ds_read_b128 v[192:195], v20 offset:0\n\t" \
"v_mfma_f32_32x32x16_bf16 v[144:159], v[236:239], v[228:231], v[144:159]\n\t" \
"ds_read_b128 v[196:199], v20 offset:4096\n\t" \
"s_waitcnt lgkmcnt(10)\n\t" \
"v_mfma_f32_32x32x16_bf16 v[96:111], v[240:243], v[224:227], v[96:111]\n\t" \
"ds_read_b64_tr_b16 v[204:205], v49 offset:0\n\t" \
"ds_read_b64_tr_b16 v[206:207], v50 offset:1024\n\t" \
"v_mfma_f32_32x32x16_bf16 v[160:175], v[240:243], v[228:231], v[160:175]\n\t" \
"ds_read_b64_tr_b16 v[208:209], v51 offset:0\n\t" \
"s_waitcnt lgkmcnt(11)\n\t" \
"v_mfma_f32_32x32x16_bf16 v[112:127], v[244:247], v[224:227], v[112:127]\n\t" \
"ds_read_b64_tr_b16 v[210:211], v52 offset:1024\n\t" \
"v_mfma_f32_32x32x16_bf16 v[176:191], v[244:247], v[228:231], v[176:191]\n\t" \
"ds_read_b64_tr_b16 v[212:213], v53 offset:0\n\t" \
"s_waitcnt lgkmcnt(11)\n\t" \
"v_mfma_f32_32x32x16_bf16 a[0:15], v[248:251], v[224:227], a[0:15]\n\t" \
"ds_read_b64_tr_b16 v[214:215], v54 offset:1024\n\t" \
"ds_read_b64_tr_b16 v[216:217], v22 offset:4096\n\t" \
"v_mfma_f32_32x32x16_bf16 a[16:31], v[248:251], v[228:231], a[16:31]\n\t" \
"ds_read_b64_tr_b16 v[218:219], v22 offset:4608\n\t" \
"s_waitcnt lgkmcnt(12)\n\t" \
"v_mfma_f32_32x32x16_bf16 a[32:47], v[224:227], v[252:255], a[32:47]\n\t" \
"ds_read_b64_tr_b16 v[220:221], v23 offset:4096\n\t" \
"v_mfma_f32_32x32x16_bf16 a[48:63], v[228:231], v[252:255], a[48:63]\n\t" \
"ds_read_b64_tr_b16 v[222:223], v23 offset:4608\n\t" \
"s_waitcnt lgkmcnt(11)\n\t" \
"v_mfma_f32_32x32x16_bf16 v[64:79], v[200:203], v[192:195], v[64:79]\n\t" \
"ds_read_b64_tr_b16 v[232:233], v47 offset:4096\n\t" \
"ds_read_b64_tr_b16 v[234:235], v48 offset:5120\n\t" \
"s_waitcnt lgkmcnt(12)\n\t" \
"v_mfma_f32_32x32x16_bf16 v[128:143], v[200:203], v[196:199], v[128:143]\n\t" \
"ds_read_b128 v[224:227], v21 offset:0\n\t" \
"s_waitcnt lgkmcnt(11)\n\t" \
"v_mfma_f32_32x32x16_bf16 v[80:95], v[204:207], v[192:195], v[80:95]\n\t" \
"ds_read_b128 v[228:231], v21 offset:4096\n\t" \
"v_mfma_f32_32x32x16_bf16 v[144:159], v[204:207], v[196:199], v[144:159]\n\t" \
"ds_read_b64_tr_b16 v[236:237], v49 offset:4096\n\t" \
"s_waitcnt lgkmcnt(11)\n\t" \
"v_mfma_f32_32x32x16_bf16 v[96:111], v[208:211], v[192:195], v[96:111]\n\t" \
"ds_read_b64_tr_b16 v[238:239], v50 offset:5120\n\t" \
"v_mfma_f32_32x32x16_bf16 v[160:175], v[208:211], v[196:199], v[160:175]\n\t" \
"ds_read_b64_tr_b16 v[240:241], v51 offset:4096\n\t" \
"s_waitcnt lgkmcnt(11)\n\t" \
"v_mfma_f32_32x32x16_bf16 v[112:127], v[212:215], v[192:195], v[112:127]\n\t" \
"ds_read_b64_tr_b16 v[242:243], v52 offset:5120\n\t" \
"ds_read_b64_tr_b16 v[244:245], v53 offset:4096\n\t" \
"v_mfma_f32_32x32x16_bf16 v[176:191], v[212:215], v[196:199], v[176:191]\n\t" \
"ds_read_b64_tr_b16 v[246:247], v54 offset:5120\n\t" \
"s_waitcnt lgkmcnt(12)\n\t" \
"v_mfma_f32_32x32x16_bf16 a[0:15], v[216:219], v[192:195], a[0:15]\n\t" \
"ds_read_b64_tr_b16 v[248:249], v22 offset:6144\n\t" \
"v_mfma_f32_32x32x16_bf16 a[16:31], v[216:219], v[196:199], a[16:31]\n\t" \
"ds_read_b64_tr_b16 v[250:251], v22 offset:6656\n\t" \
"s_waitcnt lgkmcnt(12)\n\t" \
"v_mfma_f32_32x32x16_bf16 a[32:47], v[192:195], v[220:223], a[32:47]\n\t" \
"ds_read_b64_tr_b16 v[252:253], v23 offset:6144\n\t" \
"v_mfma_f32_32x32x16_bf16 a[48:63], v[196:199], v[220:223], a[48:63]\n\t" \
"ds_read_b64_tr_b16 v[254:255], v23 offset:6656\n\t" \
"s_waitcnt lgkmcnt(11)\n\t" \
"v_mfma_f32_32x32x16_bf16 v[64:79], v[232:235], v[224:227], v[64:79]\n\t" \
"s_waitcnt lgkmcnt(0)\n\t" \
"s_waitcnt vmcnt(0)\n\t" \
"s_barrier\n\t" \
"v_mov_b32 v35, 64\n\t" \
"buffer_load_dword v35, v35, %0, s79 offen sc1\n\t" \
"v_mfma_f32_32x32x16_bf16 v[128:143], v[232:235], v[228:231], v[128:143]\n\t" \
"s_add_u32 s56, s75, 13\n\t" \
"s_sub_u32 s57, s56, 20\n\t" \
"s_cmp_ge_u32 s56, 20\n\t" \
"s_cselect_b32 s56, s57, s56\n\t" \
"s_lshl_b32 s56, s56, 13\n\t" \
"s_sub_u32 s57, s56, s83\n\t" \
"s_mov_b32 s83, s56\n\t" \
"v_add_u32 v18, s57, v18\n\t" \
"v_add_u32 v19, s57, v19\n\t" \
"v_add_u32 v20, s57, v20\n\t" \
"v_mfma_f32_32x32x16_bf16 v[80:95], v[236:239], v[224:227], v[80:95]\n\t" \
"v_add_u32 v21, s57, v21\n\t" \
"v_add_u32 v39, 0xe000, v39\n\t" \
"v_add_u32 v40, 0xe000, v40\n\t" \
"v_mfma_f32_32x32x16_bf16 v[144:159], v[236:239], v[228:231], v[144:159]\n\t" \
"v_add_u32 v41, 0xe000, v41\n\t" \
"v_add_u32 v42, 0xe000, v42\n\t" \
"v_add_u32 v43, 0xe000, v43\n\t" \
"v_add_u32 v44, 0xe000, v44\n\t" \
"v_mfma_f32_32x32x16_bf16 v[96:111], v[240:243], v[224:227], v[96:111]\n\t" \
"v_add_u32 v45, 0xe000, v45\n\t" \
"v_add_u32 v46, 0xe000, v46\n\t" \
"v_add_u32 v47, 0xe000, v47\n\t" \
"v_mfma_f32_32x32x16_bf16 v[160:175], v[240:243], v[228:231], v[160:175]\n\t" \
"v_add_u32 v48, 0xe000, v48\n\t" \
"v_add_u32 v49, 0xe000, v49\n\t" \
"v_add_u32 v50, 0xe000, v50\n\t" \
"v_mfma_f32_32x32x16_bf16 v[112:127], v[244:247], v[224:227], v[112:127]\n\t" \
"v_add_u32 v51, 0xe000, v51\n\t" \
"v_add_u32 v52, 0xe000, v52\n\t" \
"v_add_u32 v53, 0xe000, v53\n\t" \
"v_add_u32 v54, 0xe000, v54\n\t" \
"v_mfma_f32_32x32x16_bf16 v[176:191], v[244:247], v[228:231], v[176:191]\n\t" \
"v_add_u32 v22, 0xe000, v22\n\t" \
"v_add_u32 v23, 0xe000, v23\n\t" \
"ds_read_b64_tr_b16 v[200:201], v39 offset:0\n\t" \
"v_mfma_f32_32x32x16_bf16 a[0:15], v[248:251], v[224:227], a[0:15]\n\t" \
"ds_read_b64_tr_b16 v[202:203], v40 offset:1024\n\t" \
"ds_read_b128 v[192:195], v18 offset:0\n\t" \
"ds_read_b128 v[196:199], v18 offset:4096\n\t" \
"v_mfma_f32_32x32x16_bf16 a[16:31], v[248:251], v[228:231], a[16:31]\n\t" \
"ds_read_b64_tr_b16 v[204:205], v41 offset:0\n\t" \
"ds_read_b64_tr_b16 v[206:207], v42 offset:1024\n\t" \
"ds_read_b64_tr_b16 v[208:209], v43 offset:0\n\t" \
"ds_read_b64_tr_b16 v[210:211], v44 offset:1024\n\t" \
"v_mfma_f32_32x32x16_bf16 a[32:47], v[224:227], v[252:255], a[32:47]\n\t" \
"ds_read_b64_tr_b16 v[212:213], v45 offset:0\n\t" \
"ds_read_b64_tr_b16 v[214:215], v46 offset:1024\n\t" \
"ds_read_b64_tr_b16 v[216:217], v22 offset:0\n\t" \
"v_mfma_f32_32x32x16_bf16 a[48:63], v[228:231], v[252:255], a[48:63]\n\t" \
"ds_read_b64_tr_b16 v[218:219], v22 offset:512\n\t" \
"ds_read_b64_tr_b16 v[220:221], v23 offset:0\n\t" \
"ds_read_b64_tr_b16 v[222:223], v23 offset:512\n\t" \
"s_waitcnt lgkmcnt(11)\n\t" \
"v_mfma_f32_32x32x16_bf16 v[64:79], v[200:203], v[192:195], v[64:79]\n\t" \
"ds_read_b64_tr_b16 v[232:233], v39 offset:4096\n\t" \
"ds_read_b64_tr_b16 v[234:235], v40 offset:5120\n\t" \
"s_waitcnt lgkmcnt(12)\n\t" \
"v_mfma_f32_32x32x16_bf16 v[128:143], v[200:203], v[196:199], v[128:143]\n\t" \
"ds_read_b128 v[224:227], v19 offset:0\n\t" \
"s_waitcnt lgkmcnt(11)\n\t" \
"v_mfma_f32_32x32x16_bf16 v[80:95], v[204:207], v[192:195], v[80:95]\n\t" \
"ds_read_b128 v[228:231], v19 offset:4096\n\t" \
"v_mfma_f32_32x32x16_bf16 v[144:159], v[204:207], v[196:199], v[144:159]\n\t" \
"ds_read_b64_tr_b16 v[236:237], v41 offset:4096\n\t" \
"s_waitcnt lgkmcnt(11)\n\t" \
"v_mfma_f32_32x32x16_bf16 v[96:111], v[208:211], v[192:195], v[96:111]\n\t" \
"ds_read_b64_tr_b16 v[238:239], v42 offset:5120\n\t" \
"v_mfma_f32_32x32x16_bf16 v[160:175], v[208:211], v[196:199], v[160:175]\n\t" \
"ds_read_b64_tr_b16 v[240:241], v43 offset:4096\n\t" \
"s_waitcnt lgkmcnt(11)\n\t" \
"v_mfma_f32_32x32x16_bf16 v[112:127], v[212:215], v[192:195], v[112:127]\n\t" \
"ds_read_b64_tr_b16 v[242:243], v44 offset:5120\n\t" \
"ds_read_b64_tr_b16 v[244:245], v45 offset:4096\n\t" \
"v_mfma_f32_32x32x16_bf16 v[176:191], v[212:215], v[196:199], v[176:191]\n\t" \
"ds_read_b64_tr_b16 v[246:247], v46 offset:5120\n\t" \
"s_waitcnt lgkmcnt(12)\n\t" \
"v_mfma_f32_32x32x16_bf16 a[0:15], v[216:219], v[192:195], a[0:15]\n\t" \
"ds_read_b64_tr_b16 v[248:249], v22 offset:2048\n\t" \
"v_mfma_f32_32x32x16_bf16 a[16:31], v[216:219], v[196:199], a[16:31]\n\t" \
"ds_read_b64_tr_b16 v[250:251], v22 offset:2560\n\t" \
"s_waitcnt lgkmcnt(12)\n\t" \
"v_mfma_f32_32x32x16_bf16 a[32:47], v[192:195], v[220:223], a[32:47]\n\t" \
"ds_read_b64_tr_b16 v[252:253], v23 offset:2048\n\t" \
"v_mfma_f32_32x32x16_bf16 a[48:63], v[196:199], v[220:223], a[48:63]\n\t" \
"ds_read_b64_tr_b16 v[254:255], v23 offset:2560\n\t" \
"s_waitcnt lgkmcnt(11)\n\t" \
"v_mfma_f32_32x32x16_bf16 v[64:79], v[232:235], v[224:227], v[64:79]\n\t" \
"ds_read_b64_tr_b16 v[200:201], v47 offset:0\n\t" \
"ds_read_b64_tr_b16 v[202:203], v48 offset:1024\n\t" \
"s_waitcnt lgkmcnt(12)\n\t" \
"v_mfma_f32_32x32x16_bf16 v[128:143], v[232:235], v[228:231], v[128:143]\n\t" \
"ds_read_b128 v[192:195], v20 offset:0\n\t" \
"s_waitcnt lgkmcnt(11)\n\t" \
"v_mfma_f32_32x32x16_bf16 v[80:95], v[236:239], v[224:227], v[80:95]\n\t" \
"ds_read_b128 v[196:199], v20 offset:4096\n\t" \
"v_mfma_f32_32x32x16_bf16 v[144:159], v[236:239], v[228:231], v[144:159]\n\t" \
"ds_read_b64_tr_b16 v[204:205], v49 offset:0\n\t" \
"s_waitcnt lgkmcnt(11)\n\t" \
"v_mfma_f32_32x32x16_bf16 v[96:111], v[240:243], v[224:227], v[96:111]\n\t" \
"ds_read_b64_tr_b16 v[206:207], v50 offset:1024\n\t" \
"v_mfma_f32_32x32x16_bf16 v[160:175], v[240:243], v[228:231], v[160:175]\n\t" \
"ds_read_b64_tr_b16 v[208:209], v51 offset:0\n\t" \
"s_waitcnt lgkmcnt(11)\n\t" \
"v_mfma_f32_32x32x16_bf16 v[112:127], v[244:247], v[224:227], v[112:127]\n\t" \
"ds_read_b64_tr_b16 v[210:211], v52 offset:1024\n\t" \
"ds_read_b64_tr_b16 v[212:213], v53 offset:0\n\t" \
"v_mfma_f32_32x32x16_bf16 v[176:191], v[244:247], v[228:231], v[176:191]\n\t" \
"ds_read_b64_tr_b16 v[214:215], v54 offset:1024\n\t" \
"s_waitcnt lgkmcnt(12)\n\t" \
"v_mfma_f32_32x32x16_bf16 a[0:15], v[248:251], v[224:227], a[0:15]\n\t" \
"ds_read_b64_tr_b16 v[216:217], v22 offset:4096\n\t" \
"v_mfma_f32_32x32x16_bf16 a[16:31], v[248:251], v[228:231], a[16:31]\n\t" \
"ds_read_b64_tr_b16 v[218:219], v22 offset:4608\n\t" \
"s_waitcnt lgkmcnt(12)\n\t" \
"v_mfma_f32_32x32x16_bf16 a[32:47], v[224:227], v[252:255], a[32:47]\n\t" \
"ds_read_b64_tr_b16 v[220:221], v23 offset:4096\n\t" \
"v_mfma_f32_32x32x16_bf16 a[48:63], v[228:231], v[252:255], a[48:63]\n\t" \
"ds_read_b64_tr_b16 v[222:223], v23 offset:4608\n\t" \
"s_waitcnt lgkmcnt(11)\n\t" \
"v_mfma_f32_32x32x16_bf16 v[64:79], v[200:203], v[192:195], v[64:79]\n\t" \
"ds_read_b64_tr_b16 v[232:233], v47 offset:4096\n\t" \
"ds_read_b64_tr_b16 v[234:235], v48 offset:5120\n\t" \
"s_waitcnt lgkmcnt(12)\n\t" \
"v_mfma_f32_32x32x16_bf16 v[128:143], v[200:203], v[196:199], v[128:143]\n\t" \
"ds_read_b128 v[224:227], v21 offset:0\n\t" \
"s_waitcnt lgkmcnt(11)\n\t" \
"v_mfma_f32_32x32x16_bf16 v[80:95], v[204:207], v[192:195], v[80:95]\n\t" \
"ds_read_b128 v[228:231], v21 offset:4096\n\t" \
"v_mfma_f32_32x32x16_bf16 v[144:159], v[204:207], v[196:199], v[144:159]\n\t" \
"ds_read_b64_tr_b16 v[236:237], v49 offset:4096\n\t" \
"s_waitcnt lgkmcnt(11)\n\t" \
"v_mfma_f32_32x32x16_bf16 v[96:111], v[208:211], v[192:195], v[96:111]\n\t" \
"ds_read_b64_tr_b16 v[238:239], v50 offset:5120\n\t" \
"v_mfma_f32_32x32x16_bf16 v[160:175], v[208:211], v[196:199], v[160:175]\n\t" \
"ds_read_b64_tr_b16 v[240:241], v51 offset:4096\n\t" \
"s_waitcnt lgkmcnt(11)\n\t" \
"v_mfma_f32_32x32x16_bf16 v[112:127], v[212:215], v[192:195], v[112:127]\n\t" \
"ds_read_b64_tr_b16 v[242:243], v52 offset:5120\n\t" \
"ds_read_b64_tr_b16 v[244:245], v53 offset:4096\n\t" \
"v_mfma_f32_32x32x16_bf16 v[176:191], v[212:215], v[196:199], v[176:191]\n\t" \
"ds_read_b64_tr_b16 v[246:247], v54 offset:5120\n\t" \
"s_waitcnt lgkmcnt(12)\n\t" \
"v_mfma_f32_32x32x16_bf16 a[0:15], v[216:219], v[192:195], a[0:15]\n\t" \
"ds_read_b64_tr_b16 v[248:249], v22 offset:6144\n\t" \
"v_mfma_f32_32x32x16_bf16 a[16:31], v[216:219], v[196:199], a[16:31]\n\t" \
"ds_read_b64_tr_b16 v[250:251], v22 offset:6656\n\t" \
"s_waitcnt lgkmcnt(12)\n\t" \
"v_mfma_f32_32x32x16_bf16 a[32:47], v[192:195], v[220:223], a[32:47]\n\t" \
"ds_read_b64_tr_b16 v[252:253], v23 offset:6144\n\t" \
"v_mfma_f32_32x32x16_bf16 a[48:63], v[196:199], v[220:223], a[48:63]\n\t" \
"ds_read_b64_tr_b16 v[254:255], v23 offset:6656\n\t" \
"s_waitcnt lgkmcnt(11)\n\t" \
"v_mfma_f32_32x32x16_bf16 v[64:79], v[232:235], v[224:227], v[64:79]\n\t" \
"s_waitcnt lgkmcnt(0)\n\t" \
"v_mfma_f32_32x32x16_bf16 v[128:143], v[232:235], v[228:231], v[128:143]\n\t" \
"v_mfma_f32_32x32x16_bf16 v[80:95], v[236:239], v[224:227], v[80:95]\n\t" \
"v_mfma_f32_32x32x16_bf16 v[144:159], v[236:239], v[228:231], v[144:159]\n\t" \
"v_mfma_f32_32x32x16_bf16 v[96:111], v[240:243], v[224:227], v[96:111]\n\t" \
"v_mfma_f32_32x32x16_bf16 v[160:175], v[240:243], v[228:231], v[160:175]\n\t" \
"v_mfma_f32_32x32x16_bf16 v[112:127], v[244:247], v[224:227], v[112:127]\n\t" \
"v_mfma_f32_32x32x16_bf16 v[176:191], v[244:247], v[228:231], v[176:191]\n\t" \
"v_mfma_f32_32x32x16_bf16 a[0:15], v[248:251], v[224:227], a[0:15]\n\t" \
"v_mfma_f32_32x32x16_bf16 a[16:31], v[248:251], v[228:231], a[16:31]\n\t" \
"v_mfma_f32_32x32x16_bf16 a[32:47], v[224:227], v[252:255], a[32:47]\n\t" \
"v_mfma_f32_32x32x16_bf16 a[48:63], v[228:231], v[252:255], a[48:63]\n\t" \
"s_barrier\n\t" \
"s_mov_b32 s58, 0x100000\n\t" \
"s_waitcnt vmcnt(0)\n\t" \
"KPOLL_%=:\n\t" \
"v_readfirstlane_b32 s57, v35\n\t" \
"s_nop 1\n\t" \
"s_cmp_ge_u32 s57, 8\n\t" \
"s_cbranch_scc1 KPOLLED_%=\n\t" \
"s_sub_u32 s58, s58, 1\n\t" \
"s_cmp_eq_u32 s58, 0\n\t" \
"s_cbranch_scc1 KPOLLED_%=\n\t" \
"s_sleep 2\n\t" \
"v_mov_b32 v35, 64\n\t" \
"buffer_load_dword v35, v35, %0, s79 offen sc1\n\t" \
"s_waitcnt vmcnt(0)\n\t" \
"s_branch KPOLL_%=\n\t" \
"KPOLLED_%=:\n\t" \
"s_mov_b32 m0, s48\n\t" \
"s_nop 0\n\t" \
"buffer_load_dwordx4 v36, %0, s42 offen sc1 lds\n\t" \
"buffer_load_dwordx4 v36, %0, s42 offen offset:1024 sc1 lds\n\t" \
"buffer_load_dwordx4 v36, %0, s42 offen offset:2048 sc1 lds\n\t" \
"buffer_load_dwordx4 v36, %0, s42 offen offset:3072 sc1 lds\n\t" \
"s_add_u32 m0, s48, 4096\n\t" \
"s_add_u32 s54, s42, 4096\n\t" \
"buffer_load_dwordx4 v36, %0, s54 offen sc1 lds\n\t" \
"buffer_load_dwordx4 v36, %0, s54 offen offset:1024 sc1 lds\n\t" \
"buffer_load_dwordx4 v36, %0, s54 offen offset:2048 sc1 lds\n\t" \
"buffer_load_dwordx4 v36, %0, s54 offen offset:3072 sc1 lds\n\t" \
"s_xor_b32 s48, s48, 0x8000\n\t" \
"s_add_u32 s44, s44, 1\n\t" \
"s_add_u32 s42, s42, 0x8000\n\t" \
"s_cmp_eq_u32 s44, s45\n\t" \
"s_cselect_b32 s44, 0, s44\n\t" \
"s_cselect_b32 s42, %1, s42\n\t" \
"s_cselect_b32 s45, %6, s45\n\t" \
"s_and_b32 s57, s75, 1\n\t" \
"s_lshl_b32 s57, s57, 14\n\t" \
"s_lshr_b32 s56, s75, 1\n\t" \
"s_lshl_b32 s56, s56, 16\n\t" \
"s_add_u32 s56, s56, s57\n\t" \
"s_add_u32 s56, s56, %8\n\t" \
"s_add_u32 s56, s56, 0x10000\n\t" \
"v_add_u32 v39, s56, v36\n\t" \
"s_mov_b32 s62, 0x3e0293ee\n\t" \
"s_mov_b32 s63, 0x3e0293ee\n\t" \
"v_pk_mul_f32 v[64:65], v[64:65], s[62:63]\n\t" \
"v_pk_mul_f32 v[66:67], v[66:67], s[62:63]\n\t" \
"v_pk_mul_f32 v[68:69], v[68:69], s[62:63]\n\t" \
"v_pk_mul_f32 v[70:71], v[70:71], s[62:63]\n\t" \
"v_cvt_pk_bf16_f32 v192, v64, v65\n\t" \
"v_cvt_pk_bf16_f32 v193, v66, v67\n\t" \
"v_cvt_pk_bf16_f32 v194, v68, v69\n\t" \
"v_cvt_pk_bf16_f32 v195, v70, v71\n\t" \
"ds_write_b128 v39, v[192:195] offset:0\n\t" \
"v_pk_mul_f32 v[72:73], v[72:73], s[62:63]\n\t" \
"v_pk_mul_f32 v[74:75], v[74:75], s[62:63]\n\t" \
"v_pk_mul_f32 v[76:77], v[76:77], s[62:63]\n\t" \
"v_pk_mul_f32 v[78:79], v[78:79], s[62:63]\n\t" \
"v_cvt_pk_bf16_f32 v196, v72, v73\n\t" \
"v_cvt_pk_bf16_f32 v197, v74, v75\n\t" \
"v_cvt_pk_bf16_f32 v198, v76, v77\n\t" \
"v_cvt_pk_bf16_f32 v199, v78, v79\n\t" \
"ds_write_b128 v39, v[196:199] offset:1024\n\t" \
"v_pk_mul_f32 v[80:81], v[80:81], s[62:63]\n\t" \
"v_pk_mul_f32 v[82:83], v[82:83], s[62:63]\n\t" \
"v_pk_mul_f32 v[84:85], v[84:85], s[62:63]\n\t" \
"v_pk_mul_f32 v[86:87], v[86:87], s[62:63]\n\t" \
"v_cvt_pk_bf16_f32 v200, v80, v81\n\t" \
"v_cvt_pk_bf16_f32 v201, v82, v83\n\t" \
"v_cvt_pk_bf16_f32 v202, v84, v85\n\t" \
"v_cvt_pk_bf16_f32 v203, v86, v87\n\t" \
"ds_write_b128 v39, v[200:203] offset:2048\n\t" \
"v_pk_mul_f32 v[88:89], v[88:89], s[62:63]\n\t" \
"v_pk_mul_f32 v[90:91], v[90:91], s[62:63]\n\t" \
"v_pk_mul_f32 v[92:93], v[92:93], s[62:63]\n\t" \
"v_pk_mul_f32 v[94:95], v[94:95], s[62:63]\n\t" \
"v_cvt_pk_bf16_f32 v204, v88, v89\n\t" \
"v_cvt_pk_bf16_f32 v205, v90, v91\n\t" \
"v_cvt_pk_bf16_f32 v206, v92, v93\n\t" \
"v_cvt_pk_bf16_f32 v207, v94, v95\n\t" \
"ds_write_b128 v39, v[204:207] offset:3072\n\t" \
"v_pk_mul_f32 v[96:97], v[96:97], s[62:63]\n\t" \
"v_pk_mul_f32 v[98:99], v[98:99], s[62:63]\n\t" \
"v_pk_mul_f32 v[100:101], v[100:101], s[62:63]\n\t" \
"v_pk_mul_f32 v[102:103], v[102:103], s[62:63]\n\t" \
"v_cvt_pk_bf16_f32 v208, v96, v97\n\t" \
"v_cvt_pk_bf16_f32 v209, v98, v99\n\t" \
"v_cvt_pk_bf16_f32 v210, v100, v101\n\t" \
"v_cvt_pk_bf16_f32 v211, v102, v103\n\t" \
"ds_write_b128 v39, v[208:211] offset:4096\n\t" \
"v_pk_mul_f32 v[104:105], v[104:105], s[62:63]\n\t" \
"v_pk_mul_f32 v[106:107], v[106:107], s[62:63]\n\t" \
"v_pk_mul_f32 v[108:109], v[108:109], s[62:63]\n\t" \
"v_pk_mul_f32 v[110:111], v[110:111], s[62:63]\n\t" \
"v_cvt_pk_bf16_f32 v212, v104, v105\n\t" \
"v_cvt_pk_bf16_f32 v213, v106, v107\n\t" \
"v_cvt_pk_bf16_f32 v214, v108, v109\n\t" \
"v_cvt_pk_bf16_f32 v215, v110, v111\n\t" \
"ds_write_b128 v39, v[212:215] offset:5120\n\t" \
"v_pk_mul_f32 v[112:113], v[112:113], s[62:63]\n\t" \
"v_pk_mul_f32 v[114:115], v[114:115], s[62:63]\n\t" \
"v_pk_mul_f32 v[116:117], v[116:117], s[62:63]\n\t" \
"v_pk_mul_f32 v[118:119], v[118:119], s[62:63]\n\t" \
"v_cvt_pk_bf16_f32 v216, v112, v113\n\t" \
"v_cvt_pk_bf16_f32 v217, v114, v115\n\t" \
"v_cvt_pk_bf16_f32 v218, v116, v117\n\t" \
"v_cvt_pk_bf16_f32 v219, v118, v119\n\t" \
"ds_write_b128 v39, v[216:219] offset:6144\n\t" \
"v_pk_mul_f32 v[120:121], v[120:121], s[62:63]\n\t" \
"v_pk_mul_f32 v[122:123], v[122:123], s[62:63]\n\t" \
"v_pk_mul_f32 v[124:125], v[124:125], s[62:63]\n\t" \
"v_pk_mul_f32 v[126:127], v[126:127], s[62:63]\n\t" \
"v_cvt_pk_bf16_f32 v220, v120, v121\n\t" \
"v_cvt_pk_bf16_f32 v221, v122, v123\n\t" \
"v_cvt_pk_bf16_f32 v222, v124, v125\n\t" \
"v_cvt_pk_bf16_f32 v223, v126, v127\n\t" \
"ds_write_b128 v39, v[220:223] offset:7168\n\t" \
"v_pk_mul_f32 v[128:129], v[128:129], s[62:63]\n\t" \
"v_pk_mul_f32 v[130:131], v[130:131], s[62:63]\n\t" \
"v_pk_mul_f32 v[132:133], v[132:133], s[62:63]\n\t" \
"v_pk_mul_f32 v[134:135], v[134:135], s[62:63]\n\t" \
"v_cvt_pk_bf16_f32 v192, v128, v129\n\t" \
"v_cvt_pk_bf16_f32 v193, v130, v131\n\t" \
"v_cvt_pk_bf16_f32 v194, v132, v133\n\t" \
"v_cvt_pk_bf16_f32 v195, v134, v135\n\t" \
"ds_write_b128 v39, v[192:195] offset:8192\n\t" \
"v_pk_mul_f32 v[136:137], v[136:137], s[62:63]\n\t" \
"v_pk_mul_f32 v[138:139], v[138:139], s[62:63]\n\t" \
"v_pk_mul_f32 v[140:141], v[140:141], s[62:63]\n\t" \
"v_pk_mul_f32 v[142:143], v[142:143], s[62:63]\n\t" \
"v_cvt_pk_bf16_f32 v196, v136, v137\n\t" \
"v_cvt_pk_bf16_f32 v197, v138, v139\n\t" \
"v_cvt_pk_bf16_f32 v198, v140, v141\n\t" \
"v_cvt_pk_bf16_f32 v199, v142, v143\n\t" \
"ds_write_b128 v39, v[196:199] offset:9216\n\t" \
"v_pk_mul_f32 v[144:145], v[144:145], s[62:63]\n\t" \
"v_pk_mul_f32 v[146:147], v[146:147], s[62:63]\n\t" \
"v_pk_mul_f32 v[148:149], v[148:149], s[62:63]\n\t" \
"v_pk_mul_f32 v[150:151], v[150:151], s[62:63]\n\t" \
"v_cvt_pk_bf16_f32 v200, v144, v145\n\t" \
"v_cvt_pk_bf16_f32 v201, v146, v147\n\t" \
"v_cvt_pk_bf16_f32 v202, v148, v149\n\t" \
"v_cvt_pk_bf16_f32 v203, v150, v151\n\t" \
"ds_write_b128 v39, v[200:203] offset:10240\n\t" \
"v_pk_mul_f32 v[152:153], v[152:153], s[62:63]\n\t" \
"v_pk_mul_f32 v[154:155], v[154:155], s[62:63]\n\t" \
"v_pk_mul_f32 v[156:157], v[156:157], s[62:63]\n\t" \
"v_pk_mul_f32 v[158:159], v[158:159], s[62:63]\n\t" \
"v_cvt_pk_bf16_f32 v204, v152, v153\n\t" \
"v_cvt_pk_bf16_f32 v205, v154, v155\n\t" \
"v_cvt_pk_bf16_f32 v206, v156, v157\n\t" \
"v_cvt_pk_bf16_f32 v207, v158, v159\n\t" \
"ds_write_b128 v39, v[204:207] offset:11264\n\t" \
"v_pk_mul_f32 v[160:161], v[160:161], s[62:63]\n\t" \
"v_pk_mul_f32 v[162:163], v[162:163], s[62:63]\n\t" \
"v_pk_mul_f32 v[164:165], v[164:165], s[62:63]\n\t" \
"v_pk_mul_f32 v[166:167], v[166:167], s[62:63]\n\t" \
"v_cvt_pk_bf16_f32 v208, v160, v161\n\t" \
"v_cvt_pk_bf16_f32 v209, v162, v163\n\t" \
"v_cvt_pk_bf16_f32 v210, v164, v165\n\t" \
"v_cvt_pk_bf16_f32 v211, v166, v167\n\t" \
"ds_write_b128 v39, v[208:211] offset:12288\n\t" \
"v_pk_mul_f32 v[168:169], v[168:169], s[62:63]\n\t" \
"v_pk_mul_f32 v[170:171], v[170:171], s[62:63]\n\t" \
"v_pk_mul_f32 v[172:173], v[172:173], s[62:63]\n\t" \
"v_pk_mul_f32 v[174:175], v[174:175], s[62:63]\n\t" \
"v_cvt_pk_bf16_f32 v212, v168, v169\n\t" \
"v_cvt_pk_bf16_f32 v213, v170, v171\n\t" \
"v_cvt_pk_bf16_f32 v214, v172, v173\n\t" \
"v_cvt_pk_bf16_f32 v215, v174, v175\n\t" \
"ds_write_b128 v39, v[212:215] offset:13312\n\t" \
"v_pk_mul_f32 v[176:177], v[176:177], s[62:63]\n\t" \
"v_pk_mul_f32 v[178:179], v[178:179], s[62:63]\n\t" \
"v_pk_mul_f32 v[180:181], v[180:181], s[62:63]\n\t" \
"v_pk_mul_f32 v[182:183], v[182:183], s[62:63]\n\t" \
"v_cvt_pk_bf16_f32 v216, v176, v177\n\t" \
"v_cvt_pk_bf16_f32 v217, v178, v179\n\t" \
"v_cvt_pk_bf16_f32 v218, v180, v181\n\t" \
"v_cvt_pk_bf16_f32 v219, v182, v183\n\t" \
"ds_write_b128 v39, v[216:219] offset:14336\n\t" \
"v_pk_mul_f32 v[184:185], v[184:185], s[62:63]\n\t" \
"v_pk_mul_f32 v[186:187], v[186:187], s[62:63]\n\t" \
"v_pk_mul_f32 v[188:189], v[188:189], s[62:63]\n\t" \
"v_pk_mul_f32 v[190:191], v[190:191], s[62:63]\n\t" \
"v_cvt_pk_bf16_f32 v220, v184, v185\n\t" \
"v_cvt_pk_bf16_f32 v221, v186, v187\n\t" \
"v_cvt_pk_bf16_f32 v222, v188, v189\n\t" \
"v_cvt_pk_bf16_f32 v223, v190, v191\n\t" \
"s_waitcnt lgkmcnt(10)\n\t" \
"ds_write_b128 v39, v[220:223] offset:15360\n\t" \
"v_accvgpr_read_b32 v64, a0\n\t" \
"v_accvgpr_read_b32 v65, a1\n\t" \
"v_accvgpr_read_b32 v66, a2\n\t" \
"v_accvgpr_read_b32 v67, a3\n\t" \
"v_accvgpr_read_b32 v68, a4\n\t" \
"v_accvgpr_read_b32 v69, a5\n\t" \
"v_accvgpr_read_b32 v70, a6\n\t" \
"v_accvgpr_read_b32 v71, a7\n\t" \
"v_accvgpr_read_b32 v96, a32\n\t" \
"v_accvgpr_read_b32 v97, a33\n\t" \
"v_accvgpr_read_b32 v98, a34\n\t" \
"v_accvgpr_read_b32 v99, a35\n\t" \
"v_accvgpr_read_b32 v100, a36\n\t" \
"v_accvgpr_read_b32 v101, a37\n\t" \
"v_accvgpr_read_b32 v102, a38\n\t" \
"v_accvgpr_read_b32 v103, a39\n\t" \
"v_accvgpr_read_b32 v104, a40\n\t" \
"v_accvgpr_read_b32 v105, a41\n\t" \
"v_accvgpr_read_b32 v106, a42\n\t" \
"v_accvgpr_read_b32 v107, a43\n\t" \
"v_accvgpr_read_b32 v108, a44\n\t" \
"v_accvgpr_read_b32 v109, a45\n\t" \
"v_accvgpr_read_b32 v110, a46\n\t" \
"v_accvgpr_read_b32 v111, a47\n\t" \
"v_accvgpr_read_b32 v80, a16\n\t" \
"v_accvgpr_read_b32 v81, a17\n\t" \
"v_accvgpr_read_b32 v82, a18\n\t" \
"v_accvgpr_read_b32 v83, a19\n\t" \
"v_accvgpr_read_b32 v84, a20\n\t" \
"v_accvgpr_read_b32 v85, a21\n\t" \
"v_accvgpr_read_b32 v86, a22\n\t" \
"v_accvgpr_read_b32 v87, a23\n\t" \
"v_accvgpr_read_b32 v112, a48\n\t" \
"v_accvgpr_read_b32 v113, a49\n\t" \
"v_accvgpr_read_b32 v114, a50\n\t" \
"v_accvgpr_read_b32 v115, a51\n\t" \
"v_accvgpr_read_b32 v116, a52\n\t" \
"v_accvgpr_read_b32 v117, a53\n\t" \
"v_accvgpr_read_b32 v118, a54\n\t" \
"v_accvgpr_read_b32 v119, a55\n\t" \
"v_accvgpr_read_b32 v120, a56\n\t" \
"v_accvgpr_read_b32 v121, a57\n\t" \
"v_accvgpr_read_b32 v122, a58\n\t" \
"v_accvgpr_read_b32 v123, a59\n\t" \
"v_accvgpr_read_b32 v124, a60\n\t" \
"v_accvgpr_read_b32 v125, a61\n\t" \
"v_accvgpr_read_b32 v126, a62\n\t" \
"v_accvgpr_read_b32 v127, a63\n\t" \
"s_mov_b32 s58, 0x3fb8aa3b\n\t" \
"s_mov_b32 s59, 0x358637bd\n\t" \
"v_min_f32 v40, 0, v64\n\t" \
"v_mul_f32 v40, s58, v40\n\t" \
"v_exp_f32 v40, v40\n\t" \
"v_max_f32 v64, 0, v64\n\t" \
"v_add_f32 v64, v64, v40\n\t" \
"v_add_f32 v64, s59, v64\n\t" \
"v_min_f32 v41, 0, v65\n\t" \
"v_mul_f32 v41, s58, v41\n\t" \
"v_exp_f32 v41, v41\n\t" \
"v_max_f32 v65, 0, v65\n\t" \
"v_add_f32 v65, v65, v41\n\t" \
"v_add_f32 v65, s59, v65\n\t" \
"v_min_f32 v42, 0, v66\n\t" \
"v_mul_f32 v42, s58, v42\n\t" \
"v_exp_f32 v42, v42\n\t" \
"v_max_f32 v66, 0, v66\n\t" \
"v_add_f32 v66, v66, v42\n\t" \
"v_add_f32 v66, s59, v66\n\t" \
"v_min_f32 v43, 0, v67\n\t" \
"v_mul_f32 v43, s58, v43\n\t" \
"v_exp_f32 v43, v43\n\t" \
"v_max_f32 v67, 0, v67\n\t" \
"v_add_f32 v67, v67, v43\n\t" \
"v_add_f32 v67, s59, v67\n\t" \
"v_min_f32 v40, 0, v68\n\t" \
"v_mul_f32 v40, s58, v40\n\t" \
"v_exp_f32 v40, v40\n\t" \
"v_max_f32 v68, 0, v68\n\t" \
"v_add_f32 v68, v68, v40\n\t" \
"v_add_f32 v68, s59, v68\n\t" \
"v_min_f32 v41, 0, v69\n\t" \
"v_mul_f32 v41, s58, v41\n\t" \
"v_exp_f32 v41, v41\n\t" \
"v_max_f32 v69, 0, v69\n\t" \
"v_add_f32 v69, v69, v41\n\t" \
"v_add_f32 v69, s59, v69\n\t" \
"v_min_f32 v42, 0, v70\n\t" \
"v_mul_f32 v42, s58, v42\n\t" \
"v_exp_f32 v42, v42\n\t" \
"v_max_f32 v70, 0, v70\n\t" \
"v_add_f32 v70, v70, v42\n\t" \
"v_add_f32 v70, s59, v70\n\t" \
"v_min_f32 v43, 0, v71\n\t" \
"v_mul_f32 v43, s58, v43\n\t" \
"v_exp_f32 v43, v43\n\t" \
"v_max_f32 v71, 0, v71\n\t" \
"v_add_f32 v71, v71, v43\n\t" \
"v_add_f32 v71, s59, v71\n\t" \
"v_cvt_pk_bf16_f32 v72, v64, v65\n\t" \
"v_cvt_pk_bf16_f32 v73, v66, v67\n\t" \
"v_cvt_pk_bf16_f32 v74, v68, v69\n\t" \
"v_cvt_pk_bf16_f32 v75, v70, v71\n\t" \
"buffer_store_dwordx4 v[72:75], v36, %0, %10 offen\n\t" \
"v_min_f32 v40, 0, v80\n\t" \
"v_mul_f32 v40, s58, v40\n\t" \
"v_exp_f32 v40, v40\n\t" \
"v_max_f32 v80, 0, v80\n\t" \
"v_add_f32 v80, v80, v40\n\t" \
"v_add_f32 v80, s59, v80\n\t" \
"v_min_f32 v41, 0, v81\n\t" \
"v_mul_f32 v41, s58, v41\n\t" \
"v_exp_f32 v41, v41\n\t" \
"v_max_f32 v81, 0, v81\n\t" \
"v_add_f32 v81, v81, v41\n\t" \
"v_add_f32 v81, s59, v81\n\t" \
"v_min_f32 v42, 0, v82\n\t" \
"v_mul_f32 v42, s58, v42\n\t" \
"v_exp_f32 v42, v42\n\t" \
"v_max_f32 v82, 0, v82\n\t" \
"v_add_f32 v82, v82, v42\n\t" \
"v_add_f32 v82, s59, v82\n\t" \
"v_min_f32 v43, 0, v83\n\t" \
"v_mul_f32 v43, s58, v43\n\t" \
"v_exp_f32 v43, v43\n\t" \
"v_max_f32 v83, 0, v83\n\t" \
"v_add_f32 v83, v83, v43\n\t" \
"v_add_f32 v83, s59, v83\n\t" \
"v_min_f32 v40, 0, v84\n\t" \
"v_mul_f32 v40, s58, v40\n\t" \
"v_exp_f32 v40, v40\n\t" \
"v_max_f32 v84, 0, v84\n\t" \
"v_add_f32 v84, v84, v40\n\t" \
"v_add_f32 v84, s59, v84\n\t" \
"v_min_f32 v41, 0, v85\n\t" \
"v_mul_f32 v41, s58, v41\n\t" \
"v_exp_f32 v41, v41\n\t" \
"v_max_f32 v85, 0, v85\n\t" \
"v_add_f32 v85, v85, v41\n\t" \
"v_add_f32 v85, s59, v85\n\t" \
"v_min_f32 v42, 0, v86\n\t" \
"v_mul_f32 v42, s58, v42\n\t" \
"v_exp_f32 v42, v42\n\t" \
"v_max_f32 v86, 0, v86\n\t" \
"v_add_f32 v86, v86, v42\n\t" \
"v_add_f32 v86, s59, v86\n\t" \
"v_min_f32 v43, 0, v87\n\t" \
"v_mul_f32 v43, s58, v43\n\t" \
"v_exp_f32 v43, v43\n\t" \
"v_max_f32 v87, 0, v87\n\t" \
"v_add_f32 v87, v87, v43\n\t" \
"v_add_f32 v87, s59, v87\n\t" \
"v_cvt_pk_bf16_f32 v88, v80, v81\n\t" \
"v_cvt_pk_bf16_f32 v89, v82, v83\n\t" \
"v_cvt_pk_bf16_f32 v90, v84, v85\n\t" \
"v_cvt_pk_bf16_f32 v91, v86, v87\n\t" \
"buffer_store_dwordx4 v[88:91], v36, %0, %10 offen offset:1024\n\t" \
"s_mov_b32 s62, 0x0000ffff\n\t" \
"s_mov_b32 s63, 0x0000ffff\n\t" \
"v_mov_b32 v48, 0x3f803f80\n\t" \
"v_cvt_pk_bf16_f32 v164, v96, v97\n\t" \
"v_cndmask_b32 v164, v164, v48, s[62:63]\n\t" \
"v_cvt_pk_bf16_f32 v165, v98, v99\n\t" \
"v_cndmask_b32 v165, v165, v48, s[62:63]\n\t" \
"v_cvt_pk_bf16_f32 v166, v100, v101\n\t" \
"v_cndmask_b32 v166, v166, v48, s[62:63]\n\t" \
"v_cvt_pk_bf16_f32 v167, v102, v103\n\t" \
"v_cndmask_b32 v167, v167, v48, s[62:63]\n\t" \
"v_min_f32 v40, 0, v96\n\t" \
"v_mul_f32 v40, s58, v40\n\t" \
"v_exp_f32 v40, v40\n\t" \
"v_max_f32 v96, 0, v96\n\t" \
"v_add_f32 v96, v96, v40\n\t" \
"v_add_f32 v96, s59, v96\n\t" \
"v_min_f32 v41, 0, v97\n\t" \
"v_mul_f32 v41, s58, v41\n\t" \
"v_exp_f32 v41, v41\n\t" \
"v_max_f32 v97, 0, v97\n\t" \
"v_add_f32 v97, v97, v41\n\t" \
"v_add_f32 v97, s59, v97\n\t" \
"v_min_f32 v42, 0, v98\n\t" \
"v_mul_f32 v42, s58, v42\n\t" \
"v_exp_f32 v42, v42\n\t" \
"v_max_f32 v98, 0, v98\n\t" \
"v_add_f32 v98, v98, v42\n\t" \
"v_add_f32 v98, s59, v98\n\t" \
"v_min_f32 v43, 0, v99\n\t" \
"v_mul_f32 v43, s58, v43\n\t" \
"v_exp_f32 v43, v43\n\t" \
"v_max_f32 v99, 0, v99\n\t" \
"v_add_f32 v99, v99, v43\n\t" \
"v_add_f32 v99, s59, v99\n\t" \
"v_min_f32 v40, 0, v100\n\t" \
"v_mul_f32 v40, s58, v40\n\t" \
"v_exp_f32 v40, v40\n\t" \
"v_max_f32 v100, 0, v100\n\t" \
"v_add_f32 v100, v100, v40\n\t" \
"v_add_f32 v100, s59, v100\n\t" \
"v_min_f32 v41, 0, v101\n\t" \
"v_mul_f32 v41, s58, v41\n\t" \
"v_exp_f32 v41, v41\n\t" \
"v_max_f32 v101, 0, v101\n\t" \
"v_add_f32 v101, v101, v41\n\t" \
"v_add_f32 v101, s59, v101\n\t" \
"v_min_f32 v42, 0, v102\n\t" \
"v_mul_f32 v42, s58, v42\n\t" \
"v_exp_f32 v42, v42\n\t" \
"v_max_f32 v102, 0, v102\n\t" \
"v_add_f32 v102, v102, v42\n\t" \
"v_add_f32 v102, s59, v102\n\t" \
"v_min_f32 v43, 0, v103\n\t" \
"v_mul_f32 v43, s58, v43\n\t" \
"v_exp_f32 v43, v43\n\t" \
"v_max_f32 v103, 0, v103\n\t" \
"v_add_f32 v103, v103, v43\n\t" \
"v_add_f32 v103, s59, v103\n\t" \
"v_cvt_pk_bf16_f32 v160, v96, v97\n\t" \
"v_cvt_pk_bf16_f32 v161, v98, v99\n\t" \
"v_cvt_pk_bf16_f32 v162, v100, v101\n\t" \
"v_cvt_pk_bf16_f32 v163, v102, v103\n\t" \
"s_nop 0\n\t" \
"s_nop 0\n\t" \
"v_mfma_f32_32x32x16_bf16 v[128:143], v[160:163], v[164:167], 0\n\t" \
"v_cvt_pk_bf16_f32 v172, v104, v105\n\t" \
"v_cndmask_b32 v172, v172, v48, s[62:63]\n\t" \
"v_cvt_pk_bf16_f32 v173, v106, v107\n\t" \
"v_cndmask_b32 v173, v173, v48, s[62:63]\n\t" \
"v_cvt_pk_bf16_f32 v174, v108, v109\n\t" \
"v_cndmask_b32 v174, v174, v48, s[62:63]\n\t" \
"v_cvt_pk_bf16_f32 v175, v110, v111\n\t" \
"v_cndmask_b32 v175, v175, v48, s[62:63]\n\t" \
"v_min_f32 v40, 0, v104\n\t" \
"v_mul_f32 v40, s58, v40\n\t" \
"v_exp_f32 v40, v40\n\t" \
"v_max_f32 v104, 0, v104\n\t" \
"v_add_f32 v104, v104, v40\n\t" \
"v_add_f32 v104, s59, v104\n\t" \
"v_min_f32 v41, 0, v105\n\t" \
"v_mul_f32 v41, s58, v41\n\t" \
"v_exp_f32 v41, v41\n\t" \
"v_max_f32 v105, 0, v105\n\t" \
"v_add_f32 v105, v105, v41\n\t" \
"v_add_f32 v105, s59, v105\n\t" \
"v_min_f32 v42, 0, v106\n\t" \
"v_mul_f32 v42, s58, v42\n\t" \
"v_exp_f32 v42, v42\n\t" \
"v_max_f32 v106, 0, v106\n\t" \
"v_add_f32 v106, v106, v42\n\t" \
"v_add_f32 v106, s59, v106\n\t" \
"v_min_f32 v43, 0, v107\n\t" \
"v_mul_f32 v43, s58, v43\n\t" \
"v_exp_f32 v43, v43\n\t" \
"v_max_f32 v107, 0, v107\n\t" \
"v_add_f32 v107, v107, v43\n\t" \
"v_add_f32 v107, s59, v107\n\t" \
"v_min_f32 v40, 0, v108\n\t" \
"v_mul_f32 v40, s58, v40\n\t" \
"v_exp_f32 v40, v40\n\t" \
"v_max_f32 v108, 0, v108\n\t" \
"v_add_f32 v108, v108, v40\n\t" \
"v_add_f32 v108, s59, v108\n\t" \
"v_min_f32 v41, 0, v109\n\t" \
"v_mul_f32 v41, s58, v41\n\t" \
"v_exp_f32 v41, v41\n\t" \
"v_max_f32 v109, 0, v109\n\t" \
"v_add_f32 v109, v109, v41\n\t" \
"v_add_f32 v109, s59, v109\n\t" \
"v_min_f32 v42, 0, v110\n\t" \
"v_mul_f32 v42, s58, v42\n\t" \
"v_exp_f32 v42, v42\n\t" \
"v_max_f32 v110, 0, v110\n\t" \
"v_add_f32 v110, v110, v42\n\t" \
"v_add_f32 v110, s59, v110\n\t" \
"v_min_f32 v43, 0, v111\n\t" \
"v_mul_f32 v43, s58, v43\n\t" \
"v_exp_f32 v43, v43\n\t" \
"v_max_f32 v111, 0, v111\n\t" \
"v_add_f32 v111, v111, v43\n\t" \
"v_add_f32 v111, s59, v111\n\t" \
"v_cvt_pk_bf16_f32 v168, v104, v105\n\t" \
"v_cvt_pk_bf16_f32 v169, v106, v107\n\t" \
"v_cvt_pk_bf16_f32 v170, v108, v109\n\t" \
"v_cvt_pk_bf16_f32 v171, v110, v111\n\t" \
"s_nop 0\n\t" \
"s_nop 0\n\t" \
"v_mfma_f32_32x32x16_bf16 v[128:143], v[168:171], v[172:175], v[128:143]\n\t" \
"v_cvt_pk_bf16_f32 v180, v112, v113\n\t" \
"v_cndmask_b32 v180, v180, v48, s[62:63]\n\t" \
"v_cvt_pk_bf16_f32 v181, v114, v115\n\t" \
"v_cndmask_b32 v181, v181, v48, s[62:63]\n\t" \
"v_cvt_pk_bf16_f32 v182, v116, v117\n\t" \
"v_cndmask_b32 v182, v182, v48, s[62:63]\n\t" \
"v_cvt_pk_bf16_f32 v183, v118, v119\n\t" \
"v_cndmask_b32 v183, v183, v48, s[62:63]\n\t" \
"v_min_f32 v40, 0, v112\n\t" \
"v_mul_f32 v40, s58, v40\n\t" \
"v_exp_f32 v40, v40\n\t" \
"v_max_f32 v112, 0, v112\n\t" \
"v_add_f32 v112, v112, v40\n\t" \
"v_add_f32 v112, s59, v112\n\t" \
"v_min_f32 v41, 0, v113\n\t" \
"v_mul_f32 v41, s58, v41\n\t" \
"v_exp_f32 v41, v41\n\t" \
"v_max_f32 v113, 0, v113\n\t" \
"v_add_f32 v113, v113, v41\n\t" \
"v_add_f32 v113, s59, v113\n\t" \
"v_min_f32 v42, 0, v114\n\t" \
"v_mul_f32 v42, s58, v42\n\t" \
"v_exp_f32 v42, v42\n\t" \
"v_max_f32 v114, 0, v114\n\t" \
"v_add_f32 v114, v114, v42\n\t" \
"v_add_f32 v114, s59, v114\n\t" \
"v_min_f32 v43, 0, v115\n\t" \
"v_mul_f32 v43, s58, v43\n\t" \
"v_exp_f32 v43, v43\n\t" \
"v_max_f32 v115, 0, v115\n\t" \
"v_add_f32 v115, v115, v43\n\t" \
"v_add_f32 v115, s59, v115\n\t" \
"v_min_f32 v40, 0, v116\n\t" \
"v_mul_f32 v40, s58, v40\n\t" \
"v_exp_f32 v40, v40\n\t" \
"v_max_f32 v116, 0, v116\n\t" \
"v_add_f32 v116, v116, v40\n\t" \
"v_add_f32 v116, s59, v116\n\t" \
"v_min_f32 v41, 0, v117\n\t" \
"v_mul_f32 v41, s58, v41\n\t" \
"v_exp_f32 v41, v41\n\t" \
"v_max_f32 v117, 0, v117\n\t" \
"v_add_f32 v117, v117, v41\n\t" \
"v_add_f32 v117, s59, v117\n\t" \
"v_min_f32 v42, 0, v118\n\t" \
"v_mul_f32 v42, s58, v42\n\t" \
"v_exp_f32 v42, v42\n\t" \
"v_max_f32 v118, 0, v118\n\t" \
"v_add_f32 v118, v118, v42\n\t" \
"v_add_f32 v118, s59, v118\n\t" \
"v_min_f32 v43, 0, v119\n\t" \
"v_mul_f32 v43, s58, v43\n\t" \
"v_exp_f32 v43, v43\n\t" \
"v_max_f32 v119, 0, v119\n\t" \
"v_add_f32 v119, v119, v43\n\t" \
"v_add_f32 v119, s59, v119\n\t" \
"v_cvt_pk_bf16_f32 v176, v112, v113\n\t" \
"v_cvt_pk_bf16_f32 v177, v114, v115\n\t" \
"v_cvt_pk_bf16_f32 v178, v116, v117\n\t" \
"v_cvt_pk_bf16_f32 v179, v118, v119\n\t" \
"s_nop 1\n\t" \
"v_mfma_f32_32x32x16_bf16 v[128:143], v[176:179], v[180:183], v[128:143]\n\t" \
"v_cvt_pk_bf16_f32 v188, v120, v121\n\t" \
"v_cndmask_b32 v188, v188, v48, s[62:63]\n\t" \
"v_cvt_pk_bf16_f32 v189, v122, v123\n\t" \
"v_cndmask_b32 v189, v189, v48, s[62:63]\n\t" \
"v_cvt_pk_bf16_f32 v190, v124, v125\n\t" \
"v_cndmask_b32 v190, v190, v48, s[62:63]\n\t" \
"v_cvt_pk_bf16_f32 v191, v126, v127\n\t" \
"v_cndmask_b32 v191, v191, v48, s[62:63]\n\t" \
"v_min_f32 v40, 0, v120\n\t" \
"v_mul_f32 v40, s58, v40\n\t" \
"v_exp_f32 v40, v40\n\t" \
"v_max_f32 v120, 0, v120\n\t" \
"v_add_f32 v120, v120, v40\n\t" \
"v_add_f32 v120, s59, v120\n\t" \
"v_min_f32 v41, 0, v121\n\t" \
"v_mul_f32 v41, s58, v41\n\t" \
"v_exp_f32 v41, v41\n\t" \
"v_max_f32 v121, 0, v121\n\t" \
"v_add_f32 v121, v121, v41\n\t" \
"v_add_f32 v121, s59, v121\n\t" \
"v_min_f32 v42, 0, v122\n\t" \
"v_mul_f32 v42, s58, v42\n\t" \
"v_exp_f32 v42, v42\n\t" \
"v_max_f32 v122, 0, v122\n\t" \
"v_add_f32 v122, v122, v42\n\t" \
"v_add_f32 v122, s59, v122\n\t" \
"v_min_f32 v43, 0, v123\n\t" \
"v_mul_f32 v43, s58, v43\n\t" \
"v_exp_f32 v43, v43\n\t" \
"v_max_f32 v123, 0, v123\n\t" \
"v_add_f32 v123, v123, v43\n\t" \
"v_add_f32 v123, s59, v123\n\t" \
"v_min_f32 v40, 0, v124\n\t" \
"v_mul_f32 v40, s58, v40\n\t" \
"v_exp_f32 v40, v40\n\t" \
"v_max_f32 v124, 0, v124\n\t" \
"v_add_f32 v124, v124, v40\n\t" \
"v_add_f32 v124, s59, v124\n\t" \
"v_min_f32 v41, 0, v125\n\t" \
"v_mul_f32 v41, s58, v41\n\t" \
"v_exp_f32 v41, v41\n\t" \
"v_max_f32 v125, 0, v125\n\t" \
"v_add_f32 v125, v125, v41\n\t" \
"v_add_f32 v125, s59, v125\n\t" \
"v_min_f32 v42, 0, v126\n\t" \
"v_mul_f32 v42, s58, v42\n\t" \
"v_exp_f32 v42, v42\n\t" \
"v_max_f32 v126, 0, v126\n\t" \
"v_add_f32 v126, v126, v42\n\t" \
"v_add_f32 v126, s59, v126\n\t" \
"v_min_f32 v43, 0, v127\n\t" \
"v_mul_f32 v43, s58, v43\n\t" \
"v_exp_f32 v43, v43\n\t" \
"v_max_f32 v127, 0, v127\n\t" \
"v_add_f32 v127, v127, v43\n\t" \
"v_add_f32 v127, s59, v127\n\t" \
"v_cvt_pk_bf16_f32 v184, v120, v121\n\t" \
"v_cvt_pk_bf16_f32 v185, v122, v123\n\t" \
"v_cvt_pk_bf16_f32 v186, v124, v125\n\t" \
"v_cvt_pk_bf16_f32 v187, v126, v127\n\t" \
"s_nop 1\n\t" \
"v_mfma_f32_32x32x16_bf16 v[128:143], v[184:187], v[188:191], v[128:143]\n\t" \
"s_lshl_b32 s56, s75, 11\n\t" \
"s_add_u32 s56, s56, %8\n\t" \
"s_add_u32 s56, s56, 0x18000\n\t" \
"v_add_u32 v22, s56, v36\n\t" \
"s_nop 7\n\t" \
"ds_write_b128 v22, v[128:131]\n\t" \
"ds_write_b128 v22, v[132:135] offset:1024\n\t" \
"s_waitcnt lgkmcnt(0)\n\t" \
"s_barrier\n\t" \
"s_add_u32 s54, s77, 65536\n\t" \
"v_add_u32 v27, s54, v36\n\t" \
"ds_read_b128 a[128:131], v27 offset:0\n\t" \
"ds_read_b128 a[132:135], v27 offset:1024\n\t" \
"ds_read_b128 a[136:139], v27 offset:2048\n\t" \
"ds_read_b128 a[140:143], v27 offset:3072\n\t" \
"ds_read_b128 a[144:147], v27 offset:4096\n\t" \
"ds_read_b128 a[148:151], v27 offset:5120\n\t" \
"ds_read_b128 a[152:155], v27 offset:6144\n\t" \
"ds_read_b128 a[156:159], v27 offset:7168\n\t" \
"ds_read_b128 a[160:163], v27 offset:8192\n\t" \
"ds_read_b128 a[164:167], v27 offset:9216\n\t" \
"ds_read_b128 a[168:171], v27 offset:10240\n\t" \
"ds_read_b128 a[172:175], v27 offset:11264\n\t" \
"ds_read_b128 a[176:179], v27 offset:12288\n\t" \
"ds_read_b128 a[180:183], v27 offset:13312\n\t" \
"ds_read_b128 a[184:187], v27 offset:14336\n\t" \
"s_waitcnt lgkmcnt(10)\n\t" \
"ds_read_b128 a[188:191], v27 offset:15360\n\t" \
"s_cmp_lg_u32 s75, 0\n\t" \
"s_cbranch_scc1 NOPUB_%=\n\t" \
"ds_read_b128 v[168:171], v22 offset:2048\n\t" \
"ds_read_b128 v[172:175], v22 offset:3072\n\t" \
"ds_read_b128 v[176:179], v22 offset:4096\n\t" \
"ds_read_b128 v[180:183], v22 offset:5120\n\t" \
"s_waitcnt lgkmcnt(10)\n\t" \
"ds_read_b128 v[184:187], v22 offset:6144\n\t" \
"ds_read_b128 v[188:191], v22 offset:7168\n\t" \
"s_waitcnt lgkmcnt(5)\n\t" \
"v_add_f32 v128, v128, v168\n\t" \
"v_add_f32 v129, v129, v169\n\t" \
"v_add_f32 v130, v130, v170\n\t" \
"v_add_f32 v131, v131, v171\n\t" \
"s_waitcnt lgkmcnt(4)\n\t" \
"v_add_f32 v132, v132, v172\n\t" \
"v_add_f32 v133, v133, v173\n\t" \
"v_add_f32 v134, v134, v174\n\t" \
"v_add_f32 v135, v135, v175\n\t" \
"s_waitcnt lgkmcnt(3)\n\t" \
"v_add_f32 v128, v128, v176\n\t" \
"v_add_f32 v129, v129, v177\n\t" \
"v_add_f32 v130, v130, v178\n\t" \
"v_add_f32 v131, v131, v179\n\t" \
"s_waitcnt lgkmcnt(2)\n\t" \
"v_add_f32 v132, v132, v180\n\t" \
"v_add_f32 v133, v133, v181\n\t" \
"v_add_f32 v134, v134, v182\n\t" \
"v_add_f32 v135, v135, v183\n\t" \
"s_waitcnt lgkmcnt(1)\n\t" \
"v_add_f32 v128, v128, v184\n\t" \
"v_add_f32 v129, v129, v185\n\t" \
"v_add_f32 v130, v130, v186\n\t" \
"v_add_f32 v131, v131, v187\n\t" \
"s_waitcnt lgkmcnt(0)\n\t" \
"v_add_f32 v132, v132, v188\n\t" \
"v_add_f32 v133, v133, v189\n\t" \
"v_add_f32 v134, v134, v190\n\t" \
"v_add_f32 v135, v135, v191\n\t" \
"v_cvt_pk_bf16_f32 v136, v128, v129\n\t" \
"v_cvt_pk_bf16_f32 v137, v130, v131\n\t" \
"v_cvt_pk_bf16_f32 v138, v132, v133\n\t" \
"v_cvt_pk_bf16_f32 v139, v134, v135\n\t" \
"s_mov_b32 s62, 0x0000fffe\n\t" \
"s_mov_b32 s63, 0x0000fffe\n\t" \
"v_cndmask_b32 v136, v136, 0, s[62:63]\n\t" \
"v_cndmask_b32 v137, v137, 0, s[62:63]\n\t" \
"v_cndmask_b32 v138, v138, 0, s[62:63]\n\t" \
"v_cndmask_b32 v139, v139, 0, s[62:63]\n\t" \
"v_xor_b32 v23, 0x100, v36\n\t" \
"buffer_store_dwordx4 v[136:139], v23, %0, s78 offen sc1\n\t" \
"s_waitcnt vmcnt(0)\n\t" \
"v_mov_b32 v23, 0\n\t" \
"v_mov_b32 v24, 1\n\t" \
"s_mov_b64 exec, 1\n\t" \
"buffer_atomic_add v24, v23, %0, s79 offen\n\t" \
"s_mov_b64 exec, -1\n\t" \
"s_nop 3\n\t" \
"NOPUB_%=:\n\t" \
"s_waitcnt lgkmcnt(0)\n\t" \
"s_waitcnt vmcnt(0)\n\t" \
"s_barrier\n\t" \
"ds_read_b128 a[192:195], v16 offset:0\n\t" \
"ds_read_b128 a[196:199], v16 offset:1024\n\t" \
"ds_read_b128 a[200:203], v16 offset:2048\n\t" \
"ds_read_b128 a[204:207], v16 offset:3072\n\t" \
"ds_read_b128 a[208:211], v16 offset:4096\n\t" \
"ds_read_b128 a[212:215], v16 offset:5120\n\t" \
"ds_read_b128 a[216:219], v16 offset:6144\n\t" \
"ds_read_b128 a[220:223], v16 offset:7168\n\t" \
"s_mov_b32 m0, s48\n\t" \
"s_nop 0\n\t" \
"buffer_load_dwordx4 v36, %0, s42 offen sc1 lds\n\t" \
"buffer_load_dwordx4 v36, %0, s42 offen offset:1024 sc1 lds\n\t" \
"buffer_load_dwordx4 v36, %0, s42 offen offset:2048 sc1 lds\n\t" \
"buffer_load_dwordx4 v36, %0, s42 offen offset:3072 sc1 lds\n\t" \
"s_add_u32 m0, s48, 4096\n\t" \
"s_add_u32 s54, s42, 4096\n\t" \
"buffer_load_dwordx4 v36, %0, s54 offen sc1 lds\n\t" \
"buffer_load_dwordx4 v36, %0, s54 offen offset:1024 sc1 lds\n\t" \
"buffer_load_dwordx4 v36, %0, s54 offen offset:2048 sc1 lds\n\t" \
"buffer_load_dwordx4 v36, %0, s54 offen offset:3072 sc1 lds\n\t" \
"s_xor_b32 s48, s48, 0x8000\n\t" \
"s_add_u32 s44, s44, 1\n\t" \
"s_add_u32 s42, s42, 0x8000\n\t" \
"s_cmp_eq_u32 s44, s45\n\t" \
"s_cselect_b32 s44, 0, s44\n\t" \
"s_cselect_b32 s42, %1, s42\n\t" \
"s_cselect_b32 s45, %6, s45\n\t" \
"s_nop 1\n\t" \
"PASS_%=:\n\t" \
"s_mov_b32 s41, 1\n\t" \
"s_mov_b32 s50, 0\n\t" \
"v_mov_b32 v32, 0\n\t" \
"v_mov_b32 v28, 0\n\t" \
"v_mov_b32 v29, 0\n\t" \
"v_mov_b32 v33, 0\n\t" \
"v_mov_b32 v30, 0\n\t" \
"v_mov_b32 v31, 0\n\t" \
"s_mov_b32 s51, 0xf149f2ca\n\t" \
"s_waitcnt lgkmcnt(7)\n\t" \
"v_mfma_f32_32x32x16_bf16 v[64:79], a[192:195], a[128:131], 0\n\t" \
"s_mov_b32 m0, s49\n\t" \
"v_mov_b32 v224, 0\n\t" \
"v_mfma_f32_32x32x16_bf16 v[80:95], a[192:195], a[160:163], 0\n\t" \
"ds_read_b128 a[192:195], v16 offset:8192\n\t" \
"v_mov_b32 v225, 0\n\t" \
"s_waitcnt lgkmcnt(7)\n\t" \
"v_mfma_f32_32x32x16_bf16 v[64:79], a[196:199], a[132:135], v[64:79]\n\t" \
"buffer_load_dwordx4 v36, %0, s43 offen sc1 lds\n\t" \
"v_mov_b32 v226, 0\n\t" \
"v_mfma_f32_32x32x16_bf16 v[80:95], a[196:199], a[164:167], v[80:95]\n\t" \
"ds_read_b128 a[196:199], v16 offset:9216\n\t" \
"v_mov_b32 v227, 0\n\t" \
"s_waitcnt lgkmcnt(7)\n\t" \
"v_mfma_f32_32x32x16_bf16 v[64:79], a[200:203], a[136:139], v[64:79]\n\t" \
"buffer_load_dwordx4 v36, %0, s43 offen offset:1024 sc1 lds\n\t" \
"v_mov_b32 v228, 0\n\t" \
"v_mfma_f32_32x32x16_bf16 v[80:95], a[200:203], a[168:171], v[80:95]\n\t" \
"ds_read_b128 a[200:203], v16 offset:10240\n\t" \
"v_mov_b32 v229, 0\n\t" \
"s_waitcnt lgkmcnt(7)\n\t" \
"v_mfma_f32_32x32x16_bf16 v[64:79], a[204:207], a[140:143], v[64:79]\n\t" \
"buffer_load_dwordx4 v36, %0, s43 offen offset:2048 sc1 lds\n\t" \
"v_mov_b32 v230, 0\n\t" \
"v_mfma_f32_32x32x16_bf16 v[80:95], a[204:207], a[172:175], v[80:95]\n\t" \
"ds_read_b128 a[204:207], v16 offset:11264\n\t" \
"v_mov_b32 v231, 0\n\t" \
"s_waitcnt lgkmcnt(7)\n\t" \
"v_mfma_f32_32x32x16_bf16 v[64:79], a[208:211], a[144:147], v[64:79]\n\t" \
"buffer_load_dwordx4 v36, %0, s43 offen offset:3072 sc1 lds\n\t" \
"v_mov_b32 v232, 0\n\t" \
"v_mfma_f32_32x32x16_bf16 v[80:95], a[208:211], a[176:179], v[80:95]\n\t" \
"ds_read_b128 a[208:211], v16 offset:12288\n\t" \
"s_add_u32 m0, s49, 4096\n\t" \
"s_add_u32 s55, s43, 4096\n\t" \
"v_mov_b32 v233, 0\n\t" \
"s_waitcnt lgkmcnt(7)\n\t" \
"v_mfma_f32_32x32x16_bf16 v[64:79], a[212:215], a[148:151], v[64:79]\n\t" \
"buffer_load_dwordx4 v36, %0, s55 offen sc1 lds\n\t" \
"v_mov_b32 v234, 0\n\t" \
"v_mfma_f32_32x32x16_bf16 v[80:95], a[212:215], a[180:183], v[80:95]\n\t" \
"ds_read_b128 a[212:215], v16 offset:13312\n\t" \
"v_mov_b32 v235, 0\n\t" \
"s_waitcnt lgkmcnt(7)\n\t" \
"v_mfma_f32_32x32x16_bf16 v[64:79], a[216:219], a[152:155], v[64:79]\n\t" \
"buffer_load_dwordx4 v36, %0, s55 offen offset:1024 sc1 lds\n\t" \
"v_mov_b32 v236, 0\n\t" \
"v_mfma_f32_32x32x16_bf16 v[80:95], a[216:219], a[184:187], v[80:95]\n\t" \
"ds_read_b128 a[216:219], v16 offset:14336\n\t" \
"v_mov_b32 v237, 0\n\t" \
"s_waitcnt lgkmcnt(7)\n\t" \
"v_mfma_f32_32x32x16_bf16 v[64:79], a[220:223], a[156:159], v[64:79]\n\t" \
"buffer_load_dwordx4 v36, %0, s55 offen offset:2048 sc1 lds\n\t" \
"v_mov_b32 v238, 0\n\t" \
"v_mfma_f32_32x32x16_bf16 v[80:95], a[220:223], a[188:191], v[80:95]\n\t" \
"ds_read_b128 a[220:223], v16 offset:15360\n\t" \
"v_mov_b32 v239, 0\n\t" \
"s_waitcnt lgkmcnt(7)\n\t" \
"v_mfma_f32_32x32x16_bf16 v[96:111], a[192:195], a[128:131], 0\n\t" \
"buffer_load_dwordx4 v36, %0, s55 offen offset:3072 sc1 lds\n\t" \
"v_mov_b32 v240, 0\n\t" \
"v_mfma_f32_32x32x16_bf16 v[112:127], a[192:195], a[160:163], 0\n\t" \
"v_mov_b32 v241, 0\n\t" \
"s_waitcnt lgkmcnt(6)\n\t" \
"v_mfma_f32_32x32x16_bf16 v[96:111], a[196:199], a[132:135], v[96:111]\n\t" \
"s_xor_b32 s49, s49, 0x8000\n\t" \
"v_mov_b32 v242, 0\n\t" \
"v_mfma_f32_32x32x16_bf16 v[112:127], a[196:199], a[164:167], v[112:127]\n\t" \
"v_mov_b32 v243, 0\n\t" \
"s_waitcnt lgkmcnt(5)\n\t" \
"v_mfma_f32_32x32x16_bf16 v[96:111], a[200:203], a[136:139], v[96:111]\n\t" \
"s_add_u32 s46, s46, 1\n\t" \
"s_add_u32 s43, s43, 0x8000\n\t" \
"v_mov_b32 v244, 0\n\t" \
"v_max3_f32 v18, v64, v65, v66\n\t" \
"v_max3_f32 v19, v80, v81, v82\n\t" \
"v_mfma_f32_32x32x16_bf16 v[112:127], a[200:203], a[168:171], v[112:127]\n\t" \
"v_mov_b32 v245, 0\n\t" \
"v_max3_f32 v18, v18, v67, v68\n\t" \
"s_waitcnt lgkmcnt(4)\n\t" \
"v_mfma_f32_32x32x16_bf16 v[96:111], a[204:207], a[140:143], v[96:111]\n\t" \
"s_cmp_eq_u32 s46, s47\n\t" \
"s_cselect_b32 s46, 0, s46\n\t" \
"s_cselect_b32 s43, %2, s43\n\t" \
"s_cselect_b32 s47, %6, s47\n\t" \
"v_mov_b32 v246, 0\n\t" \
"v_max3_f32 v19, v19, v83, v84\n\t" \
"v_mfma_f32_32x32x16_bf16 v[112:127], a[204:207], a[172:175], v[112:127]\n\t" \
"v_mov_b32 v247, 0\n\t" \
"v_max3_f32 v18, v18, v69, v70\n\t" \
"v_max3_f32 v19, v19, v85, v86\n\t" \
"s_waitcnt lgkmcnt(3)\n\t" \
"v_mfma_f32_32x32x16_bf16 v[96:111], a[208:211], a[144:147], v[96:111]\n\t" \
"v_mov_b32 v248, 0\n\t" \
"v_max3_f32 v18, v18, v71, v72\n\t" \
"v_mfma_f32_32x32x16_bf16 v[112:127], a[208:211], a[176:179], v[112:127]\n\t" \
"v_mov_b32 v249, 0\n\t" \
"v_max3_f32 v19, v19, v87, v88\n\t" \
"s_waitcnt lgkmcnt(2)\n\t" \
"v_mfma_f32_32x32x16_bf16 v[96:111], a[212:215], a[148:151], v[96:111]\n\t" \
"v_mov_b32 v250, 0\n\t" \
"v_max3_f32 v18, v18, v73, v74\n\t" \
"v_max3_f32 v19, v19, v89, v90\n\t" \
"v_mfma_f32_32x32x16_bf16 v[112:127], a[212:215], a[180:183], v[112:127]\n\t" \
"v_mov_b32 v251, 0\n\t" \
"v_max3_f32 v18, v18, v75, v76\n\t" \
"s_waitcnt lgkmcnt(1)\n\t" \
"v_mfma_f32_32x32x16_bf16 v[96:111], a[216:219], a[152:155], v[96:111]\n\t" \
"v_mov_b32 v252, 0\n\t" \
"v_max3_f32 v19, v19, v91, v92\n\t" \
"v_mfma_f32_32x32x16_bf16 v[112:127], a[216:219], a[184:187], v[112:127]\n\t" \
"v_mov_b32 v253, 0\n\t" \
"v_max3_f32 v18, v18, v77, v78\n\t" \
"v_max3_f32 v19, v19, v93, v94\n\t" \
"s_waitcnt lgkmcnt(0)\n\t" \
"v_mfma_f32_32x32x16_bf16 v[96:111], a[220:223], a[156:159], v[96:111]\n\t" \
"v_mov_b32 v254, 0\n\t" \
"v_max_f32 v18, v18, v79\n\t" \
"v_mfma_f32_32x32x16_bf16 v[112:127], a[220:223], a[188:191], v[112:127]\n\t" \
"v_mov_b32 v255, 0\n\t" \
"v_max_f32 v19, v19, v95\n\t" \
"v_xor_b32 v16, 0x8000, v16\n\t" \
"s_cmp_lg_u32 s40, 1\n\t" \
"s_cbranch_scc1 NOMASKF_%=\n\t" \
"v_add_f32 v40, 0x00000000, v37\n\t" \
"v_min_f32 v64, v64, v40\n\t" \
"s_nop 4\n\t" \
"v_min_f32 v112, v112, v40\n\t" \
"v_add_f32 v41, 0xf1800000, v37\n\t" \
"v_min_f32 v65, v65, v41\n\t" \
"v_min_f32 v113, v113, v41\n\t" \
"v_add_f32 v42, 0xf2000000, v37\n\t" \
"v_min_f32 v66, v66, v42\n\t" \
"v_min_f32 v114, v114, v42\n\t" \
"v_add_f32 v43, 0xf2400000, v37\n\t" \
"v_min_f32 v67, v67, v43\n\t" \
"v_min_f32 v115, v115, v43\n\t" \
"v_add_f32 v44, 0xf3000000, v37\n\t" \
"v_min_f32 v68, v68, v44\n\t" \
"v_min_f32 v116, v116, v44\n\t" \
"v_add_f32 v45, 0xf3100000, v37\n\t" \
"v_min_f32 v69, v69, v45\n\t" \
"v_min_f32 v117, v117, v45\n\t" \
"v_add_f32 v46, 0xf3200000, v37\n\t" \
"v_min_f32 v70, v70, v46\n\t" \
"v_min_f32 v118, v118, v46\n\t" \
"v_add_f32 v47, 0xf3300000, v37\n\t" \
"v_min_f32 v71, v71, v47\n\t" \
"v_min_f32 v119, v119, v47\n\t" \
"v_add_f32 v40, 0xf3800000, v37\n\t" \
"v_min_f32 v72, v72, v40\n\t" \
"v_min_f32 v120, v120, v40\n\t" \
"v_add_f32 v41, 0xf3880000, v37\n\t" \
"v_min_f32 v73, v73, v41\n\t" \
"v_min_f32 v121, v121, v41\n\t" \
"v_add_f32 v42, 0xf3900000, v37\n\t" \
"v_min_f32 v74, v74, v42\n\t" \
"v_min_f32 v122, v122, v42\n\t" \
"v_add_f32 v43, 0xf3980000, v37\n\t" \
"v_min_f32 v75, v75, v43\n\t" \
"v_min_f32 v123, v123, v43\n\t" \
"v_add_f32 v44, 0xf3c00000, v37\n\t" \
"v_min_f32 v76, v76, v44\n\t" \
"v_min_f32 v124, v124, v44\n\t" \
"v_add_f32 v45, 0xf3c80000, v37\n\t" \
"v_min_f32 v77, v77, v45\n\t" \
"v_min_f32 v125, v125, v45\n\t" \
"v_add_f32 v46, 0xf3d00000, v37\n\t" \
"v_min_f32 v78, v78, v46\n\t" \
"v_min_f32 v126, v126, v46\n\t" \
"v_add_f32 v47, 0xf3d80000, v37\n\t" \
"v_min_f32 v79, v79, v47\n\t" \
"v_min_f32 v127, v127, v47\n\t" \
"v_add_f32 v40, 0xf4000000, v37\n\t" \
"v_min_f32 v96, v96, v40\n\t" \
"v_add_f32 v41, 0xf4040000, v37\n\t" \
"v_min_f32 v97, v97, v41\n\t" \
"v_add_f32 v42, 0xf4080000, v37\n\t" \
"v_min_f32 v98, v98, v42\n\t" \
"v_add_f32 v43, 0xf40c0000, v37\n\t" \
"v_min_f32 v99, v99, v43\n\t" \
"v_add_f32 v44, 0xf4200000, v37\n\t" \
"v_min_f32 v100, v100, v44\n\t" \
"v_add_f32 v45, 0xf4240000, v37\n\t" \
"v_min_f32 v101, v101, v45\n\t" \
"v_add_f32 v46, 0xf4280000, v37\n\t" \
"v_min_f32 v102, v102, v46\n\t" \
"v_add_f32 v47, 0xf42c0000, v37\n\t" \
"v_min_f32 v103, v103, v47\n\t" \
"v_add_f32 v40, 0xf4400000, v37\n\t" \
"v_min_f32 v104, v104, v40\n\t" \
"v_add_f32 v41, 0xf4440000, v37\n\t" \
"v_min_f32 v105, v105, v41\n\t" \
"v_add_f32 v42, 0xf4480000, v37\n\t" \
"v_min_f32 v106, v106, v42\n\t" \
"v_add_f32 v43, 0xf44c0000, v37\n\t" \
"v_min_f32 v107, v107, v43\n\t" \
"v_add_f32 v44, 0xf4600000, v37\n\t" \
"v_min_f32 v108, v108, v44\n\t" \
"v_add_f32 v45, 0xf4640000, v37\n\t" \
"v_min_f32 v109, v109, v45\n\t" \
"v_add_f32 v46, 0xf4680000, v37\n\t" \
"v_min_f32 v110, v110, v46\n\t" \
"v_add_f32 v47, 0xf46c0000, v37\n\t" \
"v_min_f32 v111, v111, v47\n\t" \
"v_add_f32 v40, 0x74000000, v37\n\t" \
"v_min_f32 v80, v80, v40\n\t" \
"v_add_f32 v41, 0x73f80000, v37\n\t" \
"v_min_f32 v81, v81, v41\n\t" \
"v_add_f32 v42, 0x73f00000, v37\n\t" \
"v_min_f32 v82, v82, v42\n\t" \
"v_add_f32 v43, 0x73e80000, v37\n\t" \
"v_min_f32 v83, v83, v43\n\t" \
"v_add_f32 v44, 0x73c00000, v37\n\t" \
"v_min_f32 v84, v84, v44\n\t" \
"v_add_f32 v45, 0x73b80000, v37\n\t" \
"v_min_f32 v85, v85, v45\n\t" \
"v_add_f32 v46, 0x73b00000, v37\n\t" \
"v_min_f32 v86, v86, v46\n\t" \
"v_add_f32 v47, 0x73a80000, v37\n\t" \
"v_min_f32 v87, v87, v47\n\t" \
"v_add_f32 v40, 0x73800000, v37\n\t" \
"v_min_f32 v88, v88, v40\n\t" \
"v_add_f32 v41, 0x73700000, v37\n\t" \
"v_min_f32 v89, v89, v41\n\t" \
"v_add_f32 v42, 0x73600000, v37\n\t" \
"v_min_f32 v90, v90, v42\n\t" \
"v_add_f32 v43, 0x73500000, v37\n\t" \
"v_min_f32 v91, v91, v43\n\t" \
"v_add_f32 v44, 0x73000000, v37\n\t" \
"v_min_f32 v92, v92, v44\n\t" \
"v_add_f32 v45, 0x72e00000, v37\n\t" \
"v_min_f32 v93, v93, v45\n\t" \
"v_add_f32 v46, 0x72c00000, v37\n\t" \
"v_min_f32 v94, v94, v46\n\t" \
"v_add_f32 v47, 0x72a00000, v37\n\t" \
"v_min_f32 v95, v95, v47\n\t" \
"v_max3_f32 v18, v64, v65, v66\n\t" \
"v_max3_f32 v19, v80, v81, v82\n\t" \
"v_max3_f32 v18, v18, v67, v68\n\t" \
"v_max3_f32 v19, v19, v83, v84\n\t" \
"v_max3_f32 v18, v18, v69, v70\n\t" \
"v_max3_f32 v19, v19, v85, v86\n\t" \
"v_max3_f32 v18, v18, v71, v72\n\t" \
"v_max3_f32 v19, v19, v87, v88\n\t" \
"v_max3_f32 v18, v18, v73, v74\n\t" \
"v_max3_f32 v19, v19, v89, v90\n\t" \
"v_max3_f32 v18, v18, v75, v76\n\t" \
"v_max3_f32 v19, v19, v91, v92\n\t" \
"v_max3_f32 v18, v18, v77, v78\n\t" \
"v_max3_f32 v19, v19, v93, v94\n\t" \
"v_max_f32 v18, v18, v79\n\t" \
"v_max_f32 v19, v19, v95\n\t" \
"NOMASKF_%=:\n\t" \
"s_nop 3\n\t" \
"v_max3_f32 v18, v18, v96, v97\n\t" \
"s_nop 1\n\t" \
"v_max3_f32 v19, v19, v112, v113\n\t" \
"v_max3_f32 v18, v18, v98, v99\n\t" \
"v_max3_f32 v19, v19, v114, v115\n\t" \
"v_max3_f32 v18, v18, v100, v101\n\t" \
"v_max3_f32 v19, v19, v116, v117\n\t" \
"v_max3_f32 v18, v18, v102, v103\n\t" \
"v_max3_f32 v19, v19, v118, v119\n\t" \
"v_max3_f32 v18, v18, v104, v105\n\t" \
"v_max3_f32 v19, v19, v120, v121\n\t" \
"v_max3_f32 v18, v18, v106, v107\n\t" \
"v_max3_f32 v19, v19, v122, v123\n\t" \
"v_max3_f32 v18, v18, v108, v109\n\t" \
"v_max3_f32 v19, v19, v124, v125\n\t" \
"v_max3_f32 v18, v18, v110, v111\n\t" \
"v_max3_f32 v19, v19, v126, v127\n\t" \
"v_mov_b32 v20, v18\n\t" \
"v_mov_b32 v21, v19\n\t" \
"s_nop 0\n\t" \
"v_permlane32_swap_b32 v18, v20\n\t" \
"v_permlane32_swap_b32 v19, v21\n\t" \
"v_max_f32 v20, v20, v18\n\t" \
"v_max_f32 v21, v21, v19\n\t" \
"v_max_f32 v22, v20, v21\n\t" \
"v_min_f32 v23, v20, v21\n\t" \
"v_cmp_gt_f32 s[62:63], s60, v23\n\t" \
"v_cmp_lt_f32 vcc, 0x41000000, v22\n\t" \
"s_or_b64 vcc, vcc, s[62:63]\n\t" \
"s_cbranch_vccnz RARE0_f_%=\n\t" \
"BACK0_f_%=:\n\t" \
"v_exp_f32 v64, v64\n\t" \
"v_exp_f32 v65, v65\n\t" \
"v_add_f32 v28, v28, v64\n\t" \
"v_exp_f32 v66, v66\n\t" \
"v_add_f32 v29, v29, v65\n\t" \
"v_exp_f32 v67, v67\n\t" \
"v_add_f32 v28, v28, v66\n\t" \
"v_cvt_pk_bf16_f32 v192, v64, v65\n\t" \
"v_exp_f32 v68, v68\n\t" \
"v_add_f32 v29, v29, v67\n\t" \
"v_exp_f32 v69, v69\n\t" \
"v_add_f32 v28, v28, v68\n\t" \
"v_cvt_pk_bf16_f32 v193, v66, v67\n\t" \
"s_waitcnt vmcnt(0)\n\t" \
"s_barrier\n\t" \
"v_exp_f32 v70, v70\n\t" \
"v_add_f32 v29, v29, v69\n\t" \
"ds_read_b128 a[192:195], v16 offset:0\n\t" \
"s_mov_b32 m0, s48\n\t" \
"v_exp_f32 v71, v71\n\t" \
"v_add_f32 v28, v28, v70\n\t" \
"v_cvt_pk_bf16_f32 v194, v68, v69\n\t" \
"ds_read_b128 a[196:199], v16 offset:1024\n\t" \
"buffer_load_dwordx4 v36, %0, s42 offen sc1 lds\n\t" \
"v_exp_f32 v80, v80\n\t" \
"v_add_f32 v29, v29, v71\n\t" \
"buffer_load_dwordx4 v36, %0, s42 offen offset:1024 sc1 lds\n\t" \
"v_exp_f32 v81, v81\n\t" \
"v_add_f32 v30, v30, v80\n\t" \
"v_cvt_pk_bf16_f32 v195, v70, v71\n\t" \
"ds_read_b128 a[200:203], v16 offset:2048\n\t" \
"buffer_load_dwordx4 v36, %0, s42 offen offset:2048 sc1 lds\n\t" \
"v_exp_f32 v82, v82\n\t" \
"v_add_f32 v31, v31, v81\n\t" \
"ds_read_b128 a[204:207], v16 offset:3072\n\t" \
"v_exp_f32 v83, v83\n\t" \
"v_add_f32 v30, v30, v82\n\t" \
"v_cvt_pk_bf16_f32 v208, v80, v81\n\t" \
"buffer_load_dwordx4 v36, %0, s42 offen offset:3072 sc1 lds\n\t" \
"s_add_u32 m0, s48, 4096\n\t" \
"s_add_u32 s54, s42, 4096\n\t" \
"v_exp_f32 v84, v84\n\t" \
"v_add_f32 v31, v31, v83\n\t" \
"ds_read_b128 a[208:211], v16 offset:4096\n\t" \
"buffer_load_dwordx4 v36, %0, s54 offen sc1 lds\n\t" \
"v_exp_f32 v85, v85\n\t" \
"v_add_f32 v30, v30, v84\n\t" \
"v_cvt_pk_bf16_f32 v209, v82, v83\n\t" \
"ds_read_b128 a[212:215], v16 offset:5120\n\t" \
"v_exp_f32 v86, v86\n\t" \
"v_add_f32 v31, v31, v85\n\t" \
"buffer_load_dwordx4 v36, %0, s54 offen offset:1024 sc1 lds\n\t" \
"v_exp_f32 v87, v87\n\t" \
"v_add_f32 v30, v30, v86\n\t" \
"v_cvt_pk_bf16_f32 v210, v84, v85\n\t" \
"ds_read_b128 a[216:219], v16 offset:6144\n\t" \
"buffer_load_dwordx4 v36, %0, s54 offen offset:2048 sc1 lds\n\t" \
"v_exp_f32 v72, v72\n\t" \
"v_add_f32 v31, v31, v87\n\t" \
"ds_read_b128 a[220:223], v16 offset:7168\n\t" \
"v_exp_f32 v73, v73\n\t" \
"v_add_f32 v28, v28, v72\n\t" \
"v_cvt_pk_bf16_f32 v211, v86, v87\n\t" \
"buffer_load_dwordx4 v36, %0, s54 offen offset:3072 sc1 lds\n\t" \
"v_exp_f32 v74, v74\n\t" \
"v_add_f32 v29, v29, v73\n\t" \
"s_xor_b32 s48, s48, 0x8000\n\t" \
"s_add_u32 s44, s44, 1\n\t" \
"s_add_u32 s42, s42, 0x8000\n\t" \
"s_cmp_eq_u32 s44, s45\n\t" \
"s_cselect_b32 s44, 0, s44\n\t" \
"s_cselect_b32 s42, %1, s42\n\t" \
"s_cselect_b32 s45, %6, s45\n\t" \
"s_mov_b32 s51, 0\n\t" \
"s_cmp_eq_u32 s52, 0\n\t" \
"s_cbranch_scc1 NOLIN_%=\n\t" \
"v_mfma_f32_32x32x16_bf16 v[176:191], v[128:131], v[160:163], 0\n\t" \
"v_mfma_f32_32x32x16_bf16 v[176:191], v[132:135], v[160:163], v[176:191]\n\t" \
"v_mfma_f32_32x32x16_bf16 v[176:191], v[136:139], v[160:163], v[176:191]\n\t" \
"v_mfma_f32_32x32x16_bf16 v[176:191], v[140:143], v[160:163], v[176:191]\n\t" \
"v_mfma_f32_32x32x16_bf16 v[176:191], v[144:147], v[160:163], v[176:191]\n\t" \
"v_mfma_f32_32x32x16_bf16 v[176:191], v[148:151], v[160:163], v[176:191]\n\t" \
"v_mfma_f32_32x32x16_bf16 v[176:191], v[152:155], v[160:163], v[176:191]\n\t" \
"v_mfma_f32_32x32x16_bf16 v[176:191], v[156:159], v[160:163], v[176:191]\n\t" \
"s_nop 11\n\t" \
"v_mov_b32 v22, v184\n\t" \
"s_nop 1\n\t" \
"v_permlane32_swap_b32 v184, v22\n\t" \
"v_add_f32 v184, 0x358637bd, v184\n\t" \
"v_rcp_f32 v184, v184\n\t" \
"v_sub_f32 v168, 1.0, v168\n\t" \
"v_mul_f32 v184, v184, v168\n\t" \
"v_mul_f32 v176, v176, v184\n\t" \
"v_mul_f32 v177, v177, v184\n\t" \
"v_mul_f32 v178, v178, v184\n\t" \
"v_mul_f32 v179, v179, v184\n\t" \
"v_mul_f32 v180, v180, v184\n\t" \
"v_mul_f32 v181, v181, v184\n\t" \
"v_mul_f32 v182, v182, v184\n\t" \
"v_mul_f32 v183, v183, v184\n\t" \
"v_cvt_pk_bf16_f32 v170, v176, v177\n\t" \
"v_cvt_pk_bf16_f32 v171, v178, v179\n\t" \
"v_cvt_pk_bf16_f32 v172, v180, v181\n\t" \
"v_cvt_pk_bf16_f32 v173, v182, v183\n\t" \
"s_add_u32 s57, %12, 0\n\t" \
"buffer_store_dwordx4 v[170:173], v36, %0, s57 offen sc1\n\t" \
"v_mfma_f32_32x32x16_bf16 v[176:191], v[128:131], v[164:167], 0\n\t" \
"v_mfma_f32_32x32x16_bf16 v[176:191], v[132:135], v[164:167], v[176:191]\n\t" \
"v_mfma_f32_32x32x16_bf16 v[176:191], v[136:139], v[164:167], v[176:191]\n\t" \
"v_mfma_f32_32x32x16_bf16 v[176:191], v[140:143], v[164:167], v[176:191]\n\t" \
"v_mfma_f32_32x32x16_bf16 v[176:191], v[144:147], v[164:167], v[176:191]\n\t" \
"v_mfma_f32_32x32x16_bf16 v[176:191], v[148:151], v[164:167], v[176:191]\n\t" \
"v_mfma_f32_32x32x16_bf16 v[176:191], v[152:155], v[164:167], v[176:191]\n\t" \
"v_mfma_f32_32x32x16_bf16 v[176:191], v[156:159], v[164:167], v[176:191]\n\t" \
"s_nop 11\n\t" \
"v_mov_b32 v23, v184\n\t" \
"s_nop 1\n\t" \
"v_permlane32_swap_b32 v184, v23\n\t" \
"v_add_f32 v184, 0x358637bd, v184\n\t" \
"v_rcp_f32 v184, v184\n\t" \
"v_sub_f32 v169, 1.0, v169\n\t" \
"v_mul_f32 v184, v184, v169\n\t" \
"v_mul_f32 v176, v176, v184\n\t" \
"v_mul_f32 v177, v177, v184\n\t" \
"v_mul_f32 v178, v178, v184\n\t" \
"v_mul_f32 v179, v179, v184\n\t" \
"v_mul_f32 v180, v180, v184\n\t" \
"v_mul_f32 v181, v181, v184\n\t" \
"v_mul_f32 v182, v182, v184\n\t" \
"v_mul_f32 v183, v183, v184\n\t" \
"v_cvt_pk_bf16_f32 v170, v176, v177\n\t" \
"v_cvt_pk_bf16_f32 v171, v178, v179\n\t" \
"v_cvt_pk_bf16_f32 v172, v180, v181\n\t" \
"v_cvt_pk_bf16_f32 v173, v182, v183\n\t" \
"s_add_u32 s57, %12, 294912\n\t" \
"buffer_store_dwordx4 v[170:173], v36, %0, s57 offen sc1\n\t" \
"s_nop 7\n\t" \
"NOLIN_%=:\n\t" \
"s_cmp_eq_u32 s40, 1\n\t" \
"s_cbranch_scc1 DRAIN0F_%=\n\t" \
"s_waitcnt lgkmcnt(7)\n\t" \
"v_mfma_f32_32x32x16_bf16 v[128:143], a[192:195], a[128:131], v[224:239]\n\t" \
"s_mov_b32 m0, s49\n\t" \
"v_exp_f32 v75, v75\n\t" \
"v_add_f32 v28, v28, v74\n\t" \
"v_cvt_pk_bf16_f32 v196, v72, v73\n\t" \
"v_mfma_f32_32x32x16_bf16 v[144:159], a[192:195], a[160:163], v[240:255]\n\t" \
"ds_read_b128 a[192:195], v16 offset:8192\n\t" \
"v_exp_f32 v76, v76\n\t" \
"v_add_f32 v29, v29, v75\n\t" \
"s_waitcnt lgkmcnt(7)\n\t" \
"v_mfma_f32_32x32x16_bf16 v[128:143], a[196:199], a[132:135], v[128:143]\n\t" \
"buffer_load_dwordx4 v36, %0, s43 offen sc1 lds\n\t" \
"v_exp_f32 v77, v77\n\t" \
"v_add_f32 v28, v28, v76\n\t" \
"v_cvt_pk_bf16_f32 v197, v74, v75\n\t" \
"v_mfma_f32_32x32x16_bf16 v[144:159], a[196:199], a[164:167], v[144:159]\n\t" \
"ds_read_b128 a[196:199], v16 offset:9216\n\t" \
"v_exp_f32 v78, v78\n\t" \
"v_add_f32 v29, v29, v77\n\t" \
"s_waitcnt lgkmcnt(7)\n\t" \
"v_mfma_f32_32x32x16_bf16 v[128:143], a[200:203], a[136:139], v[128:143]\n\t" \
"buffer_load_dwordx4 v36, %0, s43 offen offset:1024 sc1 lds\n\t" \
"v_exp_f32 v79, v79\n\t" \
"v_add_f32 v28, v28, v78\n\t" \
"v_cvt_pk_bf16_f32 v198, v76, v77\n\t" \
"v_mfma_f32_32x32x16_bf16 v[144:159], a[200:203], a[168:171], v[144:159]\n\t" \
"ds_read_b128 a[200:203], v16 offset:10240\n\t" \
"v_exp_f32 v88, v88\n\t" \
"v_add_f32 v29, v29, v79\n\t" \
"s_waitcnt lgkmcnt(7)\n\t" \
"v_mfma_f32_32x32x16_bf16 v[128:143], a[204:207], a[140:143], v[128:143]\n\t" \
"buffer_load_dwordx4 v36, %0, s43 offen offset:2048 sc1 lds\n\t" \
"v_exp_f32 v89, v89\n\t" \
"v_add_f32 v30, v30, v88\n\t" \
"v_cvt_pk_bf16_f32 v199, v78, v79\n\t" \
"v_mfma_f32_32x32x16_bf16 v[144:159], a[204:207], a[172:175], v[144:159]\n\t" \
"ds_read_b128 a[204:207], v16 offset:11264\n\t" \
"v_exp_f32 v90, v90\n\t" \
"v_add_f32 v31, v31, v89\n\t" \
"s_waitcnt lgkmcnt(7)\n\t" \
"v_mfma_f32_32x32x16_bf16 v[128:143], a[208:211], a[144:147], v[128:143]\n\t" \
"buffer_load_dwordx4 v36, %0, s43 offen offset:3072 sc1 lds\n\t" \
"v_exp_f32 v91, v91\n\t" \
"v_add_f32 v30, v30, v90\n\t" \
"v_cvt_pk_bf16_f32 v212, v88, v89\n\t" \
"v_mfma_f32_32x32x16_bf16 v[144:159], a[208:211], a[176:179], v[144:159]\n\t" \
"ds_read_b128 a[208:211], v16 offset:12288\n\t" \
"s_add_u32 m0, s49, 4096\n\t" \
"s_add_u32 s55, s43, 4096\n\t" \
"v_exp_f32 v92, v92\n\t" \
"v_add_f32 v31, v31, v91\n\t" \
"s_waitcnt lgkmcnt(7)\n\t" \
"v_mfma_f32_32x32x16_bf16 v[128:143], a[212:215], a[148:151], v[128:143]\n\t" \
"buffer_load_dwordx4 v36, %0, s55 offen sc1 lds\n\t" \
"v_exp_f32 v93, v93\n\t" \
"v_add_f32 v30, v30, v92\n\t" \
"v_cvt_pk_bf16_f32 v213, v90, v91\n\t" \
"v_mfma_f32_32x32x16_bf16 v[144:159], a[212:215], a[180:183], v[144:159]\n\t" \
"ds_read_b128 a[212:215], v16 offset:13312\n\t" \
"v_exp_f32 v94, v94\n\t" \
"v_add_f32 v31, v31, v93\n\t" \
"s_waitcnt lgkmcnt(7)\n\t" \
"v_mfma_f32_32x32x16_bf16 v[128:143], a[216:219], a[152:155], v[128:143]\n\t" \
"buffer_load_dwordx4 v36, %0, s55 offen offset:1024 sc1 lds\n\t" \
"v_exp_f32 v95, v95\n\t" \
"v_add_f32 v30, v30, v94\n\t" \
"v_cvt_pk_bf16_f32 v214, v92, v93\n\t" \
"v_mfma_f32_32x32x16_bf16 v[144:159], a[216:219], a[184:187], v[144:159]\n\t" \
"ds_read_b128 a[216:219], v16 offset:14336\n\t" \
"v_exp_f32 v96, v96\n\t" \
"v_add_f32 v31, v31, v95\n\t" \
"s_waitcnt lgkmcnt(7)\n\t" \
"v_mfma_f32_32x32x16_bf16 v[128:143], a[220:223], a[156:159], v[128:143]\n\t" \
"buffer_load_dwordx4 v36, %0, s55 offen offset:2048 sc1 lds\n\t" \
"v_exp_f32 v97, v97\n\t" \
"v_add_f32 v28, v28, v96\n\t" \
"v_cvt_pk_bf16_f32 v215, v94, v95\n\t" \
"v_mfma_f32_32x32x16_bf16 v[144:159], a[220:223], a[188:191], v[144:159]\n\t" \
"ds_read_b128 a[220:223], v16 offset:15360\n\t" \
"v_exp_f32 v98, v98\n\t" \
"v_add_f32 v29, v29, v97\n\t" \
"s_waitcnt lgkmcnt(7)\n\t" \
"v_mfma_f32_32x32x16_bf16 v[160:175], a[192:195], a[128:131], v[224:239]\n\t" \
"buffer_load_dwordx4 v36, %0, s55 offen offset:3072 sc1 lds\n\t" \
"v_exp_f32 v99, v99\n\t" \
"v_add_f32 v28, v28, v98\n\t" \
"v_cvt_pk_bf16_f32 v200, v96, v97\n\t" \
"v_mfma_f32_32x32x16_bf16 v[176:191], a[192:195], a[160:163], v[240:255]\n\t" \
"ds_read_b128 a[224:227], v17 offset:0\n\t" \
"v_exp_f32 v100, v100\n\t" \
"v_add_f32 v29, v29, v99\n\t" \
"s_waitcnt lgkmcnt(7)\n\t" \
"v_mfma_f32_32x32x16_bf16 v[160:175], a[196:199], a[132:135], v[160:175]\n\t" \
"s_xor_b32 s49, s49, 0x8000\n\t" \
"v_exp_f32 v101, v101\n\t" \
"v_add_f32 v28, v28, v100\n\t" \
"v_cvt_pk_bf16_f32 v201, v98, v99\n\t" \
"v_mfma_f32_32x32x16_bf16 v[176:191], a[196:199], a[164:167], v[176:191]\n\t" \
"ds_read_b128 a[228:231], v17 offset:1024\n\t" \
"v_exp_f32 v102, v102\n\t" \
"v_add_f32 v29, v29, v101\n\t" \
"s_waitcnt lgkmcnt(7)\n\t" \
"v_mfma_f32_32x32x16_bf16 v[160:175], a[200:203], a[136:139], v[160:175]\n\t" \
"s_add_u32 s46, s46, 1\n\t" \
"s_add_u32 s43, s43, 0x8000\n\t" \
"v_exp_f32 v103, v103\n\t" \
"v_add_f32 v28, v28, v102\n\t" \
"v_cvt_pk_bf16_f32 v202, v100, v101\n\t" \
"v_max3_f32 v18, v128, v129, v130\n\t" \
"v_max3_f32 v19, v144, v145, v146\n\t" \
"v_mfma_f32_32x32x16_bf16 v[176:191], a[200:203], a[168:171], v[176:191]\n\t" \
"ds_read_b128 a[232:235], v17 offset:2048\n\t" \
"v_exp_f32 v112, v112\n\t" \
"v_add_f32 v29, v29, v103\n\t" \
"v_max3_f32 v18, v18, v131, v132\n\t" \
"s_waitcnt lgkmcnt(7)\n\t" \
"v_mfma_f32_32x32x16_bf16 v[160:175], a[204:207], a[140:143], v[160:175]\n\t" \
"s_cmp_eq_u32 s46, s47\n\t" \
"s_cselect_b32 s46, 0, s46\n\t" \
"s_cselect_b32 s43, %2, s43\n\t" \
"s_cselect_b32 s47, %6, s47\n\t" \
"v_exp_f32 v113, v113\n\t" \
"v_add_f32 v30, v30, v112\n\t" \
"v_cvt_pk_bf16_f32 v203, v102, v103\n\t" \
"v_max3_f32 v19, v19, v147, v148\n\t" \
"v_mfma_f32_32x32x16_bf16 v[176:191], a[204:207], a[172:175], v[176:191]\n\t" \
"ds_read_b128 a[236:239], v17 offset:3072\n\t" \
"v_exp_f32 v114, v114\n\t" \
"v_add_f32 v31, v31, v113\n\t" \
"v_max3_f32 v18, v18, v133, v134\n\t" \
"v_max3_f32 v19, v19, v149, v150\n\t" \
"s_waitcnt lgkmcnt(7)\n\t" \
"v_mfma_f32_32x32x16_bf16 v[160:175], a[208:211], a[144:147], v[160:175]\n\t" \
"v_exp_f32 v115, v115\n\t" \
"v_add_f32 v30, v30, v114\n\t" \
"v_cvt_pk_bf16_f32 v216, v112, v113\n\t" \
"v_max3_f32 v18, v18, v135, v136\n\t" \
"v_mfma_f32_32x32x16_bf16 v[176:191], a[208:211], a[176:179], v[176:191]\n\t" \
"ds_read_b128 a[240:243], v17 offset:4096\n\t" \
"v_exp_f32 v116, v116\n\t" \
"v_add_f32 v31, v31, v115\n\t" \
"v_max3_f32 v19, v19, v151, v152\n\t" \
"s_waitcnt lgkmcnt(7)\n\t" \
"v_mfma_f32_32x32x16_bf16 v[160:175], a[212:215], a[148:151], v[160:175]\n\t" \
"v_exp_f32 v117, v117\n\t" \
"v_add_f32 v30, v30, v116\n\t" \
"v_cvt_pk_bf16_f32 v217, v114, v115\n\t" \
"v_max3_f32 v18, v18, v137, v138\n\t" \
"v_max3_f32 v19, v19, v153, v154\n\t" \
"v_mfma_f32_32x32x16_bf16 v[176:191], a[212:215], a[180:183], v[176:191]\n\t" \
"ds_read_b128 a[244:247], v17 offset:5120\n\t" \
"v_exp_f32 v118, v118\n\t" \
"v_add_f32 v31, v31, v117\n\t" \
"v_max3_f32 v18, v18, v139, v140\n\t" \
"s_waitcnt lgkmcnt(7)\n\t" \
"v_mfma_f32_32x32x16_bf16 v[160:175], a[216:219], a[152:155], v[160:175]\n\t" \
"v_exp_f32 v119, v119\n\t" \
"v_add_f32 v30, v30, v118\n\t" \
"v_cvt_pk_bf16_f32 v218, v116, v117\n\t" \
"v_max3_f32 v19, v19, v155, v156\n\t" \
"v_mfma_f32_32x32x16_bf16 v[176:191], a[216:219], a[184:187], v[176:191]\n\t" \
"ds_read_b128 a[248:251], v17 offset:6144\n\t" \
"v_exp_f32 v104, v104\n\t" \
"v_add_f32 v31, v31, v119\n\t" \
"v_max3_f32 v18, v18, v141, v142\n\t" \
"v_max3_f32 v19, v19, v157, v158\n\t" \
"s_waitcnt lgkmcnt(7)\n\t" \
"v_mfma_f32_32x32x16_bf16 v[160:175], a[220:223], a[156:159], v[160:175]\n\t" \
"v_exp_f32 v105, v105\n\t" \
"v_add_f32 v28, v28, v104\n\t" \
"v_cvt_pk_bf16_f32 v219, v118, v119\n\t" \
"v_max_f32 v18, v18, v143\n\t" \
"v_mfma_f32_32x32x16_bf16 v[176:191], a[220:223], a[188:191], v[176:191]\n\t" \
"ds_read_b128 a[252:255], v17 offset:7168\n\t" \
"v_exp_f32 v106, v106\n\t" \
"v_add_f32 v29, v29, v105\n\t" \
"v_max_f32 v19, v19, v159\n\t" \
"v_xor_b32 v16, 0x8000, v16\n\t" \
"s_add_u32 s41, s41, 1\n\t" \
"s_cmp_lg_u32 s41, s40\n\t" \
"s_cbranch_scc1 NOMASK_p1_%=\n\t" \
"v_add_f32 v40, 0x00000000, v37\n\t" \
"v_min_f32 v128, v128, v40\n\t" \
"s_nop 1\n\t" \
"v_min_f32 v176, v176, v40\n\t" \
"v_add_f32 v41, 0xf1800000, v37\n\t" \
"v_min_f32 v129, v129, v41\n\t" \
"v_min_f32 v177, v177, v41\n\t" \
"v_add_f32 v42, 0xf2000000, v37\n\t" \
"v_min_f32 v130, v130, v42\n\t" \
"v_min_f32 v178, v178, v42\n\t" \
"v_add_f32 v43, 0xf2400000, v37\n\t" \
"v_min_f32 v131, v131, v43\n\t" \
"v_min_f32 v179, v179, v43\n\t" \
"v_add_f32 v44, 0xf3000000, v37\n\t" \
"v_min_f32 v132, v132, v44\n\t" \
"v_min_f32 v180, v180, v44\n\t" \
"v_add_f32 v45, 0xf3100000, v37\n\t" \
"v_min_f32 v133, v133, v45\n\t" \
"v_min_f32 v181, v181, v45\n\t" \
"v_add_f32 v46, 0xf3200000, v37\n\t" \
"v_min_f32 v134, v134, v46\n\t" \
"v_min_f32 v182, v182, v46\n\t" \
"v_add_f32 v47, 0xf3300000, v37\n\t" \
"v_min_f32 v135, v135, v47\n\t" \
"v_min_f32 v183, v183, v47\n\t" \
"v_add_f32 v40, 0xf3800000, v37\n\t" \
"v_min_f32 v136, v136, v40\n\t" \
"v_min_f32 v184, v184, v40\n\t" \
"v_add_f32 v41, 0xf3880000, v37\n\t" \
"v_min_f32 v137, v137, v41\n\t" \
"v_min_f32 v185, v185, v41\n\t" \
"v_add_f32 v42, 0xf3900000, v37\n\t" \
"v_min_f32 v138, v138, v42\n\t" \
"v_min_f32 v186, v186, v42\n\t" \
"v_add_f32 v43, 0xf3980000, v37\n\t" \
"v_min_f32 v139, v139, v43\n\t" \
"v_min_f32 v187, v187, v43\n\t" \
"v_add_f32 v44, 0xf3c00000, v37\n\t" \
"v_min_f32 v140, v140, v44\n\t" \
"v_min_f32 v188, v188, v44\n\t" \
"v_add_f32 v45, 0xf3c80000, v37\n\t" \
"v_min_f32 v141, v141, v45\n\t" \
"v_min_f32 v189, v189, v45\n\t" \
"v_add_f32 v46, 0xf3d00000, v37\n\t" \
"v_min_f32 v142, v142, v46\n\t" \
"v_min_f32 v190, v190, v46\n\t" \
"v_add_f32 v47, 0xf3d80000, v37\n\t" \
"v_min_f32 v143, v143, v47\n\t" \
"v_min_f32 v191, v191, v47\n\t" \
"v_add_f32 v40, 0xf4000000, v37\n\t" \
"v_min_f32 v160, v160, v40\n\t" \
"v_add_f32 v41, 0xf4040000, v37\n\t" \
"v_min_f32 v161, v161, v41\n\t" \
"v_add_f32 v42, 0xf4080000, v37\n\t" \
"v_min_f32 v162, v162, v42\n\t" \
"v_add_f32 v43, 0xf40c0000, v37\n\t" \
"v_min_f32 v163, v163, v43\n\t" \
"v_add_f32 v44, 0xf4200000, v37\n\t" \
"v_min_f32 v164, v164, v44\n\t" \
"v_add_f32 v45, 0xf4240000, v37\n\t" \
"v_min_f32 v165, v165, v45\n\t" \
"v_add_f32 v46, 0xf4280000, v37\n\t" \
"v_min_f32 v166, v166, v46\n\t" \
"v_add_f32 v47, 0xf42c0000, v37\n\t" \
"v_min_f32 v167, v167, v47\n\t" \
"v_add_f32 v40, 0xf4400000, v37\n\t" \
"v_min_f32 v168, v168, v40\n\t" \
"v_add_f32 v41, 0xf4440000, v37\n\t" \
"v_min_f32 v169, v169, v41\n\t" \
"v_add_f32 v42, 0xf4480000, v37\n\t" \
"v_min_f32 v170, v170, v42\n\t" \
"v_add_f32 v43, 0xf44c0000, v37\n\t" \
"v_min_f32 v171, v171, v43\n\t" \
"v_add_f32 v44, 0xf4600000, v37\n\t" \
"v_min_f32 v172, v172, v44\n\t" \
"v_add_f32 v45, 0xf4640000, v37\n\t" \
"v_min_f32 v173, v173, v45\n\t" \
"v_add_f32 v46, 0xf4680000, v37\n\t" \
"v_min_f32 v174, v174, v46\n\t" \
"v_add_f32 v47, 0xf46c0000, v37\n\t" \
"v_min_f32 v175, v175, v47\n\t" \
"v_add_f32 v40, 0x74000000, v37\n\t" \
"v_min_f32 v144, v144, v40\n\t" \
"v_add_f32 v41, 0x73f80000, v37\n\t" \
"v_min_f32 v145, v145, v41\n\t" \
"v_add_f32 v42, 0x73f00000, v37\n\t" \
"v_min_f32 v146, v146, v42\n\t" \
"v_add_f32 v43, 0x73e80000, v37\n\t" \
"v_min_f32 v147, v147, v43\n\t" \
"v_add_f32 v44, 0x73c00000, v37\n\t" \
"v_min_f32 v148, v148, v44\n\t" \
"v_add_f32 v45, 0x73b80000, v37\n\t" \
"v_min_f32 v149, v149, v45\n\t" \
"v_add_f32 v46, 0x73b00000, v37\n\t" \
"v_min_f32 v150, v150, v46\n\t" \
"v_add_f32 v47, 0x73a80000, v37\n\t" \
"v_min_f32 v151, v151, v47\n\t" \
"v_add_f32 v40, 0x73800000, v37\n\t" \
"v_min_f32 v152, v152, v40\n\t" \
"v_add_f32 v41, 0x73700000, v37\n\t" \
"v_min_f32 v153, v153, v41\n\t" \
"v_add_f32 v42, 0x73600000, v37\n\t" \
"v_min_f32 v154, v154, v42\n\t" \
"v_add_f32 v43, 0x73500000, v37\n\t" \
"v_min_f32 v155, v155, v43\n\t" \
"v_add_f32 v44, 0x73000000, v37\n\t" \
"v_min_f32 v156, v156, v44\n\t" \
"v_add_f32 v45, 0x72e00000, v37\n\t" \
"v_min_f32 v157, v157, v45\n\t" \
"v_add_f32 v46, 0x72c00000, v37\n\t" \
"v_min_f32 v158, v158, v46\n\t" \
"v_add_f32 v47, 0x72a00000, v37\n\t" \
"v_min_f32 v159, v159, v47\n\t" \
"v_max3_f32 v18, v128, v129, v130\n\t" \
"v_max3_f32 v19, v144, v145, v146\n\t" \
"v_max3_f32 v18, v18, v131, v132\n\t" \
"v_max3_f32 v19, v19, v147, v148\n\t" \
"v_max3_f32 v18, v18, v133, v134\n\t" \
"v_max3_f32 v19, v19, v149, v150\n\t" \
"v_max3_f32 v18, v18, v135, v136\n\t" \
"v_max3_f32 v19, v19, v151, v152\n\t" \
"v_max3_f32 v18, v18, v137, v138\n\t" \
"v_max3_f32 v19, v19, v153, v154\n\t" \
"v_max3_f32 v18, v18, v139, v140\n\t" \
"v_max3_f32 v19, v19, v155, v156\n\t" \
"v_max3_f32 v18, v18, v141, v142\n\t" \
"v_max3_f32 v19, v19, v157, v158\n\t" \
"v_max_f32 v18, v18, v143\n\t" \
"v_max_f32 v19, v19, v159\n\t" \
"NOMASK_p1_%=:\n\t" \
"s_waitcnt lgkmcnt(7)\n\t" \
"v_mfma_f32_32x32x16_bf16 a[0:15], a[224:227], v[192:195], 0\n\t" \
"v_exp_f32 v107, v107\n\t" \
"v_add_f32 v28, v28, v106\n\t" \
"v_cvt_pk_bf16_f32 v204, v104, v105\n\t" \
"v_mfma_f32_32x32x16_bf16 a[16:31], a[224:227], v[208:211], 0\n\t" \
"ds_read_b128 a[224:227], v17 offset:8192\n\t" \
"v_exp_f32 v108, v108\n\t" \
"v_add_f32 v29, v29, v107\n\t" \
"s_waitcnt lgkmcnt(7)\n\t" \
"v_mfma_f32_32x32x16_bf16 a[32:47], a[228:231], v[192:195], 0\n\t" \
"v_exp_f32 v109, v109\n\t" \
"v_add_f32 v28, v28, v108\n\t" \
"v_cvt_pk_bf16_f32 v205, v106, v107\n\t" \
"v_max3_f32 v18, v18, v160, v161\n\t" \
"v_max3_f32 v19, v19, v176, v177\n\t" \
"v_mfma_f32_32x32x16_bf16 a[48:63], a[228:231], v[208:211], 0\n\t" \
"ds_read_b128 a[228:231], v17 offset:9216\n\t" \
"v_exp_f32 v110, v110\n\t" \
"v_add_f32 v29, v29, v109\n\t" \
"v_max3_f32 v18, v18, v162, v163\n\t" \
"s_waitcnt lgkmcnt(7)\n\t" \
"v_mfma_f32_32x32x16_bf16 a[64:79], a[232:235], v[192:195], 0\n\t" \
"v_exp_f32 v111, v111\n\t" \
"v_add_f32 v28, v28, v110\n\t" \
"v_cvt_pk_bf16_f32 v206, v108, v109\n\t" \
"v_max3_f32 v19, v19, v178, v179\n\t" \
"v_max3_f32 v18, v18, v164, v165\n\t" \
"v_mfma_f32_32x32x16_bf16 a[80:95], a[232:235], v[208:211], 0\n\t" \
"ds_read_b128 a[232:235], v17 offset:10240\n\t" \
"v_exp_f32 v120, v120\n\t" \
"v_add_f32 v29, v29, v111\n\t" \
"v_max3_f32 v19, v19, v180, v181\n\t" \
"s_waitcnt lgkmcnt(7)\n\t" \
"v_mfma_f32_32x32x16_bf16 a[96:111], a[236:239], v[192:195], 0\n\t" \
"v_exp_f32 v121, v121\n\t" \
"v_add_f32 v30, v30, v120\n\t" \
"v_cvt_pk_bf16_f32 v207, v110, v111\n\t" \
"v_max3_f32 v18, v18, v166, v167\n\t" \
"v_max3_f32 v19, v19, v182, v183\n\t" \
"v_mfma_f32_32x32x16_bf16 a[112:127], a[236:239], v[208:211], 0\n\t" \
"ds_read_b128 a[236:239], v17 offset:11264\n\t" \
"v_exp_f32 v122, v122\n\t" \
"v_add_f32 v31, v31, v121\n\t" \
"v_max3_f32 v18, v18, v168, v169\n\t" \
"s_waitcnt lgkmcnt(7)\n\t" \
"v_mfma_f32_32x32x16_bf16 a[0:15], a[240:243], v[196:199], a[0:15]\n\t" \
"v_exp_f32 v123, v123\n\t" \
"v_add_f32 v30, v30, v122\n\t" \
"v_cvt_pk_bf16_f32 v220, v120, v121\n\t" \
"v_max3_f32 v19, v19, v184, v185\n\t" \
"v_max3_f32 v18, v18, v170, v171\n\t" \
"v_mfma_f32_32x32x16_bf16 a[16:31], a[240:243], v[212:215], a[16:31]\n\t" \
"ds_read_b128 a[240:243], v17 offset:12288\n\t" \
"v_exp_f32 v124, v124\n\t" \
"v_add_f32 v31, v31, v123\n\t" \
"v_max3_f32 v19, v19, v186, v187\n\t" \
"v_max3_f32 v18, v18, v172, v173\n\t" \
"s_waitcnt lgkmcnt(7)\n\t" \
"v_mfma_f32_32x32x16_bf16 a[32:47], a[244:247], v[196:199], a[32:47]\n\t" \
"v_exp_f32 v125, v125\n\t" \
"v_add_f32 v30, v30, v124\n\t" \
"v_cvt_pk_bf16_f32 v221, v122, v123\n\t" \
"v_max3_f32 v19, v19, v188, v189\n\t" \
"v_max3_f32 v18, v18, v174, v175\n\t" \
"v_max3_f32 v19, v19, v190, v191\n\t" \
"v_mfma_f32_32x32x16_bf16 a[48:63], a[244:247], v[212:215], a[48:63]\n\t" \
"ds_read_b128 a[244:247], v17 offset:13312\n\t" \
"v_exp_f32 v126, v126\n\t" \
"v_add_f32 v31, v31, v125\n\t" \
"v_mov_b32 v20, v18\n\t" \
"v_mov_b32 v21, v19\n\t" \
"s_waitcnt lgkmcnt(7)\n\t" \
"v_mfma_f32_32x32x16_bf16 a[64:79], a[248:251], v[196:199], a[64:79]\n\t" \
"v_exp_f32 v127, v127\n\t" \
"v_add_f32 v30, v30, v126\n\t" \
"v_cvt_pk_bf16_f32 v222, v124, v125\n\t" \
"v_permlane32_swap_b32 v18, v20\n\t" \
"v_permlane32_swap_b32 v19, v21\n\t" \
"v_max_f32 v20, v20, v18\n\t" \
"v_max_f32 v21, v21, v19\n\t" \
"v_add_f32 v31, v31, v127\n\t" \
"v_max_f32 v22, v20, v21\n\t" \
"v_cmp_lt_f32 vcc, 0x41000000, v22\n\t" \
"s_cbranch_vccnz RARE1_p1_%=\n\t" \
"BACK1_p1_%=:\n\t" \
"v_mfma_f32_32x32x16_bf16 a[80:95], a[248:251], v[212:215], a[80:95]\n\t" \
"ds_read_b128 a[248:251], v17 offset:14336\n\t" \
"v_cvt_pk_bf16_f32 v223, v126, v127\n\t" \
"v_exp_f32 v128, v128\n\t" \
"s_waitcnt lgkmcnt(7)\n\t" \
"v_mfma_f32_32x32x16_bf16 a[96:111], a[252:255], v[196:199], a[96:111]\n\t" \
"v_exp_f32 v129, v129\n\t" \
"v_add_f32 v28, v28, v128\n\t" \
"v_mfma_f32_32x32x16_bf16 a[112:127], a[252:255], v[212:215], a[112:127]\n\t" \
"ds_read_b128 a[252:255], v17 offset:15360\n\t" \
"v_exp_f32 v130, v130\n\t" \
"v_add_f32 v29, v29, v129\n\t" \
"s_waitcnt lgkmcnt(7)\n\t" \
"v_mfma_f32_32x32x16_bf16 a[0:15], a[224:227], v[200:203], a[0:15]\n\t" \
"v_exp_f32 v131, v131\n\t" \
"v_add_f32 v28, v28, v130\n\t" \
"v_cvt_pk_bf16_f32 v192, v128, v129\n\t" \
"v_mfma_f32_32x32x16_bf16 a[16:31], a[224:227], v[216:219], a[16:31]\n\t" \
"v_exp_f32 v132, v132\n\t" \
"v_add_f32 v29, v29, v131\n\t" \
"s_waitcnt lgkmcnt(6)\n\t" \
"v_mfma_f32_32x32x16_bf16 a[32:47], a[228:231], v[200:203], a[32:47]\n\t" \
"v_exp_f32 v133, v133\n\t" \
"v_add_f32 v28, v28, v132\n\t" \
"v_cvt_pk_bf16_f32 v193, v130, v131\n\t" \
"s_waitcnt lgkmcnt(0)\n\t" \
"s_waitcnt vmcnt(0)\n\t" \
"s_barrier\n\t" \
"v_xor_b32 v17, 0x8000, v17\n\t" \
"v_mfma_f32_32x32x16_bf16 a[48:63], a[228:231], v[216:219], a[48:63]\n\t" \
"v_exp_f32 v134, v134\n\t" \
"v_add_f32 v29, v29, v133\n\t" \
"ds_read_b128 a[192:195], v16 offset:0\n\t" \
"s_mov_b32 m0, s48\n\t" \
"v_mfma_f32_32x32x16_bf16 a[64:79], a[232:235], v[200:203], a[64:79]\n\t" \
"v_exp_f32 v135, v135\n\t" \
"v_add_f32 v28, v28, v134\n\t" \
"v_cvt_pk_bf16_f32 v194, v132, v133\n\t" \
"ds_read_b128 a[196:199], v16 offset:1024\n\t" \
"buffer_load_dwordx4 v36, %0, s42 offen sc1 lds\n\t" \
"v_mfma_f32_32x32x16_bf16 a[80:95], a[232:235], v[216:219], a[80:95]\n\t" \
"v_exp_f32 v144, v144\n\t" \
"v_add_f32 v29, v29, v135\n\t" \
"buffer_load_dwordx4 v36, %0, s42 offen offset:1024 sc1 lds\n\t" \
"v_mfma_f32_32x32x16_bf16 a[96:111], a[236:239], v[200:203], a[96:111]\n\t" \
"v_exp_f32 v145, v145\n\t" \
"v_add_f32 v30, v30, v144\n\t" \
"v_cvt_pk_bf16_f32 v195, v134, v135\n\t" \
"ds_read_b128 a[200:203], v16 offset:2048\n\t" \
"buffer_load_dwordx4 v36, %0, s42 offen offset:2048 sc1 lds\n\t" \
"v_mfma_f32_32x32x16_bf16 a[112:127], a[236:239], v[216:219], a[112:127]\n\t" \
"v_exp_f32 v146, v146\n\t" \
"v_add_f32 v31, v31, v145\n\t" \
"ds_read_b128 a[204:207], v16 offset:3072\n\t" \
"v_mfma_f32_32x32x16_bf16 a[0:15], a[240:243], v[204:207], a[0:15]\n\t" \
"v_exp_f32 v147, v147\n\t" \
"v_add_f32 v30, v30, v146\n\t" \
"v_cvt_pk_bf16_f32 v208, v144, v145\n\t" \
"buffer_load_dwordx4 v36, %0, s42 offen offset:3072 sc1 lds\n\t" \
"s_add_u32 m0, s48, 4096\n\t" \
"s_add_u32 s54, s42, 4096\n\t" \
"v_mfma_f32_32x32x16_bf16 a[16:31], a[240:243], v[220:223], a[16:31]\n\t" \
"v_exp_f32 v148, v148\n\t" \
"v_add_f32 v31, v31, v147\n\t" \
"ds_read_b128 a[208:211], v16 offset:4096\n\t" \
"buffer_load_dwordx4 v36, %0, s54 offen sc1 lds\n\t" \
"v_mfma_f32_32x32x16_bf16 a[32:47], a[244:247], v[204:207], a[32:47]\n\t" \
"v_exp_f32 v149, v149\n\t" \
"v_add_f32 v30, v30, v148\n\t" \
"v_cvt_pk_bf16_f32 v209, v146, v147\n\t" \
"ds_read_b128 a[212:215], v16 offset:5120\n\t" \
"v_mfma_f32_32x32x16_bf16 a[48:63], a[244:247], v[220:223], a[48:63]\n\t" \
"v_exp_f32 v150, v150\n\t" \
"v_add_f32 v31, v31, v149\n\t" \
"buffer_load_dwordx4 v36, %0, s54 offen offset:1024 sc1 lds\n\t" \
"v_mfma_f32_32x32x16_bf16 a[64:79], a[248:251], v[204:207], a[64:79]\n\t" \
"v_exp_f32 v151, v151\n\t" \
"v_add_f32 v30, v30, v150\n\t" \
"v_cvt_pk_bf16_f32 v210, v148, v149\n\t" \
"ds_read_b128 a[216:219], v16 offset:6144\n\t" \
"buffer_load_dwordx4 v36, %0, s54 offen offset:2048 sc1 lds\n\t" \
"v_mfma_f32_32x32x16_bf16 a[80:95], a[248:251], v[220:223], a[80:95]\n\t" \
"v_exp_f32 v136, v136\n\t" \
"v_add_f32 v31, v31, v151\n\t" \
"ds_read_b128 a[220:223], v16 offset:7168\n\t" \
"v_mfma_f32_32x32x16_bf16 a[96:111], a[252:255], v[204:207], a[96:111]\n\t" \
"v_exp_f32 v137, v137\n\t" \
"v_add_f32 v28, v28, v136\n\t" \
"v_cvt_pk_bf16_f32 v211, v150, v151\n\t" \
"buffer_load_dwordx4 v36, %0, s54 offen offset:3072 sc1 lds\n\t" \
"v_mfma_f32_32x32x16_bf16 a[112:127], a[252:255], v[220:223], a[112:127]\n\t" \
"v_exp_f32 v138, v138\n\t" \
"v_add_f32 v29, v29, v137\n\t" \
"s_xor_b32 s48, s48, 0x8000\n\t" \
"s_add_u32 s44, s44, 1\n\t" \
"s_add_u32 s42, s42, 0x8000\n\t" \
"s_cmp_eq_u32 s44, s45\n\t" \
"s_cselect_b32 s44, 0, s44\n\t" \
"s_cselect_b32 s42, %1, s42\n\t" \
"s_cselect_b32 s45, %6, s45\n\t" \
"s_cmp_lg_u32 s50, 0\n\t" \
"s_cbranch_scc1 RESC_p1_%=\n\t" \
"RESCB_p1_%=:\n\t" \
"s_cmp_eq_u32 s41, s40\n\t" \
"s_cbranch_scc1 DRAIN1_%=\n\t" \
"LOOP_%=:\n\t" \
"s_waitcnt lgkmcnt(7)\n\t" \
"v_mfma_f32_32x32x16_bf16 v[64:79], a[192:195], a[128:131], v[224:239]\n\t" \
"s_mov_b32 m0, s49\n\t" \
"v_exp_f32 v139, v139\n\t" \
"v_add_f32 v28, v28, v138\n\t" \
"v_cvt_pk_bf16_f32 v196, v136, v137\n\t" \
"v_mfma_f32_32x32x16_bf16 v[80:95], a[192:195], a[160:163], v[240:255]\n\t" \
"ds_read_b128 a[192:195], v16 offset:8192\n\t" \
"v_exp_f32 v140, v140\n\t" \
"v_add_f32 v29, v29, v139\n\t" \
"s_waitcnt lgkmcnt(7)\n\t" \
"v_mfma_f32_32x32x16_bf16 v[64:79], a[196:199], a[132:135], v[64:79]\n\t" \
"buffer_load_dwordx4 v36, %0, s43 offen sc1 lds\n\t" \
"v_exp_f32 v141, v141\n\t" \
"v_add_f32 v28, v28, v140\n\t" \
"v_cvt_pk_bf16_f32 v197, v138, v139\n\t" \
"v_mfma_f32_32x32x16_bf16 v[80:95], a[196:199], a[164:167], v[80:95]\n\t" \
"ds_read_b128 a[196:199], v16 offset:9216\n\t" \
"v_exp_f32 v142, v142\n\t" \
"v_add_f32 v29, v29, v141\n\t" \
"s_waitcnt lgkmcnt(7)\n\t" \
"v_mfma_f32_32x32x16_bf16 v[64:79], a[200:203], a[136:139], v[64:79]\n\t" \
"buffer_load_dwordx4 v36, %0, s43 offen offset:1024 sc1 lds\n\t" \
"v_exp_f32 v143, v143\n\t" \
"v_add_f32 v28, v28, v142\n\t" \
"v_cvt_pk_bf16_f32 v198, v140, v141\n\t" \
"v_mfma_f32_32x32x16_bf16 v[80:95], a[200:203], a[168:171], v[80:95]\n\t" \
"ds_read_b128 a[200:203], v16 offset:10240\n\t" \
"v_exp_f32 v152, v152\n\t" \
"v_add_f32 v29, v29, v143\n\t" \
"s_waitcnt lgkmcnt(7)\n\t" \
"v_mfma_f32_32x32x16_bf16 v[64:79], a[204:207], a[140:143], v[64:79]\n\t" \
"buffer_load_dwordx4 v36, %0, s43 offen offset:2048 sc1 lds\n\t" \
"v_exp_f32 v153, v153\n\t" \
"v_add_f32 v30, v30, v152\n\t" \
"v_cvt_pk_bf16_f32 v199, v142, v143\n\t" \
"v_mfma_f32_32x32x16_bf16 v[80:95], a[204:207], a[172:175], v[80:95]\n\t" \
"ds_read_b128 a[204:207], v16 offset:11264\n\t" \
"v_exp_f32 v154, v154\n\t" \
"v_add_f32 v31, v31, v153\n\t" \
"s_waitcnt lgkmcnt(7)\n\t" \
"v_mfma_f32_32x32x16_bf16 v[64:79], a[208:211], a[144:147], v[64:79]\n\t" \
"buffer_load_dwordx4 v36, %0, s43 offen offset:3072 sc1 lds\n\t" \
"v_exp_f32 v155, v155\n\t" \
"v_add_f32 v30, v30, v154\n\t" \
"v_cvt_pk_bf16_f32 v212, v152, v153\n\t" \
"v_mfma_f32_32x32x16_bf16 v[80:95], a[208:211], a[176:179], v[80:95]\n\t" \
"ds_read_b128 a[208:211], v16 offset:12288\n\t" \
"s_add_u32 m0, s49, 4096\n\t" \
"s_add_u32 s55, s43, 4096\n\t" \
"v_exp_f32 v156, v156\n\t" \
"v_add_f32 v31, v31, v155\n\t" \
"s_waitcnt lgkmcnt(7)\n\t" \
"v_mfma_f32_32x32x16_bf16 v[64:79], a[212:215], a[148:151], v[64:79]\n\t" \
"buffer_load_dwordx4 v36, %0, s55 offen sc1 lds\n\t" \
"v_exp_f32 v157, v157\n\t" \
"v_add_f32 v30, v30, v156\n\t" \
"v_cvt_pk_bf16_f32 v213, v154, v155\n\t" \
"v_mfma_f32_32x32x16_bf16 v[80:95], a[212:215], a[180:183], v[80:95]\n\t" \
"ds_read_b128 a[212:215], v16 offset:13312\n\t" \
"v_exp_f32 v158, v158\n\t" \
"v_add_f32 v31, v31, v157\n\t" \
"s_waitcnt lgkmcnt(7)\n\t" \
"v_mfma_f32_32x32x16_bf16 v[64:79], a[216:219], a[152:155], v[64:79]\n\t" \
"buffer_load_dwordx4 v36, %0, s55 offen offset:1024 sc1 lds\n\t" \
"v_exp_f32 v159, v159\n\t" \
"v_add_f32 v30, v30, v158\n\t" \
"v_cvt_pk_bf16_f32 v214, v156, v157\n\t" \
"v_mfma_f32_32x32x16_bf16 v[80:95], a[216:219], a[184:187], v[80:95]\n\t" \
"ds_read_b128 a[216:219], v16 offset:14336\n\t" \
"v_exp_f32 v160, v160\n\t" \
"v_add_f32 v31, v31, v159\n\t" \
"s_waitcnt lgkmcnt(7)\n\t" \
"v_mfma_f32_32x32x16_bf16 v[64:79], a[220:223], a[156:159], v[64:79]\n\t" \
"buffer_load_dwordx4 v36, %0, s55 offen offset:2048 sc1 lds\n\t" \
"v_exp_f32 v161, v161\n\t" \
"v_add_f32 v28, v28, v160\n\t" \
"v_cvt_pk_bf16_f32 v215, v158, v159\n\t" \
"v_mfma_f32_32x32x16_bf16 v[80:95], a[220:223], a[188:191], v[80:95]\n\t" \
"ds_read_b128 a[220:223], v16 offset:15360\n\t" \
"v_exp_f32 v162, v162\n\t" \
"v_add_f32 v29, v29, v161\n\t" \
"s_waitcnt lgkmcnt(7)\n\t" \
"v_mfma_f32_32x32x16_bf16 v[96:111], a[192:195], a[128:131], v[224:239]\n\t" \
"buffer_load_dwordx4 v36, %0, s55 offen offset:3072 sc1 lds\n\t" \
"v_exp_f32 v163, v163\n\t" \
"v_add_f32 v28, v28, v162\n\t" \
"v_cvt_pk_bf16_f32 v200, v160, v161\n\t" \
"v_mfma_f32_32x32x16_bf16 v[112:127], a[192:195], a[160:163], v[240:255]\n\t" \
"ds_read_b128 a[224:227], v17 offset:0\n\t" \
"v_exp_f32 v164, v164\n\t" \
"v_add_f32 v29, v29, v163\n\t" \
"s_waitcnt lgkmcnt(7)\n\t" \
"v_mfma_f32_32x32x16_bf16 v[96:111], a[196:199], a[132:135], v[96:111]\n\t" \
"s_xor_b32 s49, s49, 0x8000\n\t" \
"v_exp_f32 v165, v165\n\t" \
"v_add_f32 v28, v28, v164\n\t" \
"v_cvt_pk_bf16_f32 v201, v162, v163\n\t" \
"v_mfma_f32_32x32x16_bf16 v[112:127], a[196:199], a[164:167], v[112:127]\n\t" \
"ds_read_b128 a[228:231], v17 offset:1024\n\t" \
"v_exp_f32 v166, v166\n\t" \
"v_add_f32 v29, v29, v165\n\t" \
"s_waitcnt lgkmcnt(7)\n\t" \
"v_mfma_f32_32x32x16_bf16 v[96:111], a[200:203], a[136:139], v[96:111]\n\t" \
"s_add_u32 s46, s46, 1\n\t" \
"s_add_u32 s43, s43, 0x8000\n\t" \
"v_exp_f32 v167, v167\n\t" \
"v_add_f32 v28, v28, v166\n\t" \
"v_cvt_pk_bf16_f32 v202, v164, v165\n\t" \
"v_max3_f32 v18, v64, v65, v66\n\t" \
"v_max3_f32 v19, v80, v81, v82\n\t" \
"v_mfma_f32_32x32x16_bf16 v[112:127], a[200:203], a[168:171], v[112:127]\n\t" \
"ds_read_b128 a[232:235], v17 offset:2048\n\t" \
"v_exp_f32 v176, v176\n\t" \
"v_add_f32 v29, v29, v167\n\t" \
"v_max3_f32 v18, v18, v67, v68\n\t" \
"s_waitcnt lgkmcnt(7)\n\t" \
"v_mfma_f32_32x32x16_bf16 v[96:111], a[204:207], a[140:143], v[96:111]\n\t" \
"s_cmp_eq_u32 s46, s47\n\t" \
"s_cselect_b32 s46, 0, s46\n\t" \
"s_cselect_b32 s43, %2, s43\n\t" \
"s_cselect_b32 s47, %6, s47\n\t" \
"v_exp_f32 v177, v177\n\t" \
"v_add_f32 v30, v30, v176\n\t" \
"v_cvt_pk_bf16_f32 v203, v166, v167\n\t" \
"v_max3_f32 v19, v19, v83, v84\n\t" \
"v_mfma_f32_32x32x16_bf16 v[112:127], a[204:207], a[172:175], v[112:127]\n\t" \
"ds_read_b128 a[236:239], v17 offset:3072\n\t" \
"v_exp_f32 v178, v178\n\t" \
"v_add_f32 v31, v31, v177\n\t" \
"v_max3_f32 v18, v18, v69, v70\n\t" \
"v_max3_f32 v19, v19, v85, v86\n\t" \
"s_waitcnt lgkmcnt(7)\n\t" \
"v_mfma_f32_32x32x16_bf16 v[96:111], a[208:211], a[144:147], v[96:111]\n\t" \
"v_exp_f32 v179, v179\n\t" \
"v_add_f32 v30, v30, v178\n\t" \
"v_cvt_pk_bf16_f32 v216, v176, v177\n\t" \
"v_max3_f32 v18, v18, v71, v72\n\t" \
"v_mfma_f32_32x32x16_bf16 v[112:127], a[208:211], a[176:179], v[112:127]\n\t" \
"ds_read_b128 a[240:243], v17 offset:4096\n\t" \
"v_exp_f32 v180, v180\n\t" \
"v_add_f32 v31, v31, v179\n\t" \
"v_max3_f32 v19, v19, v87, v88\n\t" \
"s_waitcnt lgkmcnt(7)\n\t" \
"v_mfma_f32_32x32x16_bf16 v[96:111], a[212:215], a[148:151], v[96:111]\n\t" \
"v_exp_f32 v181, v181\n\t" \
"v_add_f32 v30, v30, v180\n\t" \
"v_cvt_pk_bf16_f32 v217, v178, v179\n\t" \
"v_max3_f32 v18, v18, v73, v74\n\t" \
"v_max3_f32 v19, v19, v89, v90\n\t" \
"v_mfma_f32_32x32x16_bf16 v[112:127], a[212:215], a[180:183], v[112:127]\n\t" \
"ds_read_b128 a[244:247], v17 offset:5120\n\t" \
"v_exp_f32 v182, v182\n\t" \
"v_add_f32 v31, v31, v181\n\t" \
"v_max3_f32 v18, v18, v75, v76\n\t" \
"s_waitcnt lgkmcnt(7)\n\t" \
"v_mfma_f32_32x32x16_bf16 v[96:111], a[216:219], a[152:155], v[96:111]\n\t" \
"v_exp_f32 v183, v183\n\t" \
"v_add_f32 v30, v30, v182\n\t" \
"v_cvt_pk_bf16_f32 v218, v180, v181\n\t" \
"v_max3_f32 v19, v19, v91, v92\n\t" \
"v_mfma_f32_32x32x16_bf16 v[112:127], a[216:219], a[184:187], v[112:127]\n\t" \
"ds_read_b128 a[248:251], v17 offset:6144\n\t" \
"v_exp_f32 v168, v168\n\t" \
"v_add_f32 v31, v31, v183\n\t" \
"v_max3_f32 v18, v18, v77, v78\n\t" \
"v_max3_f32 v19, v19, v93, v94\n\t" \
"s_waitcnt lgkmcnt(7)\n\t" \
"v_mfma_f32_32x32x16_bf16 v[96:111], a[220:223], a[156:159], v[96:111]\n\t" \
"v_exp_f32 v169, v169\n\t" \
"v_add_f32 v28, v28, v168\n\t" \
"v_cvt_pk_bf16_f32 v219, v182, v183\n\t" \
"v_max_f32 v18, v18, v79\n\t" \
"v_mfma_f32_32x32x16_bf16 v[112:127], a[220:223], a[188:191], v[112:127]\n\t" \
"ds_read_b128 a[252:255], v17 offset:7168\n\t" \
"v_exp_f32 v170, v170\n\t" \
"v_add_f32 v29, v29, v169\n\t" \
"v_max_f32 v19, v19, v95\n\t" \
"v_xor_b32 v16, 0x8000, v16\n\t" \
"s_add_u32 s41, s41, 1\n\t" \
"s_cmp_lg_u32 s41, s40\n\t" \
"s_cbranch_scc1 NOMASK_l0_%=\n\t" \
"v_add_f32 v40, 0x00000000, v37\n\t" \
"v_min_f32 v64, v64, v40\n\t" \
"s_nop 1\n\t" \
"v_min_f32 v112, v112, v40\n\t" \
"v_add_f32 v41, 0xf1800000, v37\n\t" \
"v_min_f32 v65, v65, v41\n\t" \
"v_min_f32 v113, v113, v41\n\t" \
"v_add_f32 v42, 0xf2000000, v37\n\t" \
"v_min_f32 v66, v66, v42\n\t" \
"v_min_f32 v114, v114, v42\n\t" \
"v_add_f32 v43, 0xf2400000, v37\n\t" \
"v_min_f32 v67, v67, v43\n\t" \
"v_min_f32 v115, v115, v43\n\t" \
"v_add_f32 v44, 0xf3000000, v37\n\t" \
"v_min_f32 v68, v68, v44\n\t" \
"v_min_f32 v116, v116, v44\n\t" \
"v_add_f32 v45, 0xf3100000, v37\n\t" \
"v_min_f32 v69, v69, v45\n\t" \
"v_min_f32 v117, v117, v45\n\t" \
"v_add_f32 v46, 0xf3200000, v37\n\t" \
"v_min_f32 v70, v70, v46\n\t" \
"v_min_f32 v118, v118, v46\n\t" \
"v_add_f32 v47, 0xf3300000, v37\n\t" \
"v_min_f32 v71, v71, v47\n\t" \
"v_min_f32 v119, v119, v47\n\t" \
"v_add_f32 v40, 0xf3800000, v37\n\t" \
"v_min_f32 v72, v72, v40\n\t" \
"v_min_f32 v120, v120, v40\n\t" \
"v_add_f32 v41, 0xf3880000, v37\n\t" \
"v_min_f32 v73, v73, v41\n\t" \
"v_min_f32 v121, v121, v41\n\t" \
"v_add_f32 v42, 0xf3900000, v37\n\t" \
"v_min_f32 v74, v74, v42\n\t" \
"v_min_f32 v122, v122, v42\n\t" \
"v_add_f32 v43, 0xf3980000, v37\n\t" \
"v_min_f32 v75, v75, v43\n\t" \
"v_min_f32 v123, v123, v43\n\t" \
"v_add_f32 v44, 0xf3c00000, v37\n\t" \
"v_min_f32 v76, v76, v44\n\t" \
"v_min_f32 v124, v124, v44\n\t" \
"v_add_f32 v45, 0xf3c80000, v37\n\t" \
"v_min_f32 v77, v77, v45\n\t" \
"v_min_f32 v125, v125, v45\n\t" \
"v_add_f32 v46, 0xf3d00000, v37\n\t" \
"v_min_f32 v78, v78, v46\n\t" \
"v_min_f32 v126, v126, v46\n\t" \
"v_add_f32 v47, 0xf3d80000, v37\n\t" \
"v_min_f32 v79, v79, v47\n\t" \
"v_min_f32 v127, v127, v47\n\t" \
"v_add_f32 v40, 0xf4000000, v37\n\t" \
"v_min_f32 v96, v96, v40\n\t" \
"v_add_f32 v41, 0xf4040000, v37\n\t" \
"v_min_f32 v97, v97, v41\n\t" \
"v_add_f32 v42, 0xf4080000, v37\n\t" \
"v_min_f32 v98, v98, v42\n\t" \
"v_add_f32 v43, 0xf40c0000, v37\n\t" \
"v_min_f32 v99, v99, v43\n\t" \
"v_add_f32 v44, 0xf4200000, v37\n\t" \
"v_min_f32 v100, v100, v44\n\t" \
"v_add_f32 v45, 0xf4240000, v37\n\t" \
"v_min_f32 v101, v101, v45\n\t" \
"v_add_f32 v46, 0xf4280000, v37\n\t" \
"v_min_f32 v102, v102, v46\n\t" \
"v_add_f32 v47, 0xf42c0000, v37\n\t" \
"v_min_f32 v103, v103, v47\n\t" \
"v_add_f32 v40, 0xf4400000, v37\n\t" \
"v_min_f32 v104, v104, v40\n\t" \
"v_add_f32 v41, 0xf4440000, v37\n\t" \
"v_min_f32 v105, v105, v41\n\t" \
"v_add_f32 v42, 0xf4480000, v37\n\t" \
"v_min_f32 v106, v106, v42\n\t" \
"v_add_f32 v43, 0xf44c0000, v37\n\t" \
"v_min_f32 v107, v107, v43\n\t" \
"v_add_f32 v44, 0xf4600000, v37\n\t" \
"v_min_f32 v108, v108, v44\n\t" \
"v_add_f32 v45, 0xf4640000, v37\n\t" \
"v_min_f32 v109, v109, v45\n\t" \
"v_add_f32 v46, 0xf4680000, v37\n\t" \
"v_min_f32 v110, v110, v46\n\t" \
"v_add_f32 v47, 0xf46c0000, v37\n\t" \
"v_min_f32 v111, v111, v47\n\t" \
"v_add_f32 v40, 0x74000000, v37\n\t" \
"v_min_f32 v80, v80, v40\n\t" \
"v_add_f32 v41, 0x73f80000, v37\n\t" \
"v_min_f32 v81, v81, v41\n\t" \
"v_add_f32 v42, 0x73f00000, v37\n\t" \
"v_min_f32 v82, v82, v42\n\t" \
"v_add_f32 v43, 0x73e80000, v37\n\t" \
"v_min_f32 v83, v83, v43\n\t" \
"v_add_f32 v44, 0x73c00000, v37\n\t" \
"v_min_f32 v84, v84, v44\n\t" \
"v_add_f32 v45, 0x73b80000, v37\n\t" \
"v_min_f32 v85, v85, v45\n\t" \
"v_add_f32 v46, 0x73b00000, v37\n\t" \
"v_min_f32 v86, v86, v46\n\t" \
"v_add_f32 v47, 0x73a80000, v37\n\t" \
"v_min_f32 v87, v87, v47\n\t" \
"v_add_f32 v40, 0x73800000, v37\n\t" \
"v_min_f32 v88, v88, v40\n\t" \
"v_add_f32 v41, 0x73700000, v37\n\t" \
"v_min_f32 v89, v89, v41\n\t" \
"v_add_f32 v42, 0x73600000, v37\n\t" \
"v_min_f32 v90, v90, v42\n\t" \
"v_add_f32 v43, 0x73500000, v37\n\t" \
"v_min_f32 v91, v91, v43\n\t" \
"v_add_f32 v44, 0x73000000, v37\n\t" \
"v_min_f32 v92, v92, v44\n\t" \
"v_add_f32 v45, 0x72e00000, v37\n\t" \
"v_min_f32 v93, v93, v45\n\t" \
"v_add_f32 v46, 0x72c00000, v37\n\t" \
"v_min_f32 v94, v94, v46\n\t" \
"v_add_f32 v47, 0x72a00000, v37\n\t" \
"v_min_f32 v95, v95, v47\n\t" \
"v_max3_f32 v18, v64, v65, v66\n\t" \
"v_max3_f32 v19, v80, v81, v82\n\t" \
"v_max3_f32 v18, v18, v67, v68\n\t" \
"v_max3_f32 v19, v19, v83, v84\n\t" \
"v_max3_f32 v18, v18, v69, v70\n\t" \
"v_max3_f32 v19, v19, v85, v86\n\t" \
"v_max3_f32 v18, v18, v71, v72\n\t" \
"v_max3_f32 v19, v19, v87, v88\n\t" \
"v_max3_f32 v18, v18, v73, v74\n\t" \
"v_max3_f32 v19, v19, v89, v90\n\t" \
"v_max3_f32 v18, v18, v75, v76\n\t" \
"v_max3_f32 v19, v19, v91, v92\n\t" \
"v_max3_f32 v18, v18, v77, v78\n\t" \
"v_max3_f32 v19, v19, v93, v94\n\t" \
"v_max_f32 v18, v18, v79\n\t" \
"v_max_f32 v19, v19, v95\n\t" \
"NOMASK_l0_%=:\n\t" \
"s_waitcnt lgkmcnt(7)\n\t" \
"v_mfma_f32_32x32x16_bf16 a[0:15], a[224:227], v[192:195], a[0:15]\n\t" \
"v_exp_f32 v171, v171\n\t" \
"v_add_f32 v28, v28, v170\n\t" \
"v_cvt_pk_bf16_f32 v204, v168, v169\n\t" \
"v_mfma_f32_32x32x16_bf16 a[16:31], a[224:227], v[208:211], a[16:31]\n\t" \
"ds_read_b128 a[224:227], v17 offset:8192\n\t" \
"v_exp_f32 v172, v172\n\t" \
"v_add_f32 v29, v29, v171\n\t" \
"s_waitcnt lgkmcnt(7)\n\t" \
"v_mfma_f32_32x32x16_bf16 a[32:47], a[228:231], v[192:195], a[32:47]\n\t" \
"v_exp_f32 v173, v173\n\t" \
"v_add_f32 v28, v28, v172\n\t" \
"v_cvt_pk_bf16_f32 v205, v170, v171\n\t" \
"v_max3_f32 v18, v18, v96, v97\n\t" \
"v_max3_f32 v19, v19, v112, v113\n\t" \
"v_mfma_f32_32x32x16_bf16 a[48:63], a[228:231], v[208:211], a[48:63]\n\t" \
"ds_read_b128 a[228:231], v17 offset:9216\n\t" \
"v_exp_f32 v174, v174\n\t" \
"v_add_f32 v29, v29, v173\n\t" \
"v_max3_f32 v18, v18, v98, v99\n\t" \
"s_waitcnt lgkmcnt(7)\n\t" \
"v_mfma_f32_32x32x16_bf16 a[64:79], a[232:235], v[192:195], a[64:79]\n\t" \
"v_exp_f32 v175, v175\n\t" \
"v_add_f32 v28, v28, v174\n\t" \
"v_cvt_pk_bf16_f32 v206, v172, v173\n\t" \
"v_max3_f32 v19, v19, v114, v115\n\t" \
"v_max3_f32 v18, v18, v100, v101\n\t" \
"v_mfma_f32_32x32x16_bf16 a[80:95], a[232:235], v[208:211], a[80:95]\n\t" \
"ds_read_b128 a[232:235], v17 offset:10240\n\t" \
"v_exp_f32 v184, v184\n\t" \
"v_add_f32 v29, v29, v175\n\t" \
"v_max3_f32 v19, v19, v116, v117\n\t" \
"s_waitcnt lgkmcnt(7)\n\t" \
"v_mfma_f32_32x32x16_bf16 a[96:111], a[236:239], v[192:195], a[96:111]\n\t" \
"v_exp_f32 v185, v185\n\t" \
"v_add_f32 v30, v30, v184\n\t" \
"v_cvt_pk_bf16_f32 v207, v174, v175\n\t" \
"v_max3_f32 v18, v18, v102, v103\n\t" \
"v_max3_f32 v19, v19, v118, v119\n\t" \
"v_mfma_f32_32x32x16_bf16 a[112:127], a[236:239], v[208:211], a[112:127]\n\t" \
"ds_read_b128 a[236:239], v17 offset:11264\n\t" \
"v_exp_f32 v186, v186\n\t" \
"v_add_f32 v31, v31, v185\n\t" \
"v_max3_f32 v18, v18, v104, v105\n\t" \
"s_waitcnt lgkmcnt(7)\n\t" \
"v_mfma_f32_32x32x16_bf16 a[0:15], a[240:243], v[196:199], a[0:15]\n\t" \
"v_exp_f32 v187, v187\n\t" \
"v_add_f32 v30, v30, v186\n\t" \
"v_cvt_pk_bf16_f32 v220, v184, v185\n\t" \
"v_max3_f32 v19, v19, v120, v121\n\t" \
"v_max3_f32 v18, v18, v106, v107\n\t" \
"v_mfma_f32_32x32x16_bf16 a[16:31], a[240:243], v[212:215], a[16:31]\n\t" \
"ds_read_b128 a[240:243], v17 offset:12288\n\t" \
"v_exp_f32 v188, v188\n\t" \
"v_add_f32 v31, v31, v187\n\t" \
"v_max3_f32 v19, v19, v122, v123\n\t" \
"v_max3_f32 v18, v18, v108, v109\n\t" \
"s_waitcnt lgkmcnt(7)\n\t" \
"v_mfma_f32_32x32x16_bf16 a[32:47], a[244:247], v[196:199], a[32:47]\n\t" \
"v_exp_f32 v189, v189\n\t" \
"v_add_f32 v30, v30, v188\n\t" \
"v_cvt_pk_bf16_f32 v221, v186, v187\n\t" \
"v_max3_f32 v19, v19, v124, v125\n\t" \
"v_max3_f32 v18, v18, v110, v111\n\t" \
"v_max3_f32 v19, v19, v126, v127\n\t" \
"v_mfma_f32_32x32x16_bf16 a[48:63], a[244:247], v[212:215], a[48:63]\n\t" \
"ds_read_b128 a[244:247], v17 offset:13312\n\t" \
"v_exp_f32 v190, v190\n\t" \
"v_add_f32 v31, v31, v189\n\t" \
"v_mov_b32 v20, v18\n\t" \
"v_mov_b32 v21, v19\n\t" \
"s_waitcnt lgkmcnt(7)\n\t" \
"v_mfma_f32_32x32x16_bf16 a[64:79], a[248:251], v[196:199], a[64:79]\n\t" \
"v_exp_f32 v191, v191\n\t" \
"v_add_f32 v30, v30, v190\n\t" \
"v_cvt_pk_bf16_f32 v222, v188, v189\n\t" \
"v_permlane32_swap_b32 v18, v20\n\t" \
"v_permlane32_swap_b32 v19, v21\n\t" \
"v_max_f32 v20, v20, v18\n\t" \
"v_max_f32 v21, v21, v19\n\t" \
"v_add_f32 v31, v31, v191\n\t" \
"v_max_f32 v22, v20, v21\n\t" \
"v_cmp_lt_f32 vcc, 0x41000000, v22\n\t" \
"s_cbranch_vccnz RARE0_l0_%=\n\t" \
"BACK0_l0_%=:\n\t" \
"v_mfma_f32_32x32x16_bf16 a[80:95], a[248:251], v[212:215], a[80:95]\n\t" \
"ds_read_b128 a[248:251], v17 offset:14336\n\t" \
"v_cvt_pk_bf16_f32 v223, v190, v191\n\t" \
"v_exp_f32 v64, v64\n\t" \
"s_waitcnt lgkmcnt(7)\n\t" \
"v_mfma_f32_32x32x16_bf16 a[96:111], a[252:255], v[196:199], a[96:111]\n\t" \
"v_exp_f32 v65, v65\n\t" \
"v_add_f32 v28, v28, v64\n\t" \
"v_mfma_f32_32x32x16_bf16 a[112:127], a[252:255], v[212:215], a[112:127]\n\t" \
"ds_read_b128 a[252:255], v17 offset:15360\n\t" \
"v_exp_f32 v66, v66\n\t" \
"v_add_f32 v29, v29, v65\n\t" \
"s_waitcnt lgkmcnt(7)\n\t" \
"v_mfma_f32_32x32x16_bf16 a[0:15], a[224:227], v[200:203], a[0:15]\n\t" \
"v_exp_f32 v67, v67\n\t" \
"v_add_f32 v28, v28, v66\n\t" \
"v_cvt_pk_bf16_f32 v192, v64, v65\n\t" \
"v_mfma_f32_32x32x16_bf16 a[16:31], a[224:227], v[216:219], a[16:31]\n\t" \
"v_exp_f32 v68, v68\n\t" \
"v_add_f32 v29, v29, v67\n\t" \
"s_waitcnt lgkmcnt(6)\n\t" \
"v_mfma_f32_32x32x16_bf16 a[32:47], a[228:231], v[200:203], a[32:47]\n\t" \
"v_exp_f32 v69, v69\n\t" \
"v_add_f32 v28, v28, v68\n\t" \
"v_cvt_pk_bf16_f32 v193, v66, v67\n\t" \
"s_waitcnt lgkmcnt(0)\n\t" \
"s_waitcnt vmcnt(0)\n\t" \
"s_barrier\n\t" \
"v_xor_b32 v17, 0x8000, v17\n\t" \
"v_mfma_f32_32x32x16_bf16 a[48:63], a[228:231], v[216:219], a[48:63]\n\t" \
"v_exp_f32 v70, v70\n\t" \
"v_add_f32 v29, v29, v69\n\t" \
"ds_read_b128 a[192:195], v16 offset:0\n\t" \
"s_mov_b32 m0, s48\n\t" \
"v_mfma_f32_32x32x16_bf16 a[64:79], a[232:235], v[200:203], a[64:79]\n\t" \
"v_exp_f32 v71, v71\n\t" \
"v_add_f32 v28, v28, v70\n\t" \
"v_cvt_pk_bf16_f32 v194, v68, v69\n\t" \
"ds_read_b128 a[196:199], v16 offset:1024\n\t" \
"buffer_load_dwordx4 v36, %0, s42 offen sc1 lds\n\t" \
"v_mfma_f32_32x32x16_bf16 a[80:95], a[232:235], v[216:219], a[80:95]\n\t" \
"v_exp_f32 v80, v80\n\t" \
"v_add_f32 v29, v29, v71\n\t" \
"buffer_load_dwordx4 v36, %0, s42 offen offset:1024 sc1 lds\n\t" \
"v_mfma_f32_32x32x16_bf16 a[96:111], a[236:239], v[200:203], a[96:111]\n\t" \
"v_exp_f32 v81, v81\n\t" \
"v_add_f32 v30, v30, v80\n\t" \
"v_cvt_pk_bf16_f32 v195, v70, v71\n\t" \
"ds_read_b128 a[200:203], v16 offset:2048\n\t" \
"buffer_load_dwordx4 v36, %0, s42 offen offset:2048 sc1 lds\n\t" \
"v_mfma_f32_32x32x16_bf16 a[112:127], a[236:239], v[216:219], a[112:127]\n\t" \
"v_exp_f32 v82, v82\n\t" \
"v_add_f32 v31, v31, v81\n\t" \
"ds_read_b128 a[204:207], v16 offset:3072\n\t" \
"v_mfma_f32_32x32x16_bf16 a[0:15], a[240:243], v[204:207], a[0:15]\n\t" \
"v_exp_f32 v83, v83\n\t" \
"v_add_f32 v30, v30, v82\n\t" \
"v_cvt_pk_bf16_f32 v208, v80, v81\n\t" \
"buffer_load_dwordx4 v36, %0, s42 offen offset:3072 sc1 lds\n\t" \
"s_add_u32 m0, s48, 4096\n\t" \
"s_add_u32 s54, s42, 4096\n\t" \
"v_mfma_f32_32x32x16_bf16 a[16:31], a[240:243], v[220:223], a[16:31]\n\t" \
"v_exp_f32 v84, v84\n\t" \
"v_add_f32 v31, v31, v83\n\t" \
"ds_read_b128 a[208:211], v16 offset:4096\n\t" \
"buffer_load_dwordx4 v36, %0, s54 offen sc1 lds\n\t" \
"v_mfma_f32_32x32x16_bf16 a[32:47], a[244:247], v[204:207], a[32:47]\n\t" \
"v_exp_f32 v85, v85\n\t" \
"v_add_f32 v30, v30, v84\n\t" \
"v_cvt_pk_bf16_f32 v209, v82, v83\n\t" \
"ds_read_b128 a[212:215], v16 offset:5120\n\t" \
"v_mfma_f32_32x32x16_bf16 a[48:63], a[244:247], v[220:223], a[48:63]\n\t" \
"v_exp_f32 v86, v86\n\t" \
"v_add_f32 v31, v31, v85\n\t" \
"buffer_load_dwordx4 v36, %0, s54 offen offset:1024 sc1 lds\n\t" \
"v_mfma_f32_32x32x16_bf16 a[64:79], a[248:251], v[204:207], a[64:79]\n\t" \
"v_exp_f32 v87, v87\n\t" \
"v_add_f32 v30, v30, v86\n\t" \
"v_cvt_pk_bf16_f32 v210, v84, v85\n\t" \
"ds_read_b128 a[216:219], v16 offset:6144\n\t" \
"buffer_load_dwordx4 v36, %0, s54 offen offset:2048 sc1 lds\n\t" \
"v_mfma_f32_32x32x16_bf16 a[80:95], a[248:251], v[220:223], a[80:95]\n\t" \
"v_exp_f32 v72, v72\n\t" \
"v_add_f32 v31, v31, v87\n\t" \
"ds_read_b128 a[220:223], v16 offset:7168\n\t" \
"v_mfma_f32_32x32x16_bf16 a[96:111], a[252:255], v[204:207], a[96:111]\n\t" \
"v_exp_f32 v73, v73\n\t" \
"v_add_f32 v28, v28, v72\n\t" \
"v_cvt_pk_bf16_f32 v211, v86, v87\n\t" \
"buffer_load_dwordx4 v36, %0, s54 offen offset:3072 sc1 lds\n\t" \
"v_mfma_f32_32x32x16_bf16 a[112:127], a[252:255], v[220:223], a[112:127]\n\t" \
"v_exp_f32 v74, v74\n\t" \
"v_add_f32 v29, v29, v73\n\t" \
"s_xor_b32 s48, s48, 0x8000\n\t" \
"s_add_u32 s44, s44, 1\n\t" \
"s_add_u32 s42, s42, 0x8000\n\t" \
"s_cmp_eq_u32 s44, s45\n\t" \
"s_cselect_b32 s44, 0, s44\n\t" \
"s_cselect_b32 s42, %1, s42\n\t" \
"s_cselect_b32 s45, %6, s45\n\t" \
"s_cmp_lg_u32 s50, 0\n\t" \
"s_cbranch_scc1 RESC_l0_%=\n\t" \
"RESCB_l0_%=:\n\t" \
"s_cmp_eq_u32 s41, s40\n\t" \
"s_cbranch_scc1 DRAIN0_%=\n\t" \
"s_waitcnt lgkmcnt(7)\n\t" \
"v_mfma_f32_32x32x16_bf16 v[128:143], a[192:195], a[128:131], v[224:239]\n\t" \
"s_mov_b32 m0, s49\n\t" \
"v_exp_f32 v75, v75\n\t" \
"v_add_f32 v28, v28, v74\n\t" \
"v_cvt_pk_bf16_f32 v196, v72, v73\n\t" \
"v_mfma_f32_32x32x16_bf16 v[144:159], a[192:195], a[160:163], v[240:255]\n\t" \
"ds_read_b128 a[192:195], v16 offset:8192\n\t" \
"v_exp_f32 v76, v76\n\t" \
"v_add_f32 v29, v29, v75\n\t" \
"s_waitcnt lgkmcnt(7)\n\t" \
"v_mfma_f32_32x32x16_bf16 v[128:143], a[196:199], a[132:135], v[128:143]\n\t" \
"buffer_load_dwordx4 v36, %0, s43 offen sc1 lds\n\t" \
"v_exp_f32 v77, v77\n\t" \
"v_add_f32 v28, v28, v76\n\t" \
"v_cvt_pk_bf16_f32 v197, v74, v75\n\t" \
"v_mfma_f32_32x32x16_bf16 v[144:159], a[196:199], a[164:167], v[144:159]\n\t" \
"ds_read_b128 a[196:199], v16 offset:9216\n\t" \
"v_exp_f32 v78, v78\n\t" \
"v_add_f32 v29, v29, v77\n\t" \
"s_waitcnt lgkmcnt(7)\n\t" \
"v_mfma_f32_32x32x16_bf16 v[128:143], a[200:203], a[136:139], v[128:143]\n\t" \
"buffer_load_dwordx4 v36, %0, s43 offen offset:1024 sc1 lds\n\t" \
"v_exp_f32 v79, v79\n\t" \
"v_add_f32 v28, v28, v78\n\t" \
"v_cvt_pk_bf16_f32 v198, v76, v77\n\t" \
"v_mfma_f32_32x32x16_bf16 v[144:159], a[200:203], a[168:171], v[144:159]\n\t" \
"ds_read_b128 a[200:203], v16 offset:10240\n\t" \
"v_exp_f32 v88, v88\n\t" \
"v_add_f32 v29, v29, v79\n\t" \
"s_waitcnt lgkmcnt(7)\n\t" \
"v_mfma_f32_32x32x16_bf16 v[128:143], a[204:207], a[140:143], v[128:143]\n\t" \
"buffer_load_dwordx4 v36, %0, s43 offen offset:2048 sc1 lds\n\t" \
"v_exp_f32 v89, v89\n\t" \
"v_add_f32 v30, v30, v88\n\t" \
"v_cvt_pk_bf16_f32 v199, v78, v79\n\t" \
"v_mfma_f32_32x32x16_bf16 v[144:159], a[204:207], a[172:175], v[144:159]\n\t" \
"ds_read_b128 a[204:207], v16 offset:11264\n\t" \
"v_exp_f32 v90, v90\n\t" \
"v_add_f32 v31, v31, v89\n\t" \
"s_waitcnt lgkmcnt(7)\n\t" \
"v_mfma_f32_32x32x16_bf16 v[128:143], a[208:211], a[144:147], v[128:143]\n\t" \
"buffer_load_dwordx4 v36, %0, s43 offen offset:3072 sc1 lds\n\t" \
"v_exp_f32 v91, v91\n\t" \
"v_add_f32 v30, v30, v90\n\t" \
"v_cvt_pk_bf16_f32 v212, v88, v89\n\t" \
"v_mfma_f32_32x32x16_bf16 v[144:159], a[208:211], a[176:179], v[144:159]\n\t" \
"ds_read_b128 a[208:211], v16 offset:12288\n\t" \
"s_add_u32 m0, s49, 4096\n\t" \
"s_add_u32 s55, s43, 4096\n\t" \
"v_exp_f32 v92, v92\n\t" \
"v_add_f32 v31, v31, v91\n\t" \
"s_waitcnt lgkmcnt(7)\n\t" \
"v_mfma_f32_32x32x16_bf16 v[128:143], a[212:215], a[148:151], v[128:143]\n\t" \
"buffer_load_dwordx4 v36, %0, s55 offen sc1 lds\n\t" \
"v_exp_f32 v93, v93\n\t" \
"v_add_f32 v30, v30, v92\n\t" \
"v_cvt_pk_bf16_f32 v213, v90, v91\n\t" \
"v_mfma_f32_32x32x16_bf16 v[144:159], a[212:215], a[180:183], v[144:159]\n\t" \
"ds_read_b128 a[212:215], v16 offset:13312\n\t" \
"v_exp_f32 v94, v94\n\t" \
"v_add_f32 v31, v31, v93\n\t" \
"s_waitcnt lgkmcnt(7)\n\t" \
"v_mfma_f32_32x32x16_bf16 v[128:143], a[216:219], a[152:155], v[128:143]\n\t" \
"buffer_load_dwordx4 v36, %0, s55 offen offset:1024 sc1 lds\n\t" \
"v_exp_f32 v95, v95\n\t" \
"v_add_f32 v30, v30, v94\n\t" \
"v_cvt_pk_bf16_f32 v214, v92, v93\n\t" \
"v_mfma_f32_32x32x16_bf16 v[144:159], a[216:219], a[184:187], v[144:159]\n\t" \
"ds_read_b128 a[216:219], v16 offset:14336\n\t" \
"v_exp_f32 v96, v96\n\t" \
"v_add_f32 v31, v31, v95\n\t" \
"s_waitcnt lgkmcnt(7)\n\t" \
"v_mfma_f32_32x32x16_bf16 v[128:143], a[220:223], a[156:159], v[128:143]\n\t" \
"buffer_load_dwordx4 v36, %0, s55 offen offset:2048 sc1 lds\n\t" \
"v_exp_f32 v97, v97\n\t" \
"v_add_f32 v28, v28, v96\n\t" \
"v_cvt_pk_bf16_f32 v215, v94, v95\n\t" \
"v_mfma_f32_32x32x16_bf16 v[144:159], a[220:223], a[188:191], v[144:159]\n\t" \
"ds_read_b128 a[220:223], v16 offset:15360\n\t" \
"v_exp_f32 v98, v98\n\t" \
"v_add_f32 v29, v29, v97\n\t" \
"s_waitcnt lgkmcnt(7)\n\t" \
"v_mfma_f32_32x32x16_bf16 v[160:175], a[192:195], a[128:131], v[224:239]\n\t" \
"buffer_load_dwordx4 v36, %0, s55 offen offset:3072 sc1 lds\n\t" \
"v_exp_f32 v99, v99\n\t" \
"v_add_f32 v28, v28, v98\n\t" \
"v_cvt_pk_bf16_f32 v200, v96, v97\n\t" \
"v_mfma_f32_32x32x16_bf16 v[176:191], a[192:195], a[160:163], v[240:255]\n\t" \
"ds_read_b128 a[224:227], v17 offset:0\n\t" \
"v_exp_f32 v100, v100\n\t" \
"v_add_f32 v29, v29, v99\n\t" \
"s_waitcnt lgkmcnt(7)\n\t" \
"v_mfma_f32_32x32x16_bf16 v[160:175], a[196:199], a[132:135], v[160:175]\n\t" \
"s_xor_b32 s49, s49, 0x8000\n\t" \
"v_exp_f32 v101, v101\n\t" \
"v_add_f32 v28, v28, v100\n\t" \
"v_cvt_pk_bf16_f32 v201, v98, v99\n\t" \
"v_mfma_f32_32x32x16_bf16 v[176:191], a[196:199], a[164:167], v[176:191]\n\t" \
"ds_read_b128 a[228:231], v17 offset:1024\n\t" \
"v_exp_f32 v102, v102\n\t" \
"v_add_f32 v29, v29, v101\n\t" \
"s_waitcnt lgkmcnt(7)\n\t" \
"v_mfma_f32_32x32x16_bf16 v[160:175], a[200:203], a[136:139], v[160:175]\n\t" \
"s_add_u32 s46, s46, 1\n\t" \
"s_add_u32 s43, s43, 0x8000\n\t" \
"v_exp_f32 v103, v103\n\t" \
"v_add_f32 v28, v28, v102\n\t" \
"v_cvt_pk_bf16_f32 v202, v100, v101\n\t" \
"v_max3_f32 v18, v128, v129, v130\n\t" \
"v_max3_f32 v19, v144, v145, v146\n\t" \
"v_mfma_f32_32x32x16_bf16 v[176:191], a[200:203], a[168:171], v[176:191]\n\t" \
"ds_read_b128 a[232:235], v17 offset:2048\n\t" \
"v_exp_f32 v112, v112\n\t" \
"v_add_f32 v29, v29, v103\n\t" \
"v_max3_f32 v18, v18, v131, v132\n\t" \
"s_waitcnt lgkmcnt(7)\n\t" \
"v_mfma_f32_32x32x16_bf16 v[160:175], a[204:207], a[140:143], v[160:175]\n\t" \
"s_cmp_eq_u32 s46, s47\n\t" \
"s_cselect_b32 s46, 0, s46\n\t" \
"s_cselect_b32 s43, %2, s43\n\t" \
"s_cselect_b32 s47, %6, s47\n\t" \
"v_exp_f32 v113, v113\n\t" \
"v_add_f32 v30, v30, v112\n\t" \
"v_cvt_pk_bf16_f32 v203, v102, v103\n\t" \
"v_max3_f32 v19, v19, v147, v148\n\t" \
"v_mfma_f32_32x32x16_bf16 v[176:191], a[204:207], a[172:175], v[176:191]\n\t" \
"ds_read_b128 a[236:239], v17 offset:3072\n\t" \
"v_exp_f32 v114, v114\n\t" \
"v_add_f32 v31, v31, v113\n\t" \
"v_max3_f32 v18, v18, v133, v134\n\t" \
"v_max3_f32 v19, v19, v149, v150\n\t" \
"s_waitcnt lgkmcnt(7)\n\t" \
"v_mfma_f32_32x32x16_bf16 v[160:175], a[208:211], a[144:147], v[160:175]\n\t" \
"v_exp_f32 v115, v115\n\t" \
"v_add_f32 v30, v30, v114\n\t" \
"v_cvt_pk_bf16_f32 v216, v112, v113\n\t" \
"v_max3_f32 v18, v18, v135, v136\n\t" \
"v_mfma_f32_32x32x16_bf16 v[176:191], a[208:211], a[176:179], v[176:191]\n\t" \
"ds_read_b128 a[240:243], v17 offset:4096\n\t" \
"v_exp_f32 v116, v116\n\t" \
"v_add_f32 v31, v31, v115\n\t" \
"v_max3_f32 v19, v19, v151, v152\n\t" \
"s_waitcnt lgkmcnt(7)\n\t" \
"v_mfma_f32_32x32x16_bf16 v[160:175], a[212:215], a[148:151], v[160:175]\n\t" \
"v_exp_f32 v117, v117\n\t" \
"v_add_f32 v30, v30, v116\n\t" \
"v_cvt_pk_bf16_f32 v217, v114, v115\n\t" \
"v_max3_f32 v18, v18, v137, v138\n\t" \
"v_max3_f32 v19, v19, v153, v154\n\t" \
"v_mfma_f32_32x32x16_bf16 v[176:191], a[212:215], a[180:183], v[176:191]\n\t" \
"ds_read_b128 a[244:247], v17 offset:5120\n\t" \
"v_exp_f32 v118, v118\n\t" \
"v_add_f32 v31, v31, v117\n\t" \
"v_max3_f32 v18, v18, v139, v140\n\t" \
"s_waitcnt lgkmcnt(7)\n\t" \
"v_mfma_f32_32x32x16_bf16 v[160:175], a[216:219], a[152:155], v[160:175]\n\t" \
"v_exp_f32 v119, v119\n\t" \
"v_add_f32 v30, v30, v118\n\t" \
"v_cvt_pk_bf16_f32 v218, v116, v117\n\t" \
"v_max3_f32 v19, v19, v155, v156\n\t" \
"v_mfma_f32_32x32x16_bf16 v[176:191], a[216:219], a[184:187], v[176:191]\n\t" \
"ds_read_b128 a[248:251], v17 offset:6144\n\t" \
"v_exp_f32 v104, v104\n\t" \
"v_add_f32 v31, v31, v119\n\t" \
"v_max3_f32 v18, v18, v141, v142\n\t" \
"v_max3_f32 v19, v19, v157, v158\n\t" \
"s_waitcnt lgkmcnt(7)\n\t" \
"v_mfma_f32_32x32x16_bf16 v[160:175], a[220:223], a[156:159], v[160:175]\n\t" \
"v_exp_f32 v105, v105\n\t" \
"v_add_f32 v28, v28, v104\n\t" \
"v_cvt_pk_bf16_f32 v219, v118, v119\n\t" \
"v_max_f32 v18, v18, v143\n\t" \
"v_mfma_f32_32x32x16_bf16 v[176:191], a[220:223], a[188:191], v[176:191]\n\t" \
"ds_read_b128 a[252:255], v17 offset:7168\n\t" \
"v_exp_f32 v106, v106\n\t" \
"v_add_f32 v29, v29, v105\n\t" \
"v_max_f32 v19, v19, v159\n\t" \
"v_xor_b32 v16, 0x8000, v16\n\t" \
"s_add_u32 s41, s41, 1\n\t" \
"s_cmp_lg_u32 s41, s40\n\t" \
"s_cbranch_scc1 NOMASK_l1_%=\n\t" \
"v_add_f32 v40, 0x00000000, v37\n\t" \
"v_min_f32 v128, v128, v40\n\t" \
"s_nop 1\n\t" \
"v_min_f32 v176, v176, v40\n\t" \
"v_add_f32 v41, 0xf1800000, v37\n\t" \
"v_min_f32 v129, v129, v41\n\t" \
"v_min_f32 v177, v177, v41\n\t" \
"v_add_f32 v42, 0xf2000000, v37\n\t" \
"v_min_f32 v130, v130, v42\n\t" \
"v_min_f32 v178, v178, v42\n\t" \
"v_add_f32 v43, 0xf2400000, v37\n\t" \
"v_min_f32 v131, v131, v43\n\t" \
"v_min_f32 v179, v179, v43\n\t" \
"v_add_f32 v44, 0xf3000000, v37\n\t" \
"v_min_f32 v132, v132, v44\n\t" \
"v_min_f32 v180, v180, v44\n\t" \
"v_add_f32 v45, 0xf3100000, v37\n\t" \
"v_min_f32 v133, v133, v45\n\t" \
"v_min_f32 v181, v181, v45\n\t" \
"v_add_f32 v46, 0xf3200000, v37\n\t" \
"v_min_f32 v134, v134, v46\n\t" \
"v_min_f32 v182, v182, v46\n\t" \
"v_add_f32 v47, 0xf3300000, v37\n\t" \
"v_min_f32 v135, v135, v47\n\t" \
"v_min_f32 v183, v183, v47\n\t" \
"v_add_f32 v40, 0xf3800000, v37\n\t" \
"v_min_f32 v136, v136, v40\n\t" \
"v_min_f32 v184, v184, v40\n\t" \
"v_add_f32 v41, 0xf3880000, v37\n\t" \
"v_min_f32 v137, v137, v41\n\t" \
"v_min_f32 v185, v185, v41\n\t" \
"v_add_f32 v42, 0xf3900000, v37\n\t" \
"v_min_f32 v138, v138, v42\n\t" \
"v_min_f32 v186, v186, v42\n\t" \
"v_add_f32 v43, 0xf3980000, v37\n\t" \
"v_min_f32 v139, v139, v43\n\t" \
"v_min_f32 v187, v187, v43\n\t" \
"v_add_f32 v44, 0xf3c00000, v37\n\t" \
"v_min_f32 v140, v140, v44\n\t" \
"v_min_f32 v188, v188, v44\n\t" \
"v_add_f32 v45, 0xf3c80000, v37\n\t" \
"v_min_f32 v141, v141, v45\n\t" \
"v_min_f32 v189, v189, v45\n\t" \
"v_add_f32 v46, 0xf3d00000, v37\n\t" \
"v_min_f32 v142, v142, v46\n\t" \
"v_min_f32 v190, v190, v46\n\t" \
"v_add_f32 v47, 0xf3d80000, v37\n\t" \
"v_min_f32 v143, v143, v47\n\t" \
"v_min_f32 v191, v191, v47\n\t" \
"v_add_f32 v40, 0xf4000000, v37\n\t" \
"v_min_f32 v160, v160, v40\n\t" \
"v_add_f32 v41, 0xf4040000, v37\n\t" \
"v_min_f32 v161, v161, v41\n\t" \
"v_add_f32 v42, 0xf4080000, v37\n\t" \
"v_min_f32 v162, v162, v42\n\t" \
"v_add_f32 v43, 0xf40c0000, v37\n\t" \
"v_min_f32 v163, v163, v43\n\t" \
"v_add_f32 v44, 0xf4200000, v37\n\t" \
"v_min_f32 v164, v164, v44\n\t" \
"v_add_f32 v45, 0xf4240000, v37\n\t" \
"v_min_f32 v165, v165, v45\n\t" \
"v_add_f32 v46, 0xf4280000, v37\n\t" \
"v_min_f32 v166, v166, v46\n\t" \
"v_add_f32 v47, 0xf42c0000, v37\n\t" \
"v_min_f32 v167, v167, v47\n\t" \
"v_add_f32 v40, 0xf4400000, v37\n\t" \
"v_min_f32 v168, v168, v40\n\t" \
"v_add_f32 v41, 0xf4440000, v37\n\t" \
"v_min_f32 v169, v169, v41\n\t" \
"v_add_f32 v42, 0xf4480000, v37\n\t" \
"v_min_f32 v170, v170, v42\n\t" \
"v_add_f32 v43, 0xf44c0000, v37\n\t" \
"v_min_f32 v171, v171, v43\n\t" \
"v_add_f32 v44, 0xf4600000, v37\n\t" \
"v_min_f32 v172, v172, v44\n\t" \
"v_add_f32 v45, 0xf4640000, v37\n\t" \
"v_min_f32 v173, v173, v45\n\t" \
"v_add_f32 v46, 0xf4680000, v37\n\t" \
"v_min_f32 v174, v174, v46\n\t" \
"v_add_f32 v47, 0xf46c0000, v37\n\t" \
"v_min_f32 v175, v175, v47\n\t" \
"v_add_f32 v40, 0x74000000, v37\n\t" \
"v_min_f32 v144, v144, v40\n\t" \
"v_add_f32 v41, 0x73f80000, v37\n\t" \
"v_min_f32 v145, v145, v41\n\t" \
"v_add_f32 v42, 0x73f00000, v37\n\t" \
"v_min_f32 v146, v146, v42\n\t" \
"v_add_f32 v43, 0x73e80000, v37\n\t" \
"v_min_f32 v147, v147, v43\n\t" \
"v_add_f32 v44, 0x73c00000, v37\n\t" \
"v_min_f32 v148, v148, v44\n\t" \
"v_add_f32 v45, 0x73b80000, v37\n\t" \
"v_min_f32 v149, v149, v45\n\t" \
"v_add_f32 v46, 0x73b00000, v37\n\t" \
"v_min_f32 v150, v150, v46\n\t" \
"v_add_f32 v47, 0x73a80000, v37\n\t" \
"v_min_f32 v151, v151, v47\n\t" \
"v_add_f32 v40, 0x73800000, v37\n\t" \
"v_min_f32 v152, v152, v40\n\t" \
"v_add_f32 v41, 0x73700000, v37\n\t" \
"v_min_f32 v153, v153, v41\n\t" \
"v_add_f32 v42, 0x73600000, v37\n\t" \
"v_min_f32 v154, v154, v42\n\t" \
"v_add_f32 v43, 0x73500000, v37\n\t" \
"v_min_f32 v155, v155, v43\n\t" \
"v_add_f32 v44, 0x73000000, v37\n\t" \
"v_min_f32 v156, v156, v44\n\t" \
"v_add_f32 v45, 0x72e00000, v37\n\t" \
"v_min_f32 v157, v157, v45\n\t" \
"v_add_f32 v46, 0x72c00000, v37\n\t" \
"v_min_f32 v158, v158, v46\n\t" \
"v_add_f32 v47, 0x72a00000, v37\n\t" \
"v_min_f32 v159, v159, v47\n\t" \
"v_max3_f32 v18, v128, v129, v130\n\t" \
"v_max3_f32 v19, v144, v145, v146\n\t" \
"v_max3_f32 v18, v18, v131, v132\n\t" \
"v_max3_f32 v19, v19, v147, v148\n\t" \
"v_max3_f32 v18, v18, v133, v134\n\t" \
"v_max3_f32 v19, v19, v149, v150\n\t" \
"v_max3_f32 v18, v18, v135, v136\n\t" \
"v_max3_f32 v19, v19, v151, v152\n\t" \
"v_max3_f32 v18, v18, v137, v138\n\t" \
"v_max3_f32 v19, v19, v153, v154\n\t" \
"v_max3_f32 v18, v18, v139, v140\n\t" \
"v_max3_f32 v19, v19, v155, v156\n\t" \
"v_max3_f32 v18, v18, v141, v142\n\t" \
"v_max3_f32 v19, v19, v157, v158\n\t" \
"v_max_f32 v18, v18, v143\n\t" \
"v_max_f32 v19, v19, v159\n\t" \
"NOMASK_l1_%=:\n\t" \
"s_waitcnt lgkmcnt(7)\n\t" \
"v_mfma_f32_32x32x16_bf16 a[0:15], a[224:227], v[192:195], a[0:15]\n\t" \
"v_exp_f32 v107, v107\n\t" \
"v_add_f32 v28, v28, v106\n\t" \
"v_cvt_pk_bf16_f32 v204, v104, v105\n\t" \
"v_mfma_f32_32x32x16_bf16 a[16:31], a[224:227], v[208:211], a[16:31]\n\t" \
"ds_read_b128 a[224:227], v17 offset:8192\n\t" \
"v_exp_f32 v108, v108\n\t" \
"v_add_f32 v29, v29, v107\n\t" \
"s_waitcnt lgkmcnt(7)\n\t" \
"v_mfma_f32_32x32x16_bf16 a[32:47], a[228:231], v[192:195], a[32:47]\n\t" \
"v_exp_f32 v109, v109\n\t" \
"v_add_f32 v28, v28, v108\n\t" \
"v_cvt_pk_bf16_f32 v205, v106, v107\n\t" \
"v_max3_f32 v18, v18, v160, v161\n\t" \
"v_max3_f32 v19, v19, v176, v177\n\t" \
"v_mfma_f32_32x32x16_bf16 a[48:63], a[228:231], v[208:211], a[48:63]\n\t" \
"ds_read_b128 a[228:231], v17 offset:9216\n\t" \
"v_exp_f32 v110, v110\n\t" \
"v_add_f32 v29, v29, v109\n\t" \
"v_max3_f32 v18, v18, v162, v163\n\t" \
"s_waitcnt lgkmcnt(7)\n\t" \
"v_mfma_f32_32x32x16_bf16 a[64:79], a[232:235], v[192:195], a[64:79]\n\t" \
"v_exp_f32 v111, v111\n\t" \
"v_add_f32 v28, v28, v110\n\t" \
"v_cvt_pk_bf16_f32 v206, v108, v109\n\t" \
"v_max3_f32 v19, v19, v178, v179\n\t" \
"v_max3_f32 v18, v18, v164, v165\n\t" \
"v_mfma_f32_32x32x16_bf16 a[80:95], a[232:235], v[208:211], a[80:95]\n\t" \
"ds_read_b128 a[232:235], v17 offset:10240\n\t" \
"v_exp_f32 v120, v120\n\t" \
"v_add_f32 v29, v29, v111\n\t" \
"v_max3_f32 v19, v19, v180, v181\n\t" \
"s_waitcnt lgkmcnt(7)\n\t" \
"v_mfma_f32_32x32x16_bf16 a[96:111], a[236:239], v[192:195], a[96:111]\n\t" \
"v_exp_f32 v121, v121\n\t" \
"v_add_f32 v30, v30, v120\n\t" \
"v_cvt_pk_bf16_f32 v207, v110, v111\n\t" \
"v_max3_f32 v18, v18, v166, v167\n\t" \
"v_max3_f32 v19, v19, v182, v183\n\t" \
"v_mfma_f32_32x32x16_bf16 a[112:127], a[236:239], v[208:211], a[112:127]\n\t" \
"ds_read_b128 a[236:239], v17 offset:11264\n\t" \
"v_exp_f32 v122, v122\n\t" \
"v_add_f32 v31, v31, v121\n\t" \
"v_max3_f32 v18, v18, v168, v169\n\t" \
"s_waitcnt lgkmcnt(7)\n\t" \
"v_mfma_f32_32x32x16_bf16 a[0:15], a[240:243], v[196:199], a[0:15]\n\t" \
"v_exp_f32 v123, v123\n\t" \
"v_add_f32 v30, v30, v122\n\t" \
"v_cvt_pk_bf16_f32 v220, v120, v121\n\t" \
"v_max3_f32 v19, v19, v184, v185\n\t" \
"v_max3_f32 v18, v18, v170, v171\n\t" \
"v_mfma_f32_32x32x16_bf16 a[16:31], a[240:243], v[212:215], a[16:31]\n\t" \
"ds_read_b128 a[240:243], v17 offset:12288\n\t" \
"v_exp_f32 v124, v124\n\t" \
"v_add_f32 v31, v31, v123\n\t" \
"v_max3_f32 v19, v19, v186, v187\n\t" \
"v_max3_f32 v18, v18, v172, v173\n\t" \
"s_waitcnt lgkmcnt(7)\n\t" \
"v_mfma_f32_32x32x16_bf16 a[32:47], a[244:247], v[196:199], a[32:47]\n\t" \
"v_exp_f32 v125, v125\n\t" \
"v_add_f32 v30, v30, v124\n\t" \
"v_cvt_pk_bf16_f32 v221, v122, v123\n\t" \
"v_max3_f32 v19, v19, v188, v189\n\t" \
"v_max3_f32 v18, v18, v174, v175\n\t" \
"v_max3_f32 v19, v19, v190, v191\n\t" \
"v_mfma_f32_32x32x16_bf16 a[48:63], a[244:247], v[212:215], a[48:63]\n\t" \
"ds_read_b128 a[244:247], v17 offset:13312\n\t" \
"v_exp_f32 v126, v126\n\t" \
"v_add_f32 v31, v31, v125\n\t" \
"v_mov_b32 v20, v18\n\t" \
"v_mov_b32 v21, v19\n\t" \
"s_waitcnt lgkmcnt(7)\n\t" \
"v_mfma_f32_32x32x16_bf16 a[64:79], a[248:251], v[196:199], a[64:79]\n\t" \
"v_exp_f32 v127, v127\n\t" \
"v_add_f32 v30, v30, v126\n\t" \
"v_cvt_pk_bf16_f32 v222, v124, v125\n\t" \
"v_permlane32_swap_b32 v18, v20\n\t" \
"v_permlane32_swap_b32 v19, v21\n\t" \
"v_max_f32 v20, v20, v18\n\t" \
"v_max_f32 v21, v21, v19\n\t" \
"v_add_f32 v31, v31, v127\n\t" \
"v_max_f32 v22, v20, v21\n\t" \
"v_cmp_lt_f32 vcc, 0x41000000, v22\n\t" \
"s_cbranch_vccnz RARE1_l1_%=\n\t" \
"BACK1_l1_%=:\n\t" \
"v_mfma_f32_32x32x16_bf16 a[80:95], a[248:251], v[212:215], a[80:95]\n\t" \
"ds_read_b128 a[248:251], v17 offset:14336\n\t" \
"v_cvt_pk_bf16_f32 v223, v126, v127\n\t" \
"v_exp_f32 v128, v128\n\t" \
"s_waitcnt lgkmcnt(7)\n\t" \
"v_mfma_f32_32x32x16_bf16 a[96:111], a[252:255], v[196:199], a[96:111]\n\t" \
"v_exp_f32 v129, v129\n\t" \
"v_add_f32 v28, v28, v128\n\t" \
"v_mfma_f32_32x32x16_bf16 a[112:127], a[252:255], v[212:215], a[112:127]\n\t" \
"ds_read_b128 a[252:255], v17 offset:15360\n\t" \
"v_exp_f32 v130, v130\n\t" \
"v_add_f32 v29, v29, v129\n\t" \
"s_waitcnt lgkmcnt(7)\n\t" \
"v_mfma_f32_32x32x16_bf16 a[0:15], a[224:227], v[200:203], a[0:15]\n\t" \
"v_exp_f32 v131, v131\n\t" \
"v_add_f32 v28, v28, v130\n\t" \
"v_cvt_pk_bf16_f32 v192, v128, v129\n\t" \
"v_mfma_f32_32x32x16_bf16 a[16:31], a[224:227], v[216:219], a[16:31]\n\t" \
"v_exp_f32 v132, v132\n\t" \
"v_add_f32 v29, v29, v131\n\t" \
"s_waitcnt lgkmcnt(6)\n\t" \
"v_mfma_f32_32x32x16_bf16 a[32:47], a[228:231], v[200:203], a[32:47]\n\t" \
"v_exp_f32 v133, v133\n\t" \
"v_add_f32 v28, v28, v132\n\t" \
"v_cvt_pk_bf16_f32 v193, v130, v131\n\t" \
"s_waitcnt lgkmcnt(0)\n\t" \
"s_waitcnt vmcnt(0)\n\t" \
"s_barrier\n\t" \
"v_xor_b32 v17, 0x8000, v17\n\t" \
"v_mfma_f32_32x32x16_bf16 a[48:63], a[228:231], v[216:219], a[48:63]\n\t" \
"v_exp_f32 v134, v134\n\t" \
"v_add_f32 v29, v29, v133\n\t" \
"ds_read_b128 a[192:195], v16 offset:0\n\t" \
"s_mov_b32 m0, s48\n\t" \
"v_mfma_f32_32x32x16_bf16 a[64:79], a[232:235], v[200:203], a[64:79]\n\t" \
"v_exp_f32 v135, v135\n\t" \
"v_add_f32 v28, v28, v134\n\t" \
"v_cvt_pk_bf16_f32 v194, v132, v133\n\t" \
"ds_read_b128 a[196:199], v16 offset:1024\n\t" \
"buffer_load_dwordx4 v36, %0, s42 offen sc1 lds\n\t" \
"v_mfma_f32_32x32x16_bf16 a[80:95], a[232:235], v[216:219], a[80:95]\n\t" \
"v_exp_f32 v144, v144\n\t" \
"v_add_f32 v29, v29, v135\n\t" \
"buffer_load_dwordx4 v36, %0, s42 offen offset:1024 sc1 lds\n\t" \
"v_mfma_f32_32x32x16_bf16 a[96:111], a[236:239], v[200:203], a[96:111]\n\t" \
"v_exp_f32 v145, v145\n\t" \
"v_add_f32 v30, v30, v144\n\t" \
"v_cvt_pk_bf16_f32 v195, v134, v135\n\t" \
"ds_read_b128 a[200:203], v16 offset:2048\n\t" \
"buffer_load_dwordx4 v36, %0, s42 offen offset:2048 sc1 lds\n\t" \
"v_mfma_f32_32x32x16_bf16 a[112:127], a[236:239], v[216:219], a[112:127]\n\t" \
"v_exp_f32 v146, v146\n\t" \
"v_add_f32 v31, v31, v145\n\t" \
"ds_read_b128 a[204:207], v16 offset:3072\n\t" \
"v_mfma_f32_32x32x16_bf16 a[0:15], a[240:243], v[204:207], a[0:15]\n\t" \
"v_exp_f32 v147, v147\n\t" \
"v_add_f32 v30, v30, v146\n\t" \
"v_cvt_pk_bf16_f32 v208, v144, v145\n\t" \
"buffer_load_dwordx4 v36, %0, s42 offen offset:3072 sc1 lds\n\t" \
"s_add_u32 m0, s48, 4096\n\t" \
"s_add_u32 s54, s42, 4096\n\t" \
"v_mfma_f32_32x32x16_bf16 a[16:31], a[240:243], v[220:223], a[16:31]\n\t" \
"v_exp_f32 v148, v148\n\t" \
"v_add_f32 v31, v31, v147\n\t" \
"ds_read_b128 a[208:211], v16 offset:4096\n\t" \
"buffer_load_dwordx4 v36, %0, s54 offen sc1 lds\n\t" \
"v_mfma_f32_32x32x16_bf16 a[32:47], a[244:247], v[204:207], a[32:47]\n\t" \
"v_exp_f32 v149, v149\n\t" \
"v_add_f32 v30, v30, v148\n\t" \
"v_cvt_pk_bf16_f32 v209, v146, v147\n\t" \
"ds_read_b128 a[212:215], v16 offset:5120\n\t" \
"v_mfma_f32_32x32x16_bf16 a[48:63], a[244:247], v[220:223], a[48:63]\n\t" \
"v_exp_f32 v150, v150\n\t" \
"v_add_f32 v31, v31, v149\n\t" \
"buffer_load_dwordx4 v36, %0, s54 offen offset:1024 sc1 lds\n\t" \
"v_mfma_f32_32x32x16_bf16 a[64:79], a[248:251], v[204:207], a[64:79]\n\t" \
"v_exp_f32 v151, v151\n\t" \
"v_add_f32 v30, v30, v150\n\t" \
"v_cvt_pk_bf16_f32 v210, v148, v149\n\t" \
"ds_read_b128 a[216:219], v16 offset:6144\n\t" \
"buffer_load_dwordx4 v36, %0, s54 offen offset:2048 sc1 lds\n\t" \
"v_mfma_f32_32x32x16_bf16 a[80:95], a[248:251], v[220:223], a[80:95]\n\t" \
"v_exp_f32 v136, v136\n\t" \
"v_add_f32 v31, v31, v151\n\t" \
"ds_read_b128 a[220:223], v16 offset:7168\n\t" \
"v_mfma_f32_32x32x16_bf16 a[96:111], a[252:255], v[204:207], a[96:111]\n\t" \
"v_exp_f32 v137, v137\n\t" \
"v_add_f32 v28, v28, v136\n\t" \
"v_cvt_pk_bf16_f32 v211, v150, v151\n\t" \
"buffer_load_dwordx4 v36, %0, s54 offen offset:3072 sc1 lds\n\t" \
"v_mfma_f32_32x32x16_bf16 a[112:127], a[252:255], v[220:223], a[112:127]\n\t" \
"v_exp_f32 v138, v138\n\t" \
"v_add_f32 v29, v29, v137\n\t" \
"s_xor_b32 s48, s48, 0x8000\n\t" \
"s_add_u32 s44, s44, 1\n\t" \
"s_add_u32 s42, s42, 0x8000\n\t" \
"s_cmp_eq_u32 s44, s45\n\t" \
"s_cselect_b32 s44, 0, s44\n\t" \
"s_cselect_b32 s42, %1, s42\n\t" \
"s_cselect_b32 s45, %6, s45\n\t" \
"s_cmp_lg_u32 s50, 0\n\t" \
"s_cbranch_scc1 RESC_l1_%=\n\t" \
"RESCB_l1_%=:\n\t" \
"s_cmp_lg_u32 s41, s40\n\t" \
"s_cbranch_scc1 LOOP_%=\n\t" \
"DRAIN1_%=:\n\t" \
"s_nop 1\n\t" \
"ds_read_b128 a[224:227], v17 offset:0\n\t" \
"ds_read_b128 a[228:231], v17 offset:1024\n\t" \
"ds_read_b128 a[232:235], v17 offset:2048\n\t" \
"ds_read_b128 a[236:239], v17 offset:3072\n\t" \
"ds_read_b128 a[240:243], v17 offset:4096\n\t" \
"ds_read_b128 a[244:247], v17 offset:5120\n\t" \
"ds_read_b128 a[248:251], v17 offset:6144\n\t" \
"s_waitcnt lgkmcnt(10)\n\t" \
"ds_read_b128 a[252:255], v17 offset:7168\n\t" \
"s_waitcnt lgkmcnt(7)\n\t" \
"v_mfma_f32_32x32x16_bf16 a[0:15], a[224:227], v[192:195], a[0:15]\n\t" \
"v_exp_f32 v139, v139\n\t" \
"v_add_f32 v28, v28, v138\n\t" \
"v_cvt_pk_bf16_f32 v196, v136, v137\n\t" \
"v_exp_f32 v140, v140\n\t" \
"v_add_f32 v29, v29, v139\n\t" \
"v_mfma_f32_32x32x16_bf16 a[16:31], a[224:227], v[208:211], a[16:31]\n\t" \
"ds_read_b128 a[224:227], v17 offset:8192\n\t" \
"v_exp_f32 v141, v141\n\t" \
"v_add_f32 v28, v28, v140\n\t" \
"v_cvt_pk_bf16_f32 v197, v138, v139\n\t" \
"v_exp_f32 v142, v142\n\t" \
"v_add_f32 v29, v29, v141\n\t" \
"s_waitcnt lgkmcnt(7)\n\t" \
"v_mfma_f32_32x32x16_bf16 a[32:47], a[228:231], v[192:195], a[32:47]\n\t" \
"v_exp_f32 v143, v143\n\t" \
"v_add_f32 v28, v28, v142\n\t" \
"v_cvt_pk_bf16_f32 v198, v140, v141\n\t" \
"v_exp_f32 v152, v152\n\t" \
"v_add_f32 v29, v29, v143\n\t" \
"v_mfma_f32_32x32x16_bf16 a[48:63], a[228:231], v[208:211], a[48:63]\n\t" \
"ds_read_b128 a[228:231], v17 offset:9216\n\t" \
"v_exp_f32 v153, v153\n\t" \
"v_add_f32 v30, v30, v152\n\t" \
"v_cvt_pk_bf16_f32 v199, v142, v143\n\t" \
"v_exp_f32 v154, v154\n\t" \
"s_waitcnt lgkmcnt(7)\n\t" \
"v_mfma_f32_32x32x16_bf16 a[64:79], a[232:235], v[192:195], a[64:79]\n\t" \
"v_add_f32 v31, v31, v153\n\t" \
"v_exp_f32 v155, v155\n\t" \
"v_add_f32 v30, v30, v154\n\t" \
"v_cvt_pk_bf16_f32 v212, v152, v153\n\t" \
"v_exp_f32 v156, v156\n\t" \
"v_mfma_f32_32x32x16_bf16 a[80:95], a[232:235], v[208:211], a[80:95]\n\t" \
"ds_read_b128 a[232:235], v17 offset:10240\n\t" \
"v_add_f32 v31, v31, v155\n\t" \
"v_exp_f32 v157, v157\n\t" \
"v_add_f32 v30, v30, v156\n\t" \
"v_cvt_pk_bf16_f32 v213, v154, v155\n\t" \
"v_exp_f32 v158, v158\n\t" \
"s_waitcnt lgkmcnt(7)\n\t" \
"v_mfma_f32_32x32x16_bf16 a[96:111], a[236:239], v[192:195], a[96:111]\n\t" \
"v_add_f32 v31, v31, v157\n\t" \
"v_exp_f32 v159, v159\n\t" \
"v_add_f32 v30, v30, v158\n\t" \
"v_cvt_pk_bf16_f32 v214, v156, v157\n\t" \
"v_exp_f32 v160, v160\n\t" \
"v_mfma_f32_32x32x16_bf16 a[112:127], a[236:239], v[208:211], a[112:127]\n\t" \
"ds_read_b128 a[236:239], v17 offset:11264\n\t" \
"v_add_f32 v31, v31, v159\n\t" \
"v_exp_f32 v161, v161\n\t" \
"v_add_f32 v28, v28, v160\n\t" \
"v_cvt_pk_bf16_f32 v215, v158, v159\n\t" \
"s_waitcnt lgkmcnt(7)\n\t" \
"v_mfma_f32_32x32x16_bf16 a[0:15], a[240:243], v[196:199], a[0:15]\n\t" \
"v_exp_f32 v162, v162\n\t" \
"v_add_f32 v29, v29, v161\n\t" \
"v_exp_f32 v163, v163\n\t" \
"v_add_f32 v28, v28, v162\n\t" \
"v_cvt_pk_bf16_f32 v200, v160, v161\n\t" \
"v_mfma_f32_32x32x16_bf16 a[16:31], a[240:243], v[212:215], a[16:31]\n\t" \
"ds_read_b128 a[240:243], v17 offset:12288\n\t" \
"v_exp_f32 v164, v164\n\t" \
"v_add_f32 v29, v29, v163\n\t" \
"v_exp_f32 v165, v165\n\t" \
"v_add_f32 v28, v28, v164\n\t" \
"v_cvt_pk_bf16_f32 v201, v162, v163\n\t" \
"s_waitcnt lgkmcnt(7)\n\t" \
"v_mfma_f32_32x32x16_bf16 a[32:47], a[244:247], v[196:199], a[32:47]\n\t" \
"v_exp_f32 v166, v166\n\t" \
"v_add_f32 v29, v29, v165\n\t" \
"v_exp_f32 v167, v167\n\t" \
"v_add_f32 v28, v28, v166\n\t" \
"v_cvt_pk_bf16_f32 v202, v164, v165\n\t" \
"v_mfma_f32_32x32x16_bf16 a[48:63], a[244:247], v[212:215], a[48:63]\n\t" \
"ds_read_b128 a[244:247], v17 offset:13312\n\t" \
"v_exp_f32 v176, v176\n\t" \
"v_add_f32 v29, v29, v167\n\t" \
"v_exp_f32 v177, v177\n\t" \
"v_add_f32 v30, v30, v176\n\t" \
"v_cvt_pk_bf16_f32 v203, v166, v167\n\t" \
"s_waitcnt lgkmcnt(7)\n\t" \
"v_mfma_f32_32x32x16_bf16 a[64:79], a[248:251], v[196:199], a[64:79]\n\t" \
"v_exp_f32 v178, v178\n\t" \
"v_add_f32 v31, v31, v177\n\t" \
"v_exp_f32 v179, v179\n\t" \
"v_add_f32 v30, v30, v178\n\t" \
"v_cvt_pk_bf16_f32 v216, v176, v177\n\t" \
"v_mfma_f32_32x32x16_bf16 a[80:95], a[248:251], v[212:215], a[80:95]\n\t" \
"ds_read_b128 a[248:251], v17 offset:14336\n\t" \
"v_exp_f32 v180, v180\n\t" \
"v_add_f32 v31, v31, v179\n\t" \
"v_exp_f32 v181, v181\n\t" \
"v_add_f32 v30, v30, v180\n\t" \
"v_cvt_pk_bf16_f32 v217, v178, v179\n\t" \
"s_waitcnt lgkmcnt(7)\n\t" \
"v_mfma_f32_32x32x16_bf16 a[96:111], a[252:255], v[196:199], a[96:111]\n\t" \
"v_exp_f32 v182, v182\n\t" \
"v_add_f32 v31, v31, v181\n\t" \
"v_exp_f32 v183, v183\n\t" \
"v_add_f32 v30, v30, v182\n\t" \
"v_cvt_pk_bf16_f32 v218, v180, v181\n\t" \
"v_mfma_f32_32x32x16_bf16 a[112:127], a[252:255], v[212:215], a[112:127]\n\t" \
"ds_read_b128 a[252:255], v17 offset:15360\n\t" \
"v_exp_f32 v168, v168\n\t" \
"v_add_f32 v31, v31, v183\n\t" \
"v_exp_f32 v169, v169\n\t" \
"v_add_f32 v28, v28, v168\n\t" \
"v_cvt_pk_bf16_f32 v219, v182, v183\n\t" \
"s_waitcnt lgkmcnt(7)\n\t" \
"v_mfma_f32_32x32x16_bf16 a[0:15], a[224:227], v[200:203], a[0:15]\n\t" \
"v_exp_f32 v170, v170\n\t" \
"v_add_f32 v29, v29, v169\n\t" \
"v_exp_f32 v171, v171\n\t" \
"v_add_f32 v28, v28, v170\n\t" \
"v_cvt_pk_bf16_f32 v204, v168, v169\n\t" \
"v_mfma_f32_32x32x16_bf16 a[16:31], a[224:227], v[216:219], a[16:31]\n\t" \
"v_exp_f32 v172, v172\n\t" \
"v_add_f32 v29, v29, v171\n\t" \
"v_exp_f32 v173, v173\n\t" \
"v_add_f32 v28, v28, v172\n\t" \
"v_cvt_pk_bf16_f32 v205, v170, v171\n\t" \
"s_waitcnt lgkmcnt(6)\n\t" \
"v_mfma_f32_32x32x16_bf16 a[32:47], a[228:231], v[200:203], a[32:47]\n\t" \
"v_exp_f32 v174, v174\n\t" \
"v_add_f32 v29, v29, v173\n\t" \
"v_exp_f32 v175, v175\n\t" \
"v_add_f32 v28, v28, v174\n\t" \
"v_mfma_f32_32x32x16_bf16 a[48:63], a[228:231], v[216:219], a[48:63]\n\t" \
"v_cvt_pk_bf16_f32 v206, v172, v173\n\t" \
"v_exp_f32 v184, v184\n\t" \
"v_add_f32 v29, v29, v175\n\t" \
"v_exp_f32 v185, v185\n\t" \
"v_add_f32 v30, v30, v184\n\t" \
"s_waitcnt lgkmcnt(5)\n\t" \
"v_mfma_f32_32x32x16_bf16 a[64:79], a[232:235], v[200:203], a[64:79]\n\t" \
"v_cvt_pk_bf16_f32 v207, v174, v175\n\t" \
"v_exp_f32 v186, v186\n\t" \
"v_add_f32 v31, v31, v185\n\t" \
"v_exp_f32 v187, v187\n\t" \
"v_add_f32 v30, v30, v186\n\t" \
"v_mfma_f32_32x32x16_bf16 a[80:95], a[232:235], v[216:219], a[80:95]\n\t" \
"v_cvt_pk_bf16_f32 v220, v184, v185\n\t" \
"v_exp_f32 v188, v188\n\t" \
"v_add_f32 v31, v31, v187\n\t" \
"v_exp_f32 v189, v189\n\t" \
"s_waitcnt lgkmcnt(4)\n\t" \
"v_mfma_f32_32x32x16_bf16 a[96:111], a[236:239], v[200:203], a[96:111]\n\t" \
"v_add_f32 v30, v30, v188\n\t" \
"v_cvt_pk_bf16_f32 v221, v186, v187\n\t" \
"v_exp_f32 v190, v190\n\t" \
"v_add_f32 v31, v31, v189\n\t" \
"v_exp_f32 v191, v191\n\t" \
"v_mfma_f32_32x32x16_bf16 a[112:127], a[236:239], v[216:219], a[112:127]\n\t" \
"v_add_f32 v30, v30, v190\n\t" \
"v_cvt_pk_bf16_f32 v222, v188, v189\n\t" \
"v_add_f32 v31, v31, v191\n\t" \
"v_cvt_pk_bf16_f32 v223, v190, v191\n\t" \
"s_waitcnt lgkmcnt(3)\n\t" \
"v_mfma_f32_32x32x16_bf16 a[0:15], a[240:243], v[204:207], a[0:15]\n\t" \
"v_mfma_f32_32x32x16_bf16 a[16:31], a[240:243], v[220:223], a[16:31]\n\t" \
"s_waitcnt lgkmcnt(2)\n\t" \
"v_mfma_f32_32x32x16_bf16 a[32:47], a[244:247], v[204:207], a[32:47]\n\t" \
"v_mfma_f32_32x32x16_bf16 a[48:63], a[244:247], v[220:223], a[48:63]\n\t" \
"s_waitcnt lgkmcnt(1)\n\t" \
"v_mfma_f32_32x32x16_bf16 a[64:79], a[248:251], v[204:207], a[64:79]\n\t" \
"v_mfma_f32_32x32x16_bf16 a[80:95], a[248:251], v[220:223], a[80:95]\n\t" \
"s_waitcnt lgkmcnt(0)\n\t" \
"v_mfma_f32_32x32x16_bf16 a[96:111], a[252:255], v[204:207], a[96:111]\n\t" \
"v_mfma_f32_32x32x16_bf16 a[112:127], a[252:255], v[220:223], a[112:127]\n\t" \
"s_branch EPI_%=\n\t" \
"DRAIN0_%=:\n\t" \
"s_nop 1\n\t" \
"ds_read_b128 a[224:227], v17 offset:0\n\t" \
"ds_read_b128 a[228:231], v17 offset:1024\n\t" \
"ds_read_b128 a[232:235], v17 offset:2048\n\t" \
"ds_read_b128 a[236:239], v17 offset:3072\n\t" \
"ds_read_b128 a[240:243], v17 offset:4096\n\t" \
"ds_read_b128 a[244:247], v17 offset:5120\n\t" \
"ds_read_b128 a[248:251], v17 offset:6144\n\t" \
"s_waitcnt lgkmcnt(10)\n\t" \
"ds_read_b128 a[252:255], v17 offset:7168\n\t" \
"s_waitcnt lgkmcnt(7)\n\t" \
"v_mfma_f32_32x32x16_bf16 a[0:15], a[224:227], v[192:195], a[0:15]\n\t" \
"v_exp_f32 v75, v75\n\t" \
"v_add_f32 v28, v28, v74\n\t" \
"v_cvt_pk_bf16_f32 v196, v72, v73\n\t" \
"v_exp_f32 v76, v76\n\t" \
"v_add_f32 v29, v29, v75\n\t" \
"v_mfma_f32_32x32x16_bf16 a[16:31], a[224:227], v[208:211], a[16:31]\n\t" \
"ds_read_b128 a[224:227], v17 offset:8192\n\t" \
"v_exp_f32 v77, v77\n\t" \
"v_add_f32 v28, v28, v76\n\t" \
"v_cvt_pk_bf16_f32 v197, v74, v75\n\t" \
"v_exp_f32 v78, v78\n\t" \
"v_add_f32 v29, v29, v77\n\t" \
"s_waitcnt lgkmcnt(7)\n\t" \
"v_mfma_f32_32x32x16_bf16 a[32:47], a[228:231], v[192:195], a[32:47]\n\t" \
"v_exp_f32 v79, v79\n\t" \
"v_add_f32 v28, v28, v78\n\t" \
"v_cvt_pk_bf16_f32 v198, v76, v77\n\t" \
"v_exp_f32 v88, v88\n\t" \
"v_add_f32 v29, v29, v79\n\t" \
"v_mfma_f32_32x32x16_bf16 a[48:63], a[228:231], v[208:211], a[48:63]\n\t" \
"ds_read_b128 a[228:231], v17 offset:9216\n\t" \
"v_exp_f32 v89, v89\n\t" \
"v_add_f32 v30, v30, v88\n\t" \
"v_cvt_pk_bf16_f32 v199, v78, v79\n\t" \
"v_exp_f32 v90, v90\n\t" \
"s_waitcnt lgkmcnt(7)\n\t" \
"v_mfma_f32_32x32x16_bf16 a[64:79], a[232:235], v[192:195], a[64:79]\n\t" \
"v_add_f32 v31, v31, v89\n\t" \
"v_exp_f32 v91, v91\n\t" \
"v_add_f32 v30, v30, v90\n\t" \
"v_cvt_pk_bf16_f32 v212, v88, v89\n\t" \
"v_exp_f32 v92, v92\n\t" \
"v_mfma_f32_32x32x16_bf16 a[80:95], a[232:235], v[208:211], a[80:95]\n\t" \
"ds_read_b128 a[232:235], v17 offset:10240\n\t" \
"v_add_f32 v31, v31, v91\n\t" \
"v_exp_f32 v93, v93\n\t" \
"v_add_f32 v30, v30, v92\n\t" \
"v_cvt_pk_bf16_f32 v213, v90, v91\n\t" \
"v_exp_f32 v94, v94\n\t" \
"s_waitcnt lgkmcnt(7)\n\t" \
"v_mfma_f32_32x32x16_bf16 a[96:111], a[236:239], v[192:195], a[96:111]\n\t" \
"v_add_f32 v31, v31, v93\n\t" \
"v_exp_f32 v95, v95\n\t" \
"v_add_f32 v30, v30, v94\n\t" \
"v_cvt_pk_bf16_f32 v214, v92, v93\n\t" \
"v_exp_f32 v96, v96\n\t" \
"v_mfma_f32_32x32x16_bf16 a[112:127], a[236:239], v[208:211], a[112:127]\n\t" \
"ds_read_b128 a[236:239], v17 offset:11264\n\t" \
"v_add_f32 v31, v31, v95\n\t" \
"v_exp_f32 v97, v97\n\t" \
"v_add_f32 v28, v28, v96\n\t" \
"v_cvt_pk_bf16_f32 v215, v94, v95\n\t" \
"s_waitcnt lgkmcnt(7)\n\t" \
"v_mfma_f32_32x32x16_bf16 a[0:15], a[240:243], v[196:199], a[0:15]\n\t" \
"v_exp_f32 v98, v98\n\t" \
"v_add_f32 v29, v29, v97\n\t" \
"v_exp_f32 v99, v99\n\t" \
"v_add_f32 v28, v28, v98\n\t" \
"v_cvt_pk_bf16_f32 v200, v96, v97\n\t" \
"v_mfma_f32_32x32x16_bf16 a[16:31], a[240:243], v[212:215], a[16:31]\n\t" \
"ds_read_b128 a[240:243], v17 offset:12288\n\t" \
"v_exp_f32 v100, v100\n\t" \
"v_add_f32 v29, v29, v99\n\t" \
"v_exp_f32 v101, v101\n\t" \
"v_add_f32 v28, v28, v100\n\t" \
"v_cvt_pk_bf16_f32 v201, v98, v99\n\t" \
"s_waitcnt lgkmcnt(7)\n\t" \
"v_mfma_f32_32x32x16_bf16 a[32:47], a[244:247], v[196:199], a[32:47]\n\t" \
"v_exp_f32 v102, v102\n\t" \
"v_add_f32 v29, v29, v101\n\t" \
"v_exp_f32 v103, v103\n\t" \
"v_add_f32 v28, v28, v102\n\t" \
"v_cvt_pk_bf16_f32 v202, v100, v101\n\t" \
"v_mfma_f32_32x32x16_bf16 a[48:63], a[244:247], v[212:215], a[48:63]\n\t" \
"ds_read_b128 a[244:247], v17 offset:13312\n\t" \
"v_exp_f32 v112, v112\n\t" \
"v_add_f32 v29, v29, v103\n\t" \
"v_exp_f32 v113, v113\n\t" \
"v_add_f32 v30, v30, v112\n\t" \
"v_cvt_pk_bf16_f32 v203, v102, v103\n\t" \
"s_waitcnt lgkmcnt(7)\n\t" \
"v_mfma_f32_32x32x16_bf16 a[64:79], a[248:251], v[196:199], a[64:79]\n\t" \
"v_exp_f32 v114, v114\n\t" \
"v_add_f32 v31, v31, v113\n\t" \
"v_exp_f32 v115, v115\n\t" \
"v_add_f32 v30, v30, v114\n\t" \
"v_cvt_pk_bf16_f32 v216, v112, v113\n\t" \
"v_mfma_f32_32x32x16_bf16 a[80:95], a[248:251], v[212:215], a[80:95]\n\t" \
"ds_read_b128 a[248:251], v17 offset:14336\n\t" \
"v_exp_f32 v116, v116\n\t" \
"v_add_f32 v31, v31, v115\n\t" \
"v_exp_f32 v117, v117\n\t" \
"v_add_f32 v30, v30, v116\n\t" \
"v_cvt_pk_bf16_f32 v217, v114, v115\n\t" \
"s_waitcnt lgkmcnt(7)\n\t" \
"v_mfma_f32_32x32x16_bf16 a[96:111], a[252:255], v[196:199], a[96:111]\n\t" \
"v_exp_f32 v118, v118\n\t" \
"v_add_f32 v31, v31, v117\n\t" \
"v_exp_f32 v119, v119\n\t" \
"v_add_f32 v30, v30, v118\n\t" \
"v_cvt_pk_bf16_f32 v218, v116, v117\n\t" \
"v_mfma_f32_32x32x16_bf16 a[112:127], a[252:255], v[212:215], a[112:127]\n\t" \
"ds_read_b128 a[252:255], v17 offset:15360\n\t" \
"v_exp_f32 v104, v104\n\t" \
"v_add_f32 v31, v31, v119\n\t" \
"v_exp_f32 v105, v105\n\t" \
"v_add_f32 v28, v28, v104\n\t" \
"v_cvt_pk_bf16_f32 v219, v118, v119\n\t" \
"s_waitcnt lgkmcnt(7)\n\t" \
"v_mfma_f32_32x32x16_bf16 a[0:15], a[224:227], v[200:203], a[0:15]\n\t" \
"v_exp_f32 v106, v106\n\t" \
"v_add_f32 v29, v29, v105\n\t" \
"v_exp_f32 v107, v107\n\t" \
"v_add_f32 v28, v28, v106\n\t" \
"v_cvt_pk_bf16_f32 v204, v104, v105\n\t" \
"v_mfma_f32_32x32x16_bf16 a[16:31], a[224:227], v[216:219], a[16:31]\n\t" \
"v_exp_f32 v108, v108\n\t" \
"v_add_f32 v29, v29, v107\n\t" \
"v_exp_f32 v109, v109\n\t" \
"v_add_f32 v28, v28, v108\n\t" \
"v_cvt_pk_bf16_f32 v205, v106, v107\n\t" \
"s_waitcnt lgkmcnt(6)\n\t" \
"v_mfma_f32_32x32x16_bf16 a[32:47], a[228:231], v[200:203], a[32:47]\n\t" \
"v_exp_f32 v110, v110\n\t" \
"v_add_f32 v29, v29, v109\n\t" \
"v_exp_f32 v111, v111\n\t" \
"v_add_f32 v28, v28, v110\n\t" \
"v_mfma_f32_32x32x16_bf16 a[48:63], a[228:231], v[216:219], a[48:63]\n\t" \
"v_cvt_pk_bf16_f32 v206, v108, v109\n\t" \
"v_exp_f32 v120, v120\n\t" \
"v_add_f32 v29, v29, v111\n\t" \
"v_exp_f32 v121, v121\n\t" \
"v_add_f32 v30, v30, v120\n\t" \
"s_waitcnt lgkmcnt(5)\n\t" \
"v_mfma_f32_32x32x16_bf16 a[64:79], a[232:235], v[200:203], a[64:79]\n\t" \
"v_cvt_pk_bf16_f32 v207, v110, v111\n\t" \
"v_exp_f32 v122, v122\n\t" \
"v_add_f32 v31, v31, v121\n\t" \
"v_exp_f32 v123, v123\n\t" \
"v_add_f32 v30, v30, v122\n\t" \
"v_mfma_f32_32x32x16_bf16 a[80:95], a[232:235], v[216:219], a[80:95]\n\t" \
"v_cvt_pk_bf16_f32 v220, v120, v121\n\t" \
"v_exp_f32 v124, v124\n\t" \
"v_add_f32 v31, v31, v123\n\t" \
"v_exp_f32 v125, v125\n\t" \
"s_waitcnt lgkmcnt(4)\n\t" \
"v_mfma_f32_32x32x16_bf16 a[96:111], a[236:239], v[200:203], a[96:111]\n\t" \
"v_add_f32 v30, v30, v124\n\t" \
"v_cvt_pk_bf16_f32 v221, v122, v123\n\t" \
"v_exp_f32 v126, v126\n\t" \
"v_add_f32 v31, v31, v125\n\t" \
"v_exp_f32 v127, v127\n\t" \
"v_mfma_f32_32x32x16_bf16 a[112:127], a[236:239], v[216:219], a[112:127]\n\t" \
"v_add_f32 v30, v30, v126\n\t" \
"v_cvt_pk_bf16_f32 v222, v124, v125\n\t" \
"v_add_f32 v31, v31, v127\n\t" \
"v_cvt_pk_bf16_f32 v223, v126, v127\n\t" \
"s_waitcnt lgkmcnt(3)\n\t" \
"v_mfma_f32_32x32x16_bf16 a[0:15], a[240:243], v[204:207], a[0:15]\n\t" \
"v_mfma_f32_32x32x16_bf16 a[16:31], a[240:243], v[220:223], a[16:31]\n\t" \
"s_waitcnt lgkmcnt(2)\n\t" \
"v_mfma_f32_32x32x16_bf16 a[32:47], a[244:247], v[204:207], a[32:47]\n\t" \
"v_mfma_f32_32x32x16_bf16 a[48:63], a[244:247], v[220:223], a[48:63]\n\t" \
"s_waitcnt lgkmcnt(1)\n\t" \
"v_mfma_f32_32x32x16_bf16 a[64:79], a[248:251], v[204:207], a[64:79]\n\t" \
"v_mfma_f32_32x32x16_bf16 a[80:95], a[248:251], v[220:223], a[80:95]\n\t" \
"s_waitcnt lgkmcnt(0)\n\t" \
"v_mfma_f32_32x32x16_bf16 a[96:111], a[252:255], v[204:207], a[96:111]\n\t" \
"v_mfma_f32_32x32x16_bf16 a[112:127], a[252:255], v[220:223], a[112:127]\n\t" \
"s_branch EPI_%=\n\t" \
"DRAIN0F_%=:\n\t" \
"s_nop 1\n\t" \
"ds_read_b128 a[224:227], v17 offset:0\n\t" \
"ds_read_b128 a[228:231], v17 offset:1024\n\t" \
"ds_read_b128 a[232:235], v17 offset:2048\n\t" \
"ds_read_b128 a[236:239], v17 offset:3072\n\t" \
"ds_read_b128 a[240:243], v17 offset:4096\n\t" \
"ds_read_b128 a[244:247], v17 offset:5120\n\t" \
"ds_read_b128 a[248:251], v17 offset:6144\n\t" \
"s_waitcnt lgkmcnt(10)\n\t" \
"ds_read_b128 a[252:255], v17 offset:7168\n\t" \
"s_waitcnt lgkmcnt(7)\n\t" \
"v_mfma_f32_32x32x16_bf16 a[0:15], a[224:227], v[192:195], 0\n\t" \
"v_exp_f32 v75, v75\n\t" \
"v_add_f32 v28, v28, v74\n\t" \
"v_cvt_pk_bf16_f32 v196, v72, v73\n\t" \
"v_exp_f32 v76, v76\n\t" \
"v_add_f32 v29, v29, v75\n\t" \
"v_mfma_f32_32x32x16_bf16 a[16:31], a[224:227], v[208:211], 0\n\t" \
"ds_read_b128 a[224:227], v17 offset:8192\n\t" \
"v_exp_f32 v77, v77\n\t" \
"v_add_f32 v28, v28, v76\n\t" \
"v_cvt_pk_bf16_f32 v197, v74, v75\n\t" \
"v_exp_f32 v78, v78\n\t" \
"v_add_f32 v29, v29, v77\n\t" \
"s_waitcnt lgkmcnt(7)\n\t" \
"v_mfma_f32_32x32x16_bf16 a[32:47], a[228:231], v[192:195], 0\n\t" \
"v_exp_f32 v79, v79\n\t" \
"v_add_f32 v28, v28, v78\n\t" \
"v_cvt_pk_bf16_f32 v198, v76, v77\n\t" \
"v_exp_f32 v88, v88\n\t" \
"v_add_f32 v29, v29, v79\n\t" \
"v_mfma_f32_32x32x16_bf16 a[48:63], a[228:231], v[208:211], 0\n\t" \
"ds_read_b128 a[228:231], v17 offset:9216\n\t" \
"v_exp_f32 v89, v89\n\t" \
"v_add_f32 v30, v30, v88\n\t" \
"v_cvt_pk_bf16_f32 v199, v78, v79\n\t" \
"v_exp_f32 v90, v90\n\t" \
"s_waitcnt lgkmcnt(7)\n\t" \
"v_mfma_f32_32x32x16_bf16 a[64:79], a[232:235], v[192:195], 0\n\t" \
"v_add_f32 v31, v31, v89\n\t" \
"v_exp_f32 v91, v91\n\t" \
"v_add_f32 v30, v30, v90\n\t" \
"v_cvt_pk_bf16_f32 v212, v88, v89\n\t" \
"v_exp_f32 v92, v92\n\t" \
"v_mfma_f32_32x32x16_bf16 a[80:95], a[232:235], v[208:211], 0\n\t" \
"ds_read_b128 a[232:235], v17 offset:10240\n\t" \
"v_add_f32 v31, v31, v91\n\t" \
"v_exp_f32 v93, v93\n\t" \
"v_add_f32 v30, v30, v92\n\t" \
"v_cvt_pk_bf16_f32 v213, v90, v91\n\t" \
"v_exp_f32 v94, v94\n\t" \
"s_waitcnt lgkmcnt(7)\n\t" \
"v_mfma_f32_32x32x16_bf16 a[96:111], a[236:239], v[192:195], 0\n\t" \
"v_add_f32 v31, v31, v93\n\t" \
"v_exp_f32 v95, v95\n\t" \
"v_add_f32 v30, v30, v94\n\t" \
"v_cvt_pk_bf16_f32 v214, v92, v93\n\t" \
"v_exp_f32 v96, v96\n\t" \
"v_mfma_f32_32x32x16_bf16 a[112:127], a[236:239], v[208:211], 0\n\t" \
"ds_read_b128 a[236:239], v17 offset:11264\n\t" \
"v_add_f32 v31, v31, v95\n\t" \
"v_exp_f32 v97, v97\n\t" \
"v_add_f32 v28, v28, v96\n\t" \
"v_cvt_pk_bf16_f32 v215, v94, v95\n\t" \
"s_waitcnt lgkmcnt(7)\n\t" \
"v_mfma_f32_32x32x16_bf16 a[0:15], a[240:243], v[196:199], a[0:15]\n\t" \
"v_exp_f32 v98, v98\n\t" \
"v_add_f32 v29, v29, v97\n\t" \
"v_exp_f32 v99, v99\n\t" \
"v_add_f32 v28, v28, v98\n\t" \
"v_cvt_pk_bf16_f32 v200, v96, v97\n\t" \
"v_mfma_f32_32x32x16_bf16 a[16:31], a[240:243], v[212:215], a[16:31]\n\t" \
"ds_read_b128 a[240:243], v17 offset:12288\n\t" \
"v_exp_f32 v100, v100\n\t" \
"v_add_f32 v29, v29, v99\n\t" \
"v_exp_f32 v101, v101\n\t" \
"v_add_f32 v28, v28, v100\n\t" \
"v_cvt_pk_bf16_f32 v201, v98, v99\n\t" \
"s_waitcnt lgkmcnt(7)\n\t" \
"v_mfma_f32_32x32x16_bf16 a[32:47], a[244:247], v[196:199], a[32:47]\n\t" \
"v_exp_f32 v102, v102\n\t" \
"v_add_f32 v29, v29, v101\n\t" \
"v_exp_f32 v103, v103\n\t" \
"v_add_f32 v28, v28, v102\n\t" \
"v_cvt_pk_bf16_f32 v202, v100, v101\n\t" \
"v_mfma_f32_32x32x16_bf16 a[48:63], a[244:247], v[212:215], a[48:63]\n\t" \
"ds_read_b128 a[244:247], v17 offset:13312\n\t" \
"v_exp_f32 v112, v112\n\t" \
"v_add_f32 v29, v29, v103\n\t" \
"v_exp_f32 v113, v113\n\t" \
"v_add_f32 v30, v30, v112\n\t" \
"v_cvt_pk_bf16_f32 v203, v102, v103\n\t" \
"s_waitcnt lgkmcnt(7)\n\t" \
"v_mfma_f32_32x32x16_bf16 a[64:79], a[248:251], v[196:199], a[64:79]\n\t" \
"v_exp_f32 v114, v114\n\t" \
"v_add_f32 v31, v31, v113\n\t" \
"v_exp_f32 v115, v115\n\t" \
"v_add_f32 v30, v30, v114\n\t" \
"v_cvt_pk_bf16_f32 v216, v112, v113\n\t" \
"v_mfma_f32_32x32x16_bf16 a[80:95], a[248:251], v[212:215], a[80:95]\n\t" \
"ds_read_b128 a[248:251], v17 offset:14336\n\t" \
"v_exp_f32 v116, v116\n\t" \
"v_add_f32 v31, v31, v115\n\t" \
"v_exp_f32 v117, v117\n\t" \
"v_add_f32 v30, v30, v116\n\t" \
"v_cvt_pk_bf16_f32 v217, v114, v115\n\t" \
"s_waitcnt lgkmcnt(7)\n\t" \
"v_mfma_f32_32x32x16_bf16 a[96:111], a[252:255], v[196:199], a[96:111]\n\t" \
"v_exp_f32 v118, v118\n\t" \
"v_add_f32 v31, v31, v117\n\t" \
"v_exp_f32 v119, v119\n\t" \
"v_add_f32 v30, v30, v118\n\t" \
"v_cvt_pk_bf16_f32 v218, v116, v117\n\t" \
"v_mfma_f32_32x32x16_bf16 a[112:127], a[252:255], v[212:215], a[112:127]\n\t" \
"ds_read_b128 a[252:255], v17 offset:15360\n\t" \
"v_exp_f32 v104, v104\n\t" \
"v_add_f32 v31, v31, v119\n\t" \
"v_exp_f32 v105, v105\n\t" \
"v_add_f32 v28, v28, v104\n\t" \
"v_cvt_pk_bf16_f32 v219, v118, v119\n\t" \
"s_waitcnt lgkmcnt(7)\n\t" \
"v_mfma_f32_32x32x16_bf16 a[0:15], a[224:227], v[200:203], a[0:15]\n\t" \
"v_exp_f32 v106, v106\n\t" \
"v_add_f32 v29, v29, v105\n\t" \
"v_exp_f32 v107, v107\n\t" \
"v_add_f32 v28, v28, v106\n\t" \
"v_cvt_pk_bf16_f32 v204, v104, v105\n\t" \
"v_mfma_f32_32x32x16_bf16 a[16:31], a[224:227], v[216:219], a[16:31]\n\t" \
"v_exp_f32 v108, v108\n\t" \
"v_add_f32 v29, v29, v107\n\t" \
"v_exp_f32 v109, v109\n\t" \
"v_add_f32 v28, v28, v108\n\t" \
"v_cvt_pk_bf16_f32 v205, v106, v107\n\t" \
"s_waitcnt lgkmcnt(6)\n\t" \
"v_mfma_f32_32x32x16_bf16 a[32:47], a[228:231], v[200:203], a[32:47]\n\t" \
"v_exp_f32 v110, v110\n\t" \
"v_add_f32 v29, v29, v109\n\t" \
"v_exp_f32 v111, v111\n\t" \
"v_add_f32 v28, v28, v110\n\t" \
"v_mfma_f32_32x32x16_bf16 a[48:63], a[228:231], v[216:219], a[48:63]\n\t" \
"v_cvt_pk_bf16_f32 v206, v108, v109\n\t" \
"v_exp_f32 v120, v120\n\t" \
"v_add_f32 v29, v29, v111\n\t" \
"v_exp_f32 v121, v121\n\t" \
"v_add_f32 v30, v30, v120\n\t" \
"s_waitcnt lgkmcnt(5)\n\t" \
"v_mfma_f32_32x32x16_bf16 a[64:79], a[232:235], v[200:203], a[64:79]\n\t" \
"v_cvt_pk_bf16_f32 v207, v110, v111\n\t" \
"v_exp_f32 v122, v122\n\t" \
"v_add_f32 v31, v31, v121\n\t" \
"v_exp_f32 v123, v123\n\t" \
"v_add_f32 v30, v30, v122\n\t" \
"v_mfma_f32_32x32x16_bf16 a[80:95], a[232:235], v[216:219], a[80:95]\n\t" \
"v_cvt_pk_bf16_f32 v220, v120, v121\n\t" \
"v_exp_f32 v124, v124\n\t" \
"v_add_f32 v31, v31, v123\n\t" \
"v_exp_f32 v125, v125\n\t" \
"s_waitcnt lgkmcnt(4)\n\t" \
"v_mfma_f32_32x32x16_bf16 a[96:111], a[236:239], v[200:203], a[96:111]\n\t" \
"v_add_f32 v30, v30, v124\n\t" \
"v_cvt_pk_bf16_f32 v221, v122, v123\n\t" \
"v_exp_f32 v126, v126\n\t" \
"v_add_f32 v31, v31, v125\n\t" \
"v_exp_f32 v127, v127\n\t" \
"v_mfma_f32_32x32x16_bf16 a[112:127], a[236:239], v[216:219], a[112:127]\n\t" \
"v_add_f32 v30, v30, v126\n\t" \
"v_cvt_pk_bf16_f32 v222, v124, v125\n\t" \
"v_add_f32 v31, v31, v127\n\t" \
"v_cvt_pk_bf16_f32 v223, v126, v127\n\t" \
"s_waitcnt lgkmcnt(3)\n\t" \
"v_mfma_f32_32x32x16_bf16 a[0:15], a[240:243], v[204:207], a[0:15]\n\t" \
"v_mfma_f32_32x32x16_bf16 a[16:31], a[240:243], v[220:223], a[16:31]\n\t" \
"s_waitcnt lgkmcnt(2)\n\t" \
"v_mfma_f32_32x32x16_bf16 a[32:47], a[244:247], v[204:207], a[32:47]\n\t" \
"v_mfma_f32_32x32x16_bf16 a[48:63], a[244:247], v[220:223], a[48:63]\n\t" \
"s_waitcnt lgkmcnt(1)\n\t" \
"v_mfma_f32_32x32x16_bf16 a[64:79], a[248:251], v[204:207], a[64:79]\n\t" \
"v_mfma_f32_32x32x16_bf16 a[80:95], a[248:251], v[220:223], a[80:95]\n\t" \
"s_waitcnt lgkmcnt(0)\n\t" \
"v_mfma_f32_32x32x16_bf16 a[96:111], a[252:255], v[204:207], a[96:111]\n\t" \
"v_mfma_f32_32x32x16_bf16 a[112:127], a[252:255], v[220:223], a[112:127]\n\t" \
"EPI_%=:\n\t" \
"s_nop 15\n\t" \
"s_waitcnt lgkmcnt(0)\n\t" \
"v_mov_b32 v60, 0\n\t" \
"buffer_load_dword v60, v60, %0, s79 offen sc1\n\t" \
"s_add_u32 s54, s77, 131072\n\t" \
"v_add_u32 v27, s54, v36\n\t" \
"ds_read_b128 a[128:131], v27 offset:0\n\t" \
"ds_read_b128 a[132:135], v27 offset:1024\n\t" \
"ds_read_b128 a[136:139], v27 offset:2048\n\t" \
"ds_read_b128 a[140:143], v27 offset:3072\n\t" \
"ds_read_b128 a[144:147], v27 offset:4096\n\t" \
"ds_read_b128 a[148:151], v27 offset:5120\n\t" \
"ds_read_b128 a[152:155], v27 offset:6144\n\t" \
"ds_read_b128 a[156:159], v27 offset:7168\n\t" \
"ds_read_b128 a[160:163], v27 offset:8192\n\t" \
"ds_read_b128 a[164:167], v27 offset:9216\n\t" \
"ds_read_b128 a[168:171], v27 offset:10240\n\t" \
"ds_read_b128 a[172:175], v27 offset:11264\n\t" \
"ds_read_b128 a[176:179], v27 offset:12288\n\t" \
"ds_read_b128 a[180:183], v27 offset:13312\n\t" \
"ds_read_b128 a[184:187], v27 offset:14336\n\t" \
"s_waitcnt lgkmcnt(10)\n\t" \
"ds_read_b128 a[188:191], v27 offset:15360\n\t" \
"s_waitcnt lgkmcnt(0)\n\t" \
"s_barrier\n\t" \
"v_xor_b32 v17, 0x8000, v17\n\t" \
"v_add_f32 v28, v28, v29\n\t" \
"v_add_f32 v30, v30, v31\n\t" \
"v_mov_b32 v29, v28\n\t" \
"v_mov_b32 v31, v30\n\t" \
"s_nop 0\n\t" \
"v_permlane32_swap_b32 v28, v29\n\t" \
"v_permlane32_swap_b32 v30, v31\n\t" \
"v_add_f32 v28, v28, v29\n\t" \
"v_add_f32 v30, v30, v31\n\t" \
"s_sub_u32 s56, %7, %8\n\t" \
"s_lshl_b32 s56, s56, 1\n\t" \
"s_add_u32 s56, s56, %8\n\t" \
"s_add_u32 s56, s56, 0x10000\n\t" \
"v_add_u32 v39, s56, v36\n\t" \
"v_lshrrev_b32 v24, 2, v36\n\t" \
"s_sub_u32 s57, %7, %8\n\t" \
"s_lshr_b32 s57, s57, 4\n\t" \
"s_add_u32 s57, s57, %8\n\t" \
"s_add_u32 s57, s57, 0x20000\n\t" \
"v_add_u32 v24, s57, v24\n\t" \
"s_mov_b32 s59, s53\n\t" \
"s_sub_u32 s53, %6, 1\n\t" \
"s_cmp_lg_u32 s76, 0\n\t" \
"s_cbranch_scc1 EPI1_%=\n\t" \
"ds_write_b128 v39, a[16:19] offset:0\n\t" \
"ds_write_b128 v39, a[20:23] offset:1024\n\t" \
"ds_write_b128 v39, a[24:27] offset:2048\n\t" \
"ds_write_b128 v39, a[28:31] offset:3072\n\t" \
"ds_write_b128 v39, a[48:51] offset:4096\n\t" \
"ds_write_b128 v39, a[52:55] offset:5120\n\t" \
"ds_write_b128 v39, a[56:59] offset:6144\n\t" \
"ds_write_b128 v39, a[60:63] offset:7168\n\t" \
"ds_write_b128 v39, a[80:83] offset:8192\n\t" \
"ds_write_b128 v39, a[84:87] offset:9216\n\t" \
"ds_write_b128 v39, a[88:91] offset:10240\n\t" \
"ds_write_b128 v39, a[92:95] offset:11264\n\t" \
"s_waitcnt lgkmcnt(4)\n\t" \
"ds_write_b128 v39, a[112:115] offset:12288\n\t" \
"ds_write_b128 v39, a[116:119] offset:13312\n\t" \
"ds_write_b128 v39, a[120:123] offset:14336\n\t" \
"ds_write_b128 v39, a[124:127] offset:15360\n\t" \
"ds_write_b32 v24, v33\n\t" \
"ds_write_b32 v24, v30 offset:256\n\t" \
"s_waitcnt lgkmcnt(0)\n\t" \
"s_barrier\n\t" \
"v_xor_b32 v39, 0x8000, v39\n\t" \
"v_xor_b32 v24, 0x400, v24\n\t" \
"ds_read_b32 v25, v24\n\t" \
"ds_read_b32 v26, v24 offset:256\n\t" \
"ds_read_b128 v[64:67], v39 offset:0\n\t" \
"ds_read_b128 v[68:71], v39 offset:1024\n\t" \
"ds_read_b128 v[72:75], v39 offset:2048\n\t" \
"ds_read_b128 v[76:79], v39 offset:3072\n\t" \
"ds_read_b128 v[80:83], v39 offset:4096\n\t" \
"ds_read_b128 v[84:87], v39 offset:5120\n\t" \
"ds_read_b128 v[88:91], v39 offset:6144\n\t" \
"ds_read_b128 v[92:95], v39 offset:7168\n\t" \
"ds_read_b128 v[96:99], v39 offset:8192\n\t" \
"ds_read_b128 v[100:103], v39 offset:9216\n\t" \
"ds_read_b128 v[104:107], v39 offset:10240\n\t" \
"ds_read_b128 v[108:111], v39 offset:11264\n\t" \
"s_waitcnt lgkmcnt(13)\n\t" \
"v_max_f32 v22, v32, v25\n\t" \
"v_sub_f32 v23, v32, v22\n\t" \
"v_sub_f32 v27, v25, v22\n\t" \
"v_exp_f32 v23, v23\n\t" \
"v_exp_f32 v27, v27\n\t" \
"v_mul_f32 v22, v28, v23\n\t" \
"s_waitcnt lgkmcnt(12)\n\t" \
"v_fmac_f32 v22, v26, v27\n\t" \
"v_rcp_f32 v22, v22\n\t" \
"s_nop 0\n\t" \
"v_mul_f32 v22, v22, v38\n\t" \
"v_mul_f32 v23, v23, v22\n\t" \
"v_mul_f32 v27, v27, v22\n\t" \
"v_mov_b32 v44, v23\n\t" \
"v_mov_b32 v46, v27\n\t" \
"v_mov_b32 v45, v23\n\t" \
"v_mov_b32 v47, v27\n\t" \
"ds_read_b128 v[112:115], v39 offset:12288\n\t" \
"ds_read_b128 v[116:119], v39 offset:13312\n\t" \
"ds_read_b128 v[120:123], v39 offset:14336\n\t" \
"s_waitcnt lgkmcnt(10)\n\t" \
"ds_read_b128 v[124:127], v39 offset:15360\n\t" \
"s_mul_i32 s56, s59, 1179648\n\t" \
"s_add_u32 s56, s56, %3\n\t" \
"v_accvgpr_read_b32 v128, a0\n\t" \
"v_accvgpr_read_b32 v129, a1\n\t" \
"v_accvgpr_read_b32 v130, a2\n\t" \
"v_accvgpr_read_b32 v131, a3\n\t" \
"v_accvgpr_read_b32 v132, a4\n\t" \
"v_accvgpr_read_b32 v133, a5\n\t" \
"v_accvgpr_read_b32 v134, a6\n\t" \
"v_accvgpr_read_b32 v135, a7\n\t" \
"v_pk_mul_f32 v[64:65], v[64:65], v[46:47]\n\t" \
"v_pk_mul_f32 v[66:67], v[66:67], v[46:47]\n\t" \
"v_pk_mul_f32 v[68:69], v[68:69], v[46:47]\n\t" \
"v_pk_mul_f32 v[70:71], v[70:71], v[46:47]\n\t" \
"v_pk_fma_f32 v[64:65], v[128:129], v[44:45], v[64:65]\n\t" \
"v_pk_fma_f32 v[66:67], v[130:131], v[44:45], v[66:67]\n\t" \
"v_pk_fma_f32 v[68:69], v[132:133], v[44:45], v[68:69]\n\t" \
"v_pk_fma_f32 v[70:71], v[134:135], v[44:45], v[70:71]\n\t" \
"v_cvt_pk_bf16_f32 v136, v64, v65\n\t" \
"v_cvt_pk_bf16_f32 v137, v66, v67\n\t" \
"v_cvt_pk_bf16_f32 v138, v68, v69\n\t" \
"v_cvt_pk_bf16_f32 v139, v70, v71\n\t" \
"s_add_u32 s57, s56, 0\n\t" \
"buffer_store_dwordx4 v[136:139], v36, %0, s57 offen sc1\n\t" \
"v_accvgpr_read_b32 v144, a8\n\t" \
"v_accvgpr_read_b32 v145, a9\n\t" \
"v_accvgpr_read_b32 v146, a10\n\t" \
"v_accvgpr_read_b32 v147, a11\n\t" \
"v_accvgpr_read_b32 v148, a12\n\t" \
"v_accvgpr_read_b32 v149, a13\n\t" \
"v_accvgpr_read_b32 v150, a14\n\t" \
"v_accvgpr_read_b32 v151, a15\n\t" \
"v_pk_mul_f32 v[72:73], v[72:73], v[46:47]\n\t" \
"v_pk_mul_f32 v[74:75], v[74:75], v[46:47]\n\t" \
"v_pk_mul_f32 v[76:77], v[76:77], v[46:47]\n\t" \
"v_pk_mul_f32 v[78:79], v[78:79], v[46:47]\n\t" \
"v_pk_fma_f32 v[72:73], v[144:145], v[44:45], v[72:73]\n\t" \
"v_pk_fma_f32 v[74:75], v[146:147], v[44:45], v[74:75]\n\t" \
"v_pk_fma_f32 v[76:77], v[148:149], v[44:45], v[76:77]\n\t" \
"v_pk_fma_f32 v[78:79], v[150:151], v[44:45], v[78:79]\n\t" \
"v_cvt_pk_bf16_f32 v152, v72, v73\n\t" \
"v_cvt_pk_bf16_f32 v153, v74, v75\n\t" \
"v_cvt_pk_bf16_f32 v154, v76, v77\n\t" \
"v_cvt_pk_bf16_f32 v155, v78, v79\n\t" \
"s_add_u32 s57, s56, 1024\n\t" \
"buffer_store_dwordx4 v[152:155], v36, %0, s57 offen sc1\n\t" \
"v_accvgpr_read_b32 v160, a32\n\t" \
"v_accvgpr_read_b32 v161, a33\n\t" \
"v_accvgpr_read_b32 v162, a34\n\t" \
"v_accvgpr_read_b32 v163, a35\n\t" \
"v_accvgpr_read_b32 v164, a36\n\t" \
"v_accvgpr_read_b32 v165, a37\n\t" \
"v_accvgpr_read_b32 v166, a38\n\t" \
"v_accvgpr_read_b32 v167, a39\n\t" \
"v_pk_mul_f32 v[80:81], v[80:81], v[46:47]\n\t" \
"v_pk_mul_f32 v[82:83], v[82:83], v[46:47]\n\t" \
"s_waitcnt lgkmcnt(10)\n\t" \
"v_pk_mul_f32 v[84:85], v[84:85], v[46:47]\n\t" \
"v_pk_mul_f32 v[86:87], v[86:87], v[46:47]\n\t" \
"v_pk_fma_f32 v[80:81], v[160:161], v[44:45], v[80:81]\n\t" \
"v_pk_fma_f32 v[82:83], v[162:163], v[44:45], v[82:83]\n\t" \
"v_pk_fma_f32 v[84:85], v[164:165], v[44:45], v[84:85]\n\t" \
"v_pk_fma_f32 v[86:87], v[166:167], v[44:45], v[86:87]\n\t" \
"v_cvt_pk_bf16_f32 v168, v80, v81\n\t" \
"v_cvt_pk_bf16_f32 v169, v82, v83\n\t" \
"v_cvt_pk_bf16_f32 v170, v84, v85\n\t" \
"v_cvt_pk_bf16_f32 v171, v86, v87\n\t" \
"s_add_u32 s57, s56, 2048\n\t" \
"buffer_store_dwordx4 v[168:171], v36, %0, s57 offen sc1\n\t" \
"v_accvgpr_read_b32 v176, a40\n\t" \
"v_accvgpr_read_b32 v177, a41\n\t" \
"v_accvgpr_read_b32 v178, a42\n\t" \
"v_accvgpr_read_b32 v179, a43\n\t" \
"v_accvgpr_read_b32 v180, a44\n\t" \
"v_accvgpr_read_b32 v181, a45\n\t" \
"v_accvgpr_read_b32 v182, a46\n\t" \
"v_accvgpr_read_b32 v183, a47\n\t" \
"s_waitcnt lgkmcnt(9)\n\t" \
"v_pk_mul_f32 v[88:89], v[88:89], v[46:47]\n\t" \
"v_pk_mul_f32 v[90:91], v[90:91], v[46:47]\n\t" \
"s_waitcnt lgkmcnt(8)\n\t" \
"v_pk_mul_f32 v[92:93], v[92:93], v[46:47]\n\t" \
"v_pk_mul_f32 v[94:95], v[94:95], v[46:47]\n\t" \
"v_pk_fma_f32 v[88:89], v[176:177], v[44:45], v[88:89]\n\t" \
"v_pk_fma_f32 v[90:91], v[178:179], v[44:45], v[90:91]\n\t" \
"v_pk_fma_f32 v[92:93], v[180:181], v[44:45], v[92:93]\n\t" \
"v_pk_fma_f32 v[94:95], v[182:183], v[44:45], v[94:95]\n\t" \
"v_cvt_pk_bf16_f32 v184, v88, v89\n\t" \
"v_cvt_pk_bf16_f32 v185, v90, v91\n\t" \
"v_cvt_pk_bf16_f32 v186, v92, v93\n\t" \
"v_cvt_pk_bf16_f32 v187, v94, v95\n\t" \
"s_add_u32 s57, s56, 3072\n\t" \
"buffer_store_dwordx4 v[184:187], v36, %0, s57 offen sc1\n\t" \
"v_accvgpr_read_b32 v128, a64\n\t" \
"v_accvgpr_read_b32 v129, a65\n\t" \
"v_accvgpr_read_b32 v130, a66\n\t" \
"v_accvgpr_read_b32 v131, a67\n\t" \
"v_accvgpr_read_b32 v132, a68\n\t" \
"v_accvgpr_read_b32 v133, a69\n\t" \
"v_accvgpr_read_b32 v134, a70\n\t" \
"v_accvgpr_read_b32 v135, a71\n\t" \
"s_waitcnt lgkmcnt(7)\n\t" \
"v_pk_mul_f32 v[96:97], v[96:97], v[46:47]\n\t" \
"v_pk_mul_f32 v[98:99], v[98:99], v[46:47]\n\t" \
"s_waitcnt lgkmcnt(6)\n\t" \
"v_pk_mul_f32 v[100:101], v[100:101], v[46:47]\n\t" \
"v_pk_mul_f32 v[102:103], v[102:103], v[46:47]\n\t" \
"v_pk_fma_f32 v[96:97], v[128:129], v[44:45], v[96:97]\n\t" \
"v_pk_fma_f32 v[98:99], v[130:131], v[44:45], v[98:99]\n\t" \
"v_pk_fma_f32 v[100:101], v[132:133], v[44:45], v[100:101]\n\t" \
"v_pk_fma_f32 v[102:103], v[134:135], v[44:45], v[102:103]\n\t" \
"v_cvt_pk_bf16_f32 v136, v96, v97\n\t" \
"v_cvt_pk_bf16_f32 v137, v98, v99\n\t" \
"v_cvt_pk_bf16_f32 v138, v100, v101\n\t" \
"v_cvt_pk_bf16_f32 v139, v102, v103\n\t" \
"s_add_u32 s57, s56, 4096\n\t" \
"buffer_store_dwordx4 v[136:139], v36, %0, s57 offen sc1\n\t" \
"v_accvgpr_read_b32 v144, a72\n\t" \
"v_accvgpr_read_b32 v145, a73\n\t" \
"v_accvgpr_read_b32 v146, a74\n\t" \
"v_accvgpr_read_b32 v147, a75\n\t" \
"v_accvgpr_read_b32 v148, a76\n\t" \
"v_accvgpr_read_b32 v149, a77\n\t" \
"v_accvgpr_read_b32 v150, a78\n\t" \
"v_accvgpr_read_b32 v151, a79\n\t" \
"s_waitcnt lgkmcnt(5)\n\t" \
"v_pk_mul_f32 v[104:105], v[104:105], v[46:47]\n\t" \
"v_pk_mul_f32 v[106:107], v[106:107], v[46:47]\n\t" \
"s_waitcnt lgkmcnt(4)\n\t" \
"v_pk_mul_f32 v[108:109], v[108:109], v[46:47]\n\t" \
"v_pk_mul_f32 v[110:111], v[110:111], v[46:47]\n\t" \
"v_pk_fma_f32 v[104:105], v[144:145], v[44:45], v[104:105]\n\t" \
"v_pk_fma_f32 v[106:107], v[146:147], v[44:45], v[106:107]\n\t" \
"v_pk_fma_f32 v[108:109], v[148:149], v[44:45], v[108:109]\n\t" \
"v_pk_fma_f32 v[110:111], v[150:151], v[44:45], v[110:111]\n\t" \
"v_cvt_pk_bf16_f32 v152, v104, v105\n\t" \
"v_cvt_pk_bf16_f32 v153, v106, v107\n\t" \
"v_cvt_pk_bf16_f32 v154, v108, v109\n\t" \
"v_cvt_pk_bf16_f32 v155, v110, v111\n\t" \
"s_add_u32 s57, s56, 5120\n\t" \
"buffer_store_dwordx4 v[152:155], v36, %0, s57 offen sc1\n\t" \
"v_accvgpr_read_b32 v160, a96\n\t" \
"v_accvgpr_read_b32 v161, a97\n\t" \
"v_accvgpr_read_b32 v162, a98\n\t" \
"v_accvgpr_read_b32 v163, a99\n\t" \
"v_accvgpr_read_b32 v164, a100\n\t" \
"v_accvgpr_read_b32 v165, a101\n\t" \
"v_accvgpr_read_b32 v166, a102\n\t" \
"v_accvgpr_read_b32 v167, a103\n\t" \
"s_waitcnt lgkmcnt(3)\n\t" \
"v_pk_mul_f32 v[112:113], v[112:113], v[46:47]\n\t" \
"v_pk_mul_f32 v[114:115], v[114:115], v[46:47]\n\t" \
"s_waitcnt lgkmcnt(2)\n\t" \
"v_pk_mul_f32 v[116:117], v[116:117], v[46:47]\n\t" \
"v_pk_mul_f32 v[118:119], v[118:119], v[46:47]\n\t" \
"v_pk_fma_f32 v[112:113], v[160:161], v[44:45], v[112:113]\n\t" \
"v_pk_fma_f32 v[114:115], v[162:163], v[44:45], v[114:115]\n\t" \
"v_pk_fma_f32 v[116:117], v[164:165], v[44:45], v[116:117]\n\t" \
"v_pk_fma_f32 v[118:119], v[166:167], v[44:45], v[118:119]\n\t" \
"v_cvt_pk_bf16_f32 v168, v112, v113\n\t" \
"v_cvt_pk_bf16_f32 v169, v114, v115\n\t" \
"v_cvt_pk_bf16_f32 v170, v116, v117\n\t" \
"v_cvt_pk_bf16_f32 v171, v118, v119\n\t" \
"s_add_u32 s57, s56, 6144\n\t" \
"buffer_store_dwordx4 v[168:171], v36, %0, s57 offen sc1\n\t" \
"v_accvgpr_read_b32 v176, a104\n\t" \
"v_accvgpr_read_b32 v177, a105\n\t" \
"v_accvgpr_read_b32 v178, a106\n\t" \
"v_accvgpr_read_b32 v179, a107\n\t" \
"v_accvgpr_read_b32 v180, a108\n\t" \
"v_accvgpr_read_b32 v181, a109\n\t" \
"v_accvgpr_read_b32 v182, a110\n\t" \
"v_accvgpr_read_b32 v183, a111\n\t" \
"s_waitcnt lgkmcnt(1)\n\t" \
"v_pk_mul_f32 v[120:121], v[120:121], v[46:47]\n\t" \
"v_pk_mul_f32 v[122:123], v[122:123], v[46:47]\n\t" \
"s_waitcnt lgkmcnt(0)\n\t" \
"v_pk_mul_f32 v[124:125], v[124:125], v[46:47]\n\t" \
"v_pk_mul_f32 v[126:127], v[126:127], v[46:47]\n\t" \
"v_pk_fma_f32 v[120:121], v[176:177], v[44:45], v[120:121]\n\t" \
"v_pk_fma_f32 v[122:123], v[178:179], v[44:45], v[122:123]\n\t" \
"v_pk_fma_f32 v[124:125], v[180:181], v[44:45], v[124:125]\n\t" \
"v_pk_fma_f32 v[126:127], v[182:183], v[44:45], v[126:127]\n\t" \
"v_cvt_pk_bf16_f32 v184, v120, v121\n\t" \
"v_cvt_pk_bf16_f32 v185, v122, v123\n\t" \
"v_cvt_pk_bf16_f32 v186, v124, v125\n\t" \
"v_cvt_pk_bf16_f32 v187, v126, v127\n\t" \
"s_add_u32 s57, s56, 7168\n\t" \
"buffer_store_dwordx4 v[184:187], v36, %0, s57 offen sc1\n\t" \
"s_branch EPIEND_%=\n\t" \
"EPI1_%=:\n\t" \
"ds_write_b128 v39, a[0:3] offset:0\n\t" \
"ds_write_b128 v39, a[4:7] offset:1024\n\t" \
"ds_write_b128 v39, a[8:11] offset:2048\n\t" \
"ds_write_b128 v39, a[12:15] offset:3072\n\t" \
"ds_write_b128 v39, a[32:35] offset:4096\n\t" \
"ds_write_b128 v39, a[36:39] offset:5120\n\t" \
"ds_write_b128 v39, a[40:43] offset:6144\n\t" \
"ds_write_b128 v39, a[44:47] offset:7168\n\t" \
"ds_write_b128 v39, a[64:67] offset:8192\n\t" \
"ds_write_b128 v39, a[68:71] offset:9216\n\t" \
"ds_write_b128 v39, a[72:75] offset:10240\n\t" \
"ds_write_b128 v39, a[76:79] offset:11264\n\t" \
"s_waitcnt lgkmcnt(4)\n\t" \
"ds_write_b128 v39, a[96:99] offset:12288\n\t" \
"ds_write_b128 v39, a[100:103] offset:13312\n\t" \
"ds_write_b128 v39, a[104:107] offset:14336\n\t" \
"ds_write_b128 v39, a[108:111] offset:15360\n\t" \
"ds_write_b32 v24, v32\n\t" \
"ds_write_b32 v24, v28 offset:256\n\t" \
"s_waitcnt lgkmcnt(0)\n\t" \
"s_barrier\n\t" \
"v_xor_b32 v39, 0x8000, v39\n\t" \
"v_xor_b32 v24, 0x400, v24\n\t" \
"ds_read_b32 v25, v24\n\t" \
"ds_read_b32 v26, v24 offset:256\n\t" \
"ds_read_b128 v[64:67], v39 offset:0\n\t" \
"ds_read_b128 v[68:71], v39 offset:1024\n\t" \
"ds_read_b128 v[72:75], v39 offset:2048\n\t" \
"ds_read_b128 v[76:79], v39 offset:3072\n\t" \
"ds_read_b128 v[80:83], v39 offset:4096\n\t" \
"ds_read_b128 v[84:87], v39 offset:5120\n\t" \
"ds_read_b128 v[88:91], v39 offset:6144\n\t" \
"ds_read_b128 v[92:95], v39 offset:7168\n\t" \
"ds_read_b128 v[96:99], v39 offset:8192\n\t" \
"ds_read_b128 v[100:103], v39 offset:9216\n\t" \
"ds_read_b128 v[104:107], v39 offset:10240\n\t" \
"ds_read_b128 v[108:111], v39 offset:11264\n\t" \
"s_waitcnt lgkmcnt(13)\n\t" \
"v_max_f32 v22, v33, v25\n\t" \
"v_sub_f32 v23, v33, v22\n\t" \
"v_sub_f32 v27, v25, v22\n\t" \
"v_exp_f32 v23, v23\n\t" \
"v_exp_f32 v27, v27\n\t" \
"v_mul_f32 v22, v30, v23\n\t" \
"s_waitcnt lgkmcnt(12)\n\t" \
"v_fmac_f32 v22, v26, v27\n\t" \
"v_rcp_f32 v22, v22\n\t" \
"s_nop 0\n\t" \
"v_mul_f32 v22, v22, v38\n\t" \
"v_mul_f32 v23, v23, v22\n\t" \
"v_mul_f32 v27, v27, v22\n\t" \
"v_mov_b32 v44, v23\n\t" \
"v_mov_b32 v46, v27\n\t" \
"v_mov_b32 v45, v23\n\t" \
"v_mov_b32 v47, v27\n\t" \
"ds_read_b128 v[112:115], v39 offset:12288\n\t" \
"ds_read_b128 v[116:119], v39 offset:13312\n\t" \
"ds_read_b128 v[120:123], v39 offset:14336\n\t" \
"s_waitcnt lgkmcnt(10)\n\t" \
"ds_read_b128 v[124:127], v39 offset:15360\n\t" \
"s_mul_i32 s56, s59, 1179648\n\t" \
"s_add_u32 s56, s56, %3\n\t" \
"v_accvgpr_read_b32 v128, a16\n\t" \
"v_accvgpr_read_b32 v129, a17\n\t" \
"v_accvgpr_read_b32 v130, a18\n\t" \
"v_accvgpr_read_b32 v131, a19\n\t" \
"v_accvgpr_read_b32 v132, a20\n\t" \
"v_accvgpr_read_b32 v133, a21\n\t" \
"v_accvgpr_read_b32 v134, a22\n\t" \
"v_accvgpr_read_b32 v135, a23\n\t" \
"v_pk_mul_f32 v[64:65], v[64:65], v[46:47]\n\t" \
"v_pk_mul_f32 v[66:67], v[66:67], v[46:47]\n\t" \
"v_pk_mul_f32 v[68:69], v[68:69], v[46:47]\n\t" \
"v_pk_mul_f32 v[70:71], v[70:71], v[46:47]\n\t" \
"v_pk_fma_f32 v[64:65], v[128:129], v[44:45], v[64:65]\n\t" \
"v_pk_fma_f32 v[66:67], v[130:131], v[44:45], v[66:67]\n\t" \
"v_pk_fma_f32 v[68:69], v[132:133], v[44:45], v[68:69]\n\t" \
"v_pk_fma_f32 v[70:71], v[134:135], v[44:45], v[70:71]\n\t" \
"v_cvt_pk_bf16_f32 v136, v64, v65\n\t" \
"v_cvt_pk_bf16_f32 v137, v66, v67\n\t" \
"v_cvt_pk_bf16_f32 v138, v68, v69\n\t" \
"v_cvt_pk_bf16_f32 v139, v70, v71\n\t" \
"s_add_u32 s57, s56, 0\n\t" \
"buffer_store_dwordx4 v[136:139], v36, %0, s57 offen sc1\n\t" \
"v_accvgpr_read_b32 v144, a24\n\t" \
"v_accvgpr_read_b32 v145, a25\n\t" \
"v_accvgpr_read_b32 v146, a26\n\t" \
"v_accvgpr_read_b32 v147, a27\n\t" \
"v_accvgpr_read_b32 v148, a28\n\t" \
"v_accvgpr_read_b32 v149, a29\n\t" \
"v_accvgpr_read_b32 v150, a30\n\t" \
"v_accvgpr_read_b32 v151, a31\n\t" \
"v_pk_mul_f32 v[72:73], v[72:73], v[46:47]\n\t" \
"v_pk_mul_f32 v[74:75], v[74:75], v[46:47]\n\t" \
"v_pk_mul_f32 v[76:77], v[76:77], v[46:47]\n\t" \
"v_pk_mul_f32 v[78:79], v[78:79], v[46:47]\n\t" \
"v_pk_fma_f32 v[72:73], v[144:145], v[44:45], v[72:73]\n\t" \
"v_pk_fma_f32 v[74:75], v[146:147], v[44:45], v[74:75]\n\t" \
"v_pk_fma_f32 v[76:77], v[148:149], v[44:45], v[76:77]\n\t" \
"v_pk_fma_f32 v[78:79], v[150:151], v[44:45], v[78:79]\n\t" \
"v_cvt_pk_bf16_f32 v152, v72, v73\n\t" \
"v_cvt_pk_bf16_f32 v153, v74, v75\n\t" \
"v_cvt_pk_bf16_f32 v154, v76, v77\n\t" \
"v_cvt_pk_bf16_f32 v155, v78, v79\n\t" \
"s_add_u32 s57, s56, 1024\n\t" \
"buffer_store_dwordx4 v[152:155], v36, %0, s57 offen sc1\n\t" \
"v_accvgpr_read_b32 v160, a48\n\t" \
"v_accvgpr_read_b32 v161, a49\n\t" \
"v_accvgpr_read_b32 v162, a50\n\t" \
"v_accvgpr_read_b32 v163, a51\n\t" \
"v_accvgpr_read_b32 v164, a52\n\t" \
"v_accvgpr_read_b32 v165, a53\n\t" \
"v_accvgpr_read_b32 v166, a54\n\t" \
"v_accvgpr_read_b32 v167, a55\n\t" \
"v_pk_mul_f32 v[80:81], v[80:81], v[46:47]\n\t" \
"v_pk_mul_f32 v[82:83], v[82:83], v[46:47]\n\t" \
"s_waitcnt lgkmcnt(10)\n\t" \
"v_pk_mul_f32 v[84:85], v[84:85], v[46:47]\n\t" \
"v_pk_mul_f32 v[86:87], v[86:87], v[46:47]\n\t" \
"v_pk_fma_f32 v[80:81], v[160:161], v[44:45], v[80:81]\n\t" \
"v_pk_fma_f32 v[82:83], v[162:163], v[44:45], v[82:83]\n\t" \
"v_pk_fma_f32 v[84:85], v[164:165], v[44:45], v[84:85]\n\t" \
"v_pk_fma_f32 v[86:87], v[166:167], v[44:45], v[86:87]\n\t" \
"v_cvt_pk_bf16_f32 v168, v80, v81\n\t" \
"v_cvt_pk_bf16_f32 v169, v82, v83\n\t" \
"v_cvt_pk_bf16_f32 v170, v84, v85\n\t" \
"v_cvt_pk_bf16_f32 v171, v86, v87\n\t" \
"s_add_u32 s57, s56, 2048\n\t" \
"buffer_store_dwordx4 v[168:171], v36, %0, s57 offen sc1\n\t" \
"v_accvgpr_read_b32 v176, a56\n\t" \
"v_accvgpr_read_b32 v177, a57\n\t" \
"v_accvgpr_read_b32 v178, a58\n\t" \
"v_accvgpr_read_b32 v179, a59\n\t" \
"v_accvgpr_read_b32 v180, a60\n\t" \
"v_accvgpr_read_b32 v181, a61\n\t" \
"v_accvgpr_read_b32 v182, a62\n\t" \
"v_accvgpr_read_b32 v183, a63\n\t" \
"s_waitcnt lgkmcnt(9)\n\t" \
"v_pk_mul_f32 v[88:89], v[88:89], v[46:47]\n\t" \
"v_pk_mul_f32 v[90:91], v[90:91], v[46:47]\n\t" \
"s_waitcnt lgkmcnt(8)\n\t" \
"v_pk_mul_f32 v[92:93], v[92:93], v[46:47]\n\t" \
"v_pk_mul_f32 v[94:95], v[94:95], v[46:47]\n\t" \
"v_pk_fma_f32 v[88:89], v[176:177], v[44:45], v[88:89]\n\t" \
"v_pk_fma_f32 v[90:91], v[178:179], v[44:45], v[90:91]\n\t" \
"v_pk_fma_f32 v[92:93], v[180:181], v[44:45], v[92:93]\n\t" \
"v_pk_fma_f32 v[94:95], v[182:183], v[44:45], v[94:95]\n\t" \
"v_cvt_pk_bf16_f32 v184, v88, v89\n\t" \
"v_cvt_pk_bf16_f32 v185, v90, v91\n\t" \
"v_cvt_pk_bf16_f32 v186, v92, v93\n\t" \
"v_cvt_pk_bf16_f32 v187, v94, v95\n\t" \
"s_add_u32 s57, s56, 3072\n\t" \
"buffer_store_dwordx4 v[184:187], v36, %0, s57 offen sc1\n\t" \
"v_accvgpr_read_b32 v128, a80\n\t" \
"v_accvgpr_read_b32 v129, a81\n\t" \
"v_accvgpr_read_b32 v130, a82\n\t" \
"v_accvgpr_read_b32 v131, a83\n\t" \
"v_accvgpr_read_b32 v132, a84\n\t" \
"v_accvgpr_read_b32 v133, a85\n\t" \
"v_accvgpr_read_b32 v134, a86\n\t" \
"v_accvgpr_read_b32 v135, a87\n\t" \
"s_waitcnt lgkmcnt(7)\n\t" \
"v_pk_mul_f32 v[96:97], v[96:97], v[46:47]\n\t" \
"v_pk_mul_f32 v[98:99], v[98:99], v[46:47]\n\t" \
"s_waitcnt lgkmcnt(6)\n\t" \
"v_pk_mul_f32 v[100:101], v[100:101], v[46:47]\n\t" \
"v_pk_mul_f32 v[102:103], v[102:103], v[46:47]\n\t" \
"v_pk_fma_f32 v[96:97], v[128:129], v[44:45], v[96:97]\n\t" \
"v_pk_fma_f32 v[98:99], v[130:131], v[44:45], v[98:99]\n\t" \
"v_pk_fma_f32 v[100:101], v[132:133], v[44:45], v[100:101]\n\t" \
"v_pk_fma_f32 v[102:103], v[134:135], v[44:45], v[102:103]\n\t" \
"v_cvt_pk_bf16_f32 v136, v96, v97\n\t" \
"v_cvt_pk_bf16_f32 v137, v98, v99\n\t" \
"v_cvt_pk_bf16_f32 v138, v100, v101\n\t" \
"v_cvt_pk_bf16_f32 v139, v102, v103\n\t" \
"s_add_u32 s57, s56, 4096\n\t" \
"buffer_store_dwordx4 v[136:139], v36, %0, s57 offen sc1\n\t" \
"v_accvgpr_read_b32 v144, a88\n\t" \
"v_accvgpr_read_b32 v145, a89\n\t" \
"v_accvgpr_read_b32 v146, a90\n\t" \
"v_accvgpr_read_b32 v147, a91\n\t" \
"v_accvgpr_read_b32 v148, a92\n\t" \
"v_accvgpr_read_b32 v149, a93\n\t" \
"v_accvgpr_read_b32 v150, a94\n\t" \
"v_accvgpr_read_b32 v151, a95\n\t" \
"s_waitcnt lgkmcnt(5)\n\t" \
"v_pk_mul_f32 v[104:105], v[104:105], v[46:47]\n\t" \
"v_pk_mul_f32 v[106:107], v[106:107], v[46:47]\n\t" \
"s_waitcnt lgkmcnt(4)\n\t" \
"v_pk_mul_f32 v[108:109], v[108:109], v[46:47]\n\t" \
"v_pk_mul_f32 v[110:111], v[110:111], v[46:47]\n\t" \
"v_pk_fma_f32 v[104:105], v[144:145], v[44:45], v[104:105]\n\t" \
"v_pk_fma_f32 v[106:107], v[146:147], v[44:45], v[106:107]\n\t" \
"v_pk_fma_f32 v[108:109], v[148:149], v[44:45], v[108:109]\n\t" \
"v_pk_fma_f32 v[110:111], v[150:151], v[44:45], v[110:111]\n\t" \
"v_cvt_pk_bf16_f32 v152, v104, v105\n\t" \
"v_cvt_pk_bf16_f32 v153, v106, v107\n\t" \
"v_cvt_pk_bf16_f32 v154, v108, v109\n\t" \
"v_cvt_pk_bf16_f32 v155, v110, v111\n\t" \
"s_add_u32 s57, s56, 5120\n\t" \
"buffer_store_dwordx4 v[152:155], v36, %0, s57 offen sc1\n\t" \
"v_accvgpr_read_b32 v160, a112\n\t" \
"v_accvgpr_read_b32 v161, a113\n\t" \
"v_accvgpr_read_b32 v162, a114\n\t" \
"v_accvgpr_read_b32 v163, a115\n\t" \
"v_accvgpr_read_b32 v164, a116\n\t" \
"v_accvgpr_read_b32 v165, a117\n\t" \
"v_accvgpr_read_b32 v166, a118\n\t" \
"v_accvgpr_read_b32 v167, a119\n\t" \
"s_waitcnt lgkmcnt(3)\n\t" \
"v_pk_mul_f32 v[112:113], v[112:113], v[46:47]\n\t" \
"v_pk_mul_f32 v[114:115], v[114:115], v[46:47]\n\t" \
"s_waitcnt lgkmcnt(2)\n\t" \
"v_pk_mul_f32 v[116:117], v[116:117], v[46:47]\n\t" \
"v_pk_mul_f32 v[118:119], v[118:119], v[46:47]\n\t" \
"v_pk_fma_f32 v[112:113], v[160:161], v[44:45], v[112:113]\n\t" \
"v_pk_fma_f32 v[114:115], v[162:163], v[44:45], v[114:115]\n\t" \
"v_pk_fma_f32 v[116:117], v[164:165], v[44:45], v[116:117]\n\t" \
"v_pk_fma_f32 v[118:119], v[166:167], v[44:45], v[118:119]\n\t" \
"v_cvt_pk_bf16_f32 v168, v112, v113\n\t" \
"v_cvt_pk_bf16_f32 v169, v114, v115\n\t" \
"v_cvt_pk_bf16_f32 v170, v116, v117\n\t" \
"v_cvt_pk_bf16_f32 v171, v118, v119\n\t" \
"s_add_u32 s57, s56, 6144\n\t" \
"buffer_store_dwordx4 v[168:171], v36, %0, s57 offen sc1\n\t" \
"v_accvgpr_read_b32 v176, a120\n\t" \
"v_accvgpr_read_b32 v177, a121\n\t" \
"v_accvgpr_read_b32 v178, a122\n\t" \
"v_accvgpr_read_b32 v179, a123\n\t" \
"v_accvgpr_read_b32 v180, a124\n\t" \
"v_accvgpr_read_b32 v181, a125\n\t" \
"v_accvgpr_read_b32 v182, a126\n\t" \
"v_accvgpr_read_b32 v183, a127\n\t" \
"s_waitcnt lgkmcnt(1)\n\t" \
"v_pk_mul_f32 v[120:121], v[120:121], v[46:47]\n\t" \
"v_pk_mul_f32 v[122:123], v[122:123], v[46:47]\n\t" \
"s_waitcnt lgkmcnt(0)\n\t" \
"v_pk_mul_f32 v[124:125], v[124:125], v[46:47]\n\t" \
"v_pk_mul_f32 v[126:127], v[126:127], v[46:47]\n\t" \
"v_pk_fma_f32 v[120:121], v[176:177], v[44:45], v[120:121]\n\t" \
"v_pk_fma_f32 v[122:123], v[178:179], v[44:45], v[122:123]\n\t" \
"v_pk_fma_f32 v[124:125], v[180:181], v[44:45], v[124:125]\n\t" \
"v_pk_fma_f32 v[126:127], v[182:183], v[44:45], v[126:127]\n\t" \
"v_cvt_pk_bf16_f32 v184, v120, v121\n\t" \
"v_cvt_pk_bf16_f32 v185, v122, v123\n\t" \
"v_cvt_pk_bf16_f32 v186, v124, v125\n\t" \
"v_cvt_pk_bf16_f32 v187, v126, v127\n\t" \
"s_add_u32 s57, s56, 7168\n\t" \
"buffer_store_dwordx4 v[184:187], v36, %0, s57 offen sc1\n\t" \
"EPIEND_%=:\n\t" \
"s_add_u32 s52, s52, 1\n\t" \
"s_cmp_eq_u32 s52, 2\n\t" \
"s_cbranch_scc1 DONE_%=\n\t" \
"s_mov_b32 s40, %6\n\t" \
"s_mov_b32 s58, 0x100000\n\t" \
"s_waitcnt vmcnt(8)\n\t" \
"POLL_%=:\n\t" \
"s_waitcnt vmcnt(8)\n\t" \
"v_readfirstlane_b32 s57, v60\n\t" \
"s_nop 1\n\t" \
"s_cmp_ge_u32 s57, 8\n\t" \
"s_cbranch_scc1 POLLED_%=\n\t" \
"s_sub_u32 s58, s58, 1\n\t" \
"s_cmp_eq_u32 s58, 0\n\t" \
"s_cbranch_scc1 POLLED_%=\n\t" \
"s_sleep 2\n\t" \
"v_mov_b32 v60, 0\n\t" \
"buffer_load_dword v60, v60, %0, s79 offen sc1\n\t" \
"s_waitcnt vmcnt(0)\n\t" \
"s_branch POLL_%=\n\t" \
"POLLED_%=:\n\t" \
"s_add_u32 s57, %9, 0\n\t" \
"buffer_load_dwordx4 v[128:131], v36, %0, s57 offen sc1\n\t" \
"buffer_load_dwordx4 v[132:135], v36, %0, s57 offen offset:1024 sc1\n\t" \
"buffer_load_dwordx4 v[136:139], v36, %0, s57 offen offset:2048 sc1\n\t" \
"buffer_load_dwordx4 v[140:143], v36, %0, s57 offen offset:3072 sc1\n\t" \
"s_add_u32 s57, %9, 4096\n\t" \
"buffer_load_dwordx4 v[144:147], v36, %0, s57 offen sc1\n\t" \
"buffer_load_dwordx4 v[148:151], v36, %0, s57 offen offset:1024 sc1\n\t" \
"buffer_load_dwordx4 v[152:155], v36, %0, s57 offen offset:2048 sc1\n\t" \
"buffer_load_dwordx4 v[156:159], v36, %0, s57 offen offset:3072 sc1\n\t" \
"v_lshrrev_b32 v61, 2, v36\n\t" \
"v_and_b32 v61, 0x7c, v61\n\t" \
"buffer_load_dwordx4 v[160:163], v36, %0, %10 offen sc1\n\t" \
"buffer_load_dword v168, v61, %0, %11 offen\n\t" \
"buffer_load_dwordx4 v[164:167], v36, %0, %10 offen offset:1024 sc1\n\t" \
"buffer_load_dword v169, v61, %0, %11 offen offset:128\n\t" \
"s_lshl_b32 s58, s53, 9\n\t" \
"s_add_u32 s58, s58, %4\n\t" \
"v_lshrrev_b32 v38, 2, v36\n\t" \
"v_and_b32 v38, 0x7c, v38\n\t" \
"buffer_load_dword v38, v38, %0, s58 offen\n\t" \
"s_barrier\n\t" \
"s_branch PASS_%=\n\t" \
"RARE0_f_%=:\n\t" \
"s_nop 1\n\t" \
"v_max_f32 v24, s51, v20\n\t" \
"v_add_f32 v32, v32, v24\n\t" \
"v_sub_f32 v34, 0, v24\n\t" \
"v_min_f32 v34, 0, v34\n\t" \
"v_exp_f32 v34, v34\n\t" \
"v_sub_f32 v64, v64, v24\n\t" \
"v_sub_f32 v65, v65, v24\n\t" \
"v_sub_f32 v66, v66, v24\n\t" \
"v_sub_f32 v67, v67, v24\n\t" \
"v_sub_f32 v68, v68, v24\n\t" \
"v_sub_f32 v69, v69, v24\n\t" \
"v_sub_f32 v70, v70, v24\n\t" \
"v_sub_f32 v71, v71, v24\n\t" \
"v_sub_f32 v72, v72, v24\n\t" \
"v_sub_f32 v73, v73, v24\n\t" \
"v_sub_f32 v74, v74, v24\n\t" \
"v_sub_f32 v75, v75, v24\n\t" \
"v_sub_f32 v76, v76, v24\n\t" \
"v_sub_f32 v77, v77, v24\n\t" \
"v_sub_f32 v78, v78, v24\n\t" \
"v_sub_f32 v79, v79, v24\n\t" \
"v_sub_f32 v96, v96, v24\n\t" \
"v_sub_f32 v97, v97, v24\n\t" \
"v_sub_f32 v98, v98, v24\n\t" \
"v_sub_f32 v99, v99, v24\n\t" \
"v_sub_f32 v100, v100, v24\n\t" \
"v_sub_f32 v101, v101, v24\n\t" \
"v_sub_f32 v102, v102, v24\n\t" \
"v_sub_f32 v103, v103, v24\n\t" \
"v_sub_f32 v104, v104, v24\n\t" \
"v_sub_f32 v105, v105, v24\n\t" \
"v_sub_f32 v106, v106, v24\n\t" \
"v_sub_f32 v107, v107, v24\n\t" \
"v_sub_f32 v108, v108, v24\n\t" \
"v_sub_f32 v109, v109, v24\n\t" \
"v_sub_f32 v110, v110, v24\n\t" \
"v_sub_f32 v111, v111, v24\n\t" \
"v_sub_f32 v26, 0, v32\n\t" \
"v_mov_b32 v224, v26\n\t" \
"v_mov_b32 v225, v26\n\t" \
"v_mov_b32 v226, v26\n\t" \
"v_mov_b32 v227, v26\n\t" \
"v_mov_b32 v228, v26\n\t" \
"v_mov_b32 v229, v26\n\t" \
"v_mov_b32 v230, v26\n\t" \
"v_mov_b32 v231, v26\n\t" \
"v_mov_b32 v232, v26\n\t" \
"v_mov_b32 v233, v26\n\t" \
"v_mov_b32 v234, v26\n\t" \
"v_mov_b32 v235, v26\n\t" \
"v_mov_b32 v236, v26\n\t" \
"v_mov_b32 v237, v26\n\t" \
"v_mov_b32 v238, v26\n\t" \
"v_mov_b32 v239, v26\n\t" \
"v_mul_f32 v28, v28, v34\n\t" \
"v_mul_f32 v29, v29, v34\n\t" \
"v_max_f32 v25, s51, v21\n\t" \
"v_add_f32 v33, v33, v25\n\t" \
"v_sub_f32 v35, 0, v25\n\t" \
"v_min_f32 v35, 0, v35\n\t" \
"v_exp_f32 v35, v35\n\t" \
"v_sub_f32 v80, v80, v25\n\t" \
"v_sub_f32 v81, v81, v25\n\t" \
"v_sub_f32 v82, v82, v25\n\t" \
"v_sub_f32 v83, v83, v25\n\t" \
"v_sub_f32 v84, v84, v25\n\t" \
"v_sub_f32 v85, v85, v25\n\t" \
"v_sub_f32 v86, v86, v25\n\t" \
"v_sub_f32 v87, v87, v25\n\t" \
"v_sub_f32 v88, v88, v25\n\t" \
"v_sub_f32 v89, v89, v25\n\t" \
"v_sub_f32 v90, v90, v25\n\t" \
"v_sub_f32 v91, v91, v25\n\t" \
"v_sub_f32 v92, v92, v25\n\t" \
"v_sub_f32 v93, v93, v25\n\t" \
"v_sub_f32 v94, v94, v25\n\t" \
"v_sub_f32 v95, v95, v25\n\t" \
"v_sub_f32 v112, v112, v25\n\t" \
"v_sub_f32 v113, v113, v25\n\t" \
"v_sub_f32 v114, v114, v25\n\t" \
"v_sub_f32 v115, v115, v25\n\t" \
"v_sub_f32 v116, v116, v25\n\t" \
"v_sub_f32 v117, v117, v25\n\t" \
"v_sub_f32 v118, v118, v25\n\t" \
"v_sub_f32 v119, v119, v25\n\t" \
"v_sub_f32 v120, v120, v25\n\t" \
"v_sub_f32 v121, v121, v25\n\t" \
"v_sub_f32 v122, v122, v25\n\t" \
"v_sub_f32 v123, v123, v25\n\t" \
"v_sub_f32 v124, v124, v25\n\t" \
"v_sub_f32 v125, v125, v25\n\t" \
"v_sub_f32 v126, v126, v25\n\t" \
"v_sub_f32 v127, v127, v25\n\t" \
"v_sub_f32 v26, 0, v33\n\t" \
"v_mov_b32 v240, v26\n\t" \
"v_mov_b32 v241, v26\n\t" \
"v_mov_b32 v242, v26\n\t" \
"v_mov_b32 v243, v26\n\t" \
"v_mov_b32 v244, v26\n\t" \
"v_mov_b32 v245, v26\n\t" \
"v_mov_b32 v246, v26\n\t" \
"v_mov_b32 v247, v26\n\t" \
"v_mov_b32 v248, v26\n\t" \
"v_mov_b32 v249, v26\n\t" \
"v_mov_b32 v250, v26\n\t" \
"v_mov_b32 v251, v26\n\t" \
"v_mov_b32 v252, v26\n\t" \
"v_mov_b32 v253, v26\n\t" \
"v_mov_b32 v254, v26\n\t" \
"v_mov_b32 v255, v26\n\t" \
"v_mul_f32 v30, v30, v35\n\t" \
"v_mul_f32 v31, v31, v35\n\t" \
"s_nop 1\n\t" \
"s_branch BACK0_f_%=\n\t" \
"RARE1_p1_%=:\n\t" \
"s_nop 1\n\t" \
"v_max_f32 v24, s51, v20\n\t" \
"v_add_f32 v32, v32, v24\n\t" \
"v_sub_f32 v34, 0, v24\n\t" \
"v_min_f32 v34, 0, v34\n\t" \
"v_exp_f32 v34, v34\n\t" \
"v_sub_f32 v128, v128, v24\n\t" \
"v_sub_f32 v129, v129, v24\n\t" \
"v_sub_f32 v130, v130, v24\n\t" \
"v_sub_f32 v131, v131, v24\n\t" \
"v_sub_f32 v132, v132, v24\n\t" \
"v_sub_f32 v133, v133, v24\n\t" \
"v_sub_f32 v134, v134, v24\n\t" \
"v_sub_f32 v135, v135, v24\n\t" \
"v_sub_f32 v136, v136, v24\n\t" \
"v_sub_f32 v137, v137, v24\n\t" \
"v_sub_f32 v138, v138, v24\n\t" \
"v_sub_f32 v139, v139, v24\n\t" \
"v_sub_f32 v140, v140, v24\n\t" \
"v_sub_f32 v141, v141, v24\n\t" \
"v_sub_f32 v142, v142, v24\n\t" \
"v_sub_f32 v143, v143, v24\n\t" \
"v_sub_f32 v160, v160, v24\n\t" \
"v_sub_f32 v161, v161, v24\n\t" \
"v_sub_f32 v162, v162, v24\n\t" \
"v_sub_f32 v163, v163, v24\n\t" \
"v_sub_f32 v164, v164, v24\n\t" \
"v_sub_f32 v165, v165, v24\n\t" \
"v_sub_f32 v166, v166, v24\n\t" \
"v_sub_f32 v167, v167, v24\n\t" \
"v_sub_f32 v168, v168, v24\n\t" \
"v_sub_f32 v169, v169, v24\n\t" \
"v_sub_f32 v170, v170, v24\n\t" \
"v_sub_f32 v171, v171, v24\n\t" \
"v_sub_f32 v172, v172, v24\n\t" \
"v_sub_f32 v173, v173, v24\n\t" \
"v_sub_f32 v174, v174, v24\n\t" \
"v_sub_f32 v175, v175, v24\n\t" \
"v_sub_f32 v26, 0, v32\n\t" \
"v_mov_b32 v224, v26\n\t" \
"v_mov_b32 v225, v26\n\t" \
"v_mov_b32 v226, v26\n\t" \
"v_mov_b32 v227, v26\n\t" \
"v_mov_b32 v228, v26\n\t" \
"v_mov_b32 v229, v26\n\t" \
"v_mov_b32 v230, v26\n\t" \
"v_mov_b32 v231, v26\n\t" \
"v_mov_b32 v232, v26\n\t" \
"v_mov_b32 v233, v26\n\t" \
"v_mov_b32 v234, v26\n\t" \
"v_mov_b32 v235, v26\n\t" \
"v_mov_b32 v236, v26\n\t" \
"v_mov_b32 v237, v26\n\t" \
"v_mov_b32 v238, v26\n\t" \
"v_mov_b32 v239, v26\n\t" \
"v_mul_f32 v28, v28, v34\n\t" \
"v_mul_f32 v29, v29, v34\n\t" \
"v_max_f32 v25, s51, v21\n\t" \
"v_add_f32 v33, v33, v25\n\t" \
"v_sub_f32 v35, 0, v25\n\t" \
"v_min_f32 v35, 0, v35\n\t" \
"v_exp_f32 v35, v35\n\t" \
"v_sub_f32 v144, v144, v25\n\t" \
"v_sub_f32 v145, v145, v25\n\t" \
"v_sub_f32 v146, v146, v25\n\t" \
"v_sub_f32 v147, v147, v25\n\t" \
"v_sub_f32 v148, v148, v25\n\t" \
"v_sub_f32 v149, v149, v25\n\t" \
"v_sub_f32 v150, v150, v25\n\t" \
"v_sub_f32 v151, v151, v25\n\t" \
"v_sub_f32 v152, v152, v25\n\t" \
"v_sub_f32 v153, v153, v25\n\t" \
"v_sub_f32 v154, v154, v25\n\t" \
"v_sub_f32 v155, v155, v25\n\t" \
"v_sub_f32 v156, v156, v25\n\t" \
"v_sub_f32 v157, v157, v25\n\t" \
"v_sub_f32 v158, v158, v25\n\t" \
"v_sub_f32 v159, v159, v25\n\t" \
"v_sub_f32 v176, v176, v25\n\t" \
"v_sub_f32 v177, v177, v25\n\t" \
"v_sub_f32 v178, v178, v25\n\t" \
"v_sub_f32 v179, v179, v25\n\t" \
"v_sub_f32 v180, v180, v25\n\t" \
"v_sub_f32 v181, v181, v25\n\t" \
"v_sub_f32 v182, v182, v25\n\t" \
"v_sub_f32 v183, v183, v25\n\t" \
"v_sub_f32 v184, v184, v25\n\t" \
"v_sub_f32 v185, v185, v25\n\t" \
"v_sub_f32 v186, v186, v25\n\t" \
"v_sub_f32 v187, v187, v25\n\t" \
"v_sub_f32 v188, v188, v25\n\t" \
"v_sub_f32 v189, v189, v25\n\t" \
"v_sub_f32 v190, v190, v25\n\t" \
"v_sub_f32 v191, v191, v25\n\t" \
"v_sub_f32 v26, 0, v33\n\t" \
"v_mov_b32 v240, v26\n\t" \
"v_mov_b32 v241, v26\n\t" \
"v_mov_b32 v242, v26\n\t" \
"v_mov_b32 v243, v26\n\t" \
"v_mov_b32 v244, v26\n\t" \
"v_mov_b32 v245, v26\n\t" \
"v_mov_b32 v246, v26\n\t" \
"v_mov_b32 v247, v26\n\t" \
"v_mov_b32 v248, v26\n\t" \
"v_mov_b32 v249, v26\n\t" \
"v_mov_b32 v250, v26\n\t" \
"v_mov_b32 v251, v26\n\t" \
"v_mov_b32 v252, v26\n\t" \
"v_mov_b32 v253, v26\n\t" \
"v_mov_b32 v254, v26\n\t" \
"v_mov_b32 v255, v26\n\t" \
"v_mul_f32 v30, v30, v35\n\t" \
"v_mul_f32 v31, v31, v35\n\t" \
"s_mov_b32 s50, 1\n\t" \
"s_nop 1\n\t" \
"s_branch BACK1_p1_%=\n\t" \
"RARE0_l0_%=:\n\t" \
"s_nop 1\n\t" \
"v_max_f32 v24, s51, v20\n\t" \
"v_add_f32 v32, v32, v24\n\t" \
"v_sub_f32 v34, 0, v24\n\t" \
"v_min_f32 v34, 0, v34\n\t" \
"v_exp_f32 v34, v34\n\t" \
"v_sub_f32 v64, v64, v24\n\t" \
"v_sub_f32 v65, v65, v24\n\t" \
"v_sub_f32 v66, v66, v24\n\t" \
"v_sub_f32 v67, v67, v24\n\t" \
"v_sub_f32 v68, v68, v24\n\t" \
"v_sub_f32 v69, v69, v24\n\t" \
"v_sub_f32 v70, v70, v24\n\t" \
"v_sub_f32 v71, v71, v24\n\t" \
"v_sub_f32 v72, v72, v24\n\t" \
"v_sub_f32 v73, v73, v24\n\t" \
"v_sub_f32 v74, v74, v24\n\t" \
"v_sub_f32 v75, v75, v24\n\t" \
"v_sub_f32 v76, v76, v24\n\t" \
"v_sub_f32 v77, v77, v24\n\t" \
"v_sub_f32 v78, v78, v24\n\t" \
"v_sub_f32 v79, v79, v24\n\t" \
"v_sub_f32 v96, v96, v24\n\t" \
"v_sub_f32 v97, v97, v24\n\t" \
"v_sub_f32 v98, v98, v24\n\t" \
"v_sub_f32 v99, v99, v24\n\t" \
"v_sub_f32 v100, v100, v24\n\t" \
"v_sub_f32 v101, v101, v24\n\t" \
"v_sub_f32 v102, v102, v24\n\t" \
"v_sub_f32 v103, v103, v24\n\t" \
"v_sub_f32 v104, v104, v24\n\t" \
"v_sub_f32 v105, v105, v24\n\t" \
"v_sub_f32 v106, v106, v24\n\t" \
"v_sub_f32 v107, v107, v24\n\t" \
"v_sub_f32 v108, v108, v24\n\t" \
"v_sub_f32 v109, v109, v24\n\t" \
"v_sub_f32 v110, v110, v24\n\t" \
"v_sub_f32 v111, v111, v24\n\t" \
"v_sub_f32 v26, 0, v32\n\t" \
"v_mov_b32 v224, v26\n\t" \
"v_mov_b32 v225, v26\n\t" \
"v_mov_b32 v226, v26\n\t" \
"v_mov_b32 v227, v26\n\t" \
"v_mov_b32 v228, v26\n\t" \
"v_mov_b32 v229, v26\n\t" \
"v_mov_b32 v230, v26\n\t" \
"v_mov_b32 v231, v26\n\t" \
"v_mov_b32 v232, v26\n\t" \
"v_mov_b32 v233, v26\n\t" \
"v_mov_b32 v234, v26\n\t" \
"v_mov_b32 v235, v26\n\t" \
"v_mov_b32 v236, v26\n\t" \
"v_mov_b32 v237, v26\n\t" \
"v_mov_b32 v238, v26\n\t" \
"v_mov_b32 v239, v26\n\t" \
"v_mul_f32 v28, v28, v34\n\t" \
"v_mul_f32 v29, v29, v34\n\t" \
"v_max_f32 v25, s51, v21\n\t" \
"v_add_f32 v33, v33, v25\n\t" \
"v_sub_f32 v35, 0, v25\n\t" \
"v_min_f32 v35, 0, v35\n\t" \
"v_exp_f32 v35, v35\n\t" \
"v_sub_f32 v80, v80, v25\n\t" \
"v_sub_f32 v81, v81, v25\n\t" \
"v_sub_f32 v82, v82, v25\n\t" \
"v_sub_f32 v83, v83, v25\n\t" \
"v_sub_f32 v84, v84, v25\n\t" \
"v_sub_f32 v85, v85, v25\n\t" \
"v_sub_f32 v86, v86, v25\n\t" \
"v_sub_f32 v87, v87, v25\n\t" \
"v_sub_f32 v88, v88, v25\n\t" \
"v_sub_f32 v89, v89, v25\n\t" \
"v_sub_f32 v90, v90, v25\n\t" \
"v_sub_f32 v91, v91, v25\n\t" \
"v_sub_f32 v92, v92, v25\n\t" \
"v_sub_f32 v93, v93, v25\n\t" \
"v_sub_f32 v94, v94, v25\n\t" \
"v_sub_f32 v95, v95, v25\n\t" \
"v_sub_f32 v112, v112, v25\n\t" \
"v_sub_f32 v113, v113, v25\n\t" \
"v_sub_f32 v114, v114, v25\n\t" \
"v_sub_f32 v115, v115, v25\n\t" \
"v_sub_f32 v116, v116, v25\n\t" \
"v_sub_f32 v117, v117, v25\n\t" \
"v_sub_f32 v118, v118, v25\n\t" \
"v_sub_f32 v119, v119, v25\n\t" \
"v_sub_f32 v120, v120, v25\n\t" \
"v_sub_f32 v121, v121, v25\n\t" \
"v_sub_f32 v122, v122, v25\n\t" \
"v_sub_f32 v123, v123, v25\n\t" \
"v_sub_f32 v124, v124, v25\n\t" \
"v_sub_f32 v125, v125, v25\n\t" \
"v_sub_f32 v126, v126, v25\n\t" \
"v_sub_f32 v127, v127, v25\n\t" \
"v_sub_f32 v26, 0, v33\n\t" \
"v_mov_b32 v240, v26\n\t" \
"v_mov_b32 v241, v26\n\t" \
"v_mov_b32 v242, v26\n\t" \
"v_mov_b32 v243, v26\n\t" \
"v_mov_b32 v244, v26\n\t" \
"v_mov_b32 v245, v26\n\t" \
"v_mov_b32 v246, v26\n\t" \
"v_mov_b32 v247, v26\n\t" \
"v_mov_b32 v248, v26\n\t" \
"v_mov_b32 v249, v26\n\t" \
"v_mov_b32 v250, v26\n\t" \
"v_mov_b32 v251, v26\n\t" \
"v_mov_b32 v252, v26\n\t" \
"v_mov_b32 v253, v26\n\t" \
"v_mov_b32 v254, v26\n\t" \
"v_mov_b32 v255, v26\n\t" \
"v_mul_f32 v30, v30, v35\n\t" \
"v_mul_f32 v31, v31, v35\n\t" \
"s_mov_b32 s50, 1\n\t" \
"s_nop 1\n\t" \
"s_branch BACK0_l0_%=\n\t" \
"RARE1_l1_%=:\n\t" \
"s_nop 1\n\t" \
"v_max_f32 v24, s51, v20\n\t" \
"v_add_f32 v32, v32, v24\n\t" \
"v_sub_f32 v34, 0, v24\n\t" \
"v_min_f32 v34, 0, v34\n\t" \
"v_exp_f32 v34, v34\n\t" \
"v_sub_f32 v128, v128, v24\n\t" \
"v_sub_f32 v129, v129, v24\n\t" \
"v_sub_f32 v130, v130, v24\n\t" \
"v_sub_f32 v131, v131, v24\n\t" \
"v_sub_f32 v132, v132, v24\n\t" \
"v_sub_f32 v133, v133, v24\n\t" \
"v_sub_f32 v134, v134, v24\n\t" \
"v_sub_f32 v135, v135, v24\n\t" \
"v_sub_f32 v136, v136, v24\n\t" \
"v_sub_f32 v137, v137, v24\n\t" \
"v_sub_f32 v138, v138, v24\n\t" \
"v_sub_f32 v139, v139, v24\n\t" \
"v_sub_f32 v140, v140, v24\n\t" \
"v_sub_f32 v141, v141, v24\n\t" \
"v_sub_f32 v142, v142, v24\n\t" \
"v_sub_f32 v143, v143, v24\n\t" \
"v_sub_f32 v160, v160, v24\n\t" \
"v_sub_f32 v161, v161, v24\n\t" \
"v_sub_f32 v162, v162, v24\n\t" \
"v_sub_f32 v163, v163, v24\n\t" \
"v_sub_f32 v164, v164, v24\n\t" \
"v_sub_f32 v165, v165, v24\n\t" \
"v_sub_f32 v166, v166, v24\n\t" \
"v_sub_f32 v167, v167, v24\n\t" \
"v_sub_f32 v168, v168, v24\n\t" \
"v_sub_f32 v169, v169, v24\n\t" \
"v_sub_f32 v170, v170, v24\n\t" \
"v_sub_f32 v171, v171, v24\n\t" \
"v_sub_f32 v172, v172, v24\n\t" \
"v_sub_f32 v173, v173, v24\n\t" \
"v_sub_f32 v174, v174, v24\n\t" \
"v_sub_f32 v175, v175, v24\n\t" \
"v_sub_f32 v26, 0, v32\n\t" \
"v_mov_b32 v224, v26\n\t" \
"v_mov_b32 v225, v26\n\t" \
"v_mov_b32 v226, v26\n\t" \
"v_mov_b32 v227, v26\n\t" \
"v_mov_b32 v228, v26\n\t" \
"v_mov_b32 v229, v26\n\t" \
"v_mov_b32 v230, v26\n\t" \
"v_mov_b32 v231, v26\n\t" \
"v_mov_b32 v232, v26\n\t" \
"v_mov_b32 v233, v26\n\t" \
"v_mov_b32 v234, v26\n\t" \
"v_mov_b32 v235, v26\n\t" \
"v_mov_b32 v236, v26\n\t" \
"v_mov_b32 v237, v26\n\t" \
"v_mov_b32 v238, v26\n\t" \
"v_mov_b32 v239, v26\n\t" \
"v_mul_f32 v28, v28, v34\n\t" \
"v_mul_f32 v29, v29, v34\n\t" \
"v_max_f32 v25, s51, v21\n\t" \
"v_add_f32 v33, v33, v25\n\t" \
"v_sub_f32 v35, 0, v25\n\t" \
"v_min_f32 v35, 0, v35\n\t" \
"v_exp_f32 v35, v35\n\t" \
"v_sub_f32 v144, v144, v25\n\t" \
"v_sub_f32 v145, v145, v25\n\t" \
"v_sub_f32 v146, v146, v25\n\t" \
"v_sub_f32 v147, v147, v25\n\t" \
"v_sub_f32 v148, v148, v25\n\t" \
"v_sub_f32 v149, v149, v25\n\t" \
"v_sub_f32 v150, v150, v25\n\t" \
"v_sub_f32 v151, v151, v25\n\t" \
"v_sub_f32 v152, v152, v25\n\t" \
"v_sub_f32 v153, v153, v25\n\t" \
"v_sub_f32 v154, v154, v25\n\t" \
"v_sub_f32 v155, v155, v25\n\t" \
"v_sub_f32 v156, v156, v25\n\t" \
"v_sub_f32 v157, v157, v25\n\t" \
"v_sub_f32 v158, v158, v25\n\t" \
"v_sub_f32 v159, v159, v25\n\t" \
"v_sub_f32 v176, v176, v25\n\t" \
"v_sub_f32 v177, v177, v25\n\t" \
"v_sub_f32 v178, v178, v25\n\t" \
"v_sub_f32 v179, v179, v25\n\t" \
"v_sub_f32 v180, v180, v25\n\t" \
"v_sub_f32 v181, v181, v25\n\t" \
"v_sub_f32 v182, v182, v25\n\t" \
"v_sub_f32 v183, v183, v25\n\t" \
"v_sub_f32 v184, v184, v25\n\t" \
"v_sub_f32 v185, v185, v25\n\t" \
"v_sub_f32 v186, v186, v25\n\t" \
"v_sub_f32 v187, v187, v25\n\t" \
"v_sub_f32 v188, v188, v25\n\t" \
"v_sub_f32 v189, v189, v25\n\t" \
"v_sub_f32 v190, v190, v25\n\t" \
"v_sub_f32 v191, v191, v25\n\t" \
"v_sub_f32 v26, 0, v33\n\t" \
"v_mov_b32 v240, v26\n\t" \
"v_mov_b32 v241, v26\n\t" \
"v_mov_b32 v242, v26\n\t" \
"v_mov_b32 v243, v26\n\t" \
"v_mov_b32 v244, v26\n\t" \
"v_mov_b32 v245, v26\n\t" \
"v_mov_b32 v246, v26\n\t" \
"v_mov_b32 v247, v26\n\t" \
"v_mov_b32 v248, v26\n\t" \
"v_mov_b32 v249, v26\n\t" \
"v_mov_b32 v250, v26\n\t" \
"v_mov_b32 v251, v26\n\t" \
"v_mov_b32 v252, v26\n\t" \
"v_mov_b32 v253, v26\n\t" \
"v_mov_b32 v254, v26\n\t" \
"v_mov_b32 v255, v26\n\t" \
"v_mul_f32 v30, v30, v35\n\t" \
"v_mul_f32 v31, v31, v35\n\t" \
"s_mov_b32 s50, 1\n\t" \
"s_nop 1\n\t" \
"s_branch BACK1_l1_%=\n\t" \
"RESCALL_%=:\n\t" \
"RESC_p1_%=:\n\t" \
"s_nop 15\n\t" \
"v_accvgpr_read_b32 v40, a0\n\t" \
"v_mul_f32 v40, v40, v34\n\t" \
"v_accvgpr_write_b32 a0, v40\n\t" \
"v_accvgpr_read_b32 v41, a1\n\t" \
"v_mul_f32 v41, v41, v34\n\t" \
"v_accvgpr_write_b32 a1, v41\n\t" \
"v_accvgpr_read_b32 v42, a2\n\t" \
"v_mul_f32 v42, v42, v34\n\t" \
"v_accvgpr_write_b32 a2, v42\n\t" \
"v_accvgpr_read_b32 v43, a3\n\t" \
"v_mul_f32 v43, v43, v34\n\t" \
"v_accvgpr_write_b32 a3, v43\n\t" \
"v_accvgpr_read_b32 v44, a4\n\t" \
"v_mul_f32 v44, v44, v34\n\t" \
"v_accvgpr_write_b32 a4, v44\n\t" \
"v_accvgpr_read_b32 v45, a5\n\t" \
"v_mul_f32 v45, v45, v34\n\t" \
"v_accvgpr_write_b32 a5, v45\n\t" \
"v_accvgpr_read_b32 v46, a6\n\t" \
"v_mul_f32 v46, v46, v34\n\t" \
"v_accvgpr_write_b32 a6, v46\n\t" \
"v_accvgpr_read_b32 v47, a7\n\t" \
"v_mul_f32 v47, v47, v34\n\t" \
"v_accvgpr_write_b32 a7, v47\n\t" \
"v_accvgpr_read_b32 v40, a8\n\t" \
"v_mul_f32 v40, v40, v34\n\t" \
"v_accvgpr_write_b32 a8, v40\n\t" \
"v_accvgpr_read_b32 v41, a9\n\t" \
"v_mul_f32 v41, v41, v34\n\t" \
"v_accvgpr_write_b32 a9, v41\n\t" \
"v_accvgpr_read_b32 v42, a10\n\t" \
"v_mul_f32 v42, v42, v34\n\t" \
"v_accvgpr_write_b32 a10, v42\n\t" \
"v_accvgpr_read_b32 v43, a11\n\t" \
"v_mul_f32 v43, v43, v34\n\t" \
"v_accvgpr_write_b32 a11, v43\n\t" \
"v_accvgpr_read_b32 v44, a12\n\t" \
"v_mul_f32 v44, v44, v34\n\t" \
"v_accvgpr_write_b32 a12, v44\n\t" \
"v_accvgpr_read_b32 v45, a13\n\t" \
"v_mul_f32 v45, v45, v34\n\t" \
"v_accvgpr_write_b32 a13, v45\n\t" \
"v_accvgpr_read_b32 v46, a14\n\t" \
"v_mul_f32 v46, v46, v34\n\t" \
"v_accvgpr_write_b32 a14, v46\n\t" \
"v_accvgpr_read_b32 v47, a15\n\t" \
"v_mul_f32 v47, v47, v34\n\t" \
"v_accvgpr_write_b32 a15, v47\n\t" \
"v_accvgpr_read_b32 v40, a16\n\t" \
"v_mul_f32 v40, v40, v35\n\t" \
"v_accvgpr_write_b32 a16, v40\n\t" \
"v_accvgpr_read_b32 v41, a17\n\t" \
"v_mul_f32 v41, v41, v35\n\t" \
"v_accvgpr_write_b32 a17, v41\n\t" \
"v_accvgpr_read_b32 v42, a18\n\t" \
"v_mul_f32 v42, v42, v35\n\t" \
"v_accvgpr_write_b32 a18, v42\n\t" \
"v_accvgpr_read_b32 v43, a19\n\t" \
"v_mul_f32 v43, v43, v35\n\t" \
"v_accvgpr_write_b32 a19, v43\n\t" \
"v_accvgpr_read_b32 v44, a20\n\t" \
"v_mul_f32 v44, v44, v35\n\t" \
"v_accvgpr_write_b32 a20, v44\n\t" \
"v_accvgpr_read_b32 v45, a21\n\t" \
"v_mul_f32 v45, v45, v35\n\t" \
"v_accvgpr_write_b32 a21, v45\n\t" \
"v_accvgpr_read_b32 v46, a22\n\t" \
"v_mul_f32 v46, v46, v35\n\t" \
"v_accvgpr_write_b32 a22, v46\n\t" \
"v_accvgpr_read_b32 v47, a23\n\t" \
"v_mul_f32 v47, v47, v35\n\t" \
"v_accvgpr_write_b32 a23, v47\n\t" \
"v_accvgpr_read_b32 v40, a24\n\t" \
"v_mul_f32 v40, v40, v35\n\t" \
"v_accvgpr_write_b32 a24, v40\n\t" \
"v_accvgpr_read_b32 v41, a25\n\t" \
"v_mul_f32 v41, v41, v35\n\t" \
"v_accvgpr_write_b32 a25, v41\n\t" \
"v_accvgpr_read_b32 v42, a26\n\t" \
"v_mul_f32 v42, v42, v35\n\t" \
"v_accvgpr_write_b32 a26, v42\n\t" \
"v_accvgpr_read_b32 v43, a27\n\t" \
"v_mul_f32 v43, v43, v35\n\t" \
"v_accvgpr_write_b32 a27, v43\n\t" \
"v_accvgpr_read_b32 v44, a28\n\t" \
"v_mul_f32 v44, v44, v35\n\t" \
"v_accvgpr_write_b32 a28, v44\n\t" \
"v_accvgpr_read_b32 v45, a29\n\t" \
"v_mul_f32 v45, v45, v35\n\t" \
"v_accvgpr_write_b32 a29, v45\n\t" \
"v_accvgpr_read_b32 v46, a30\n\t" \
"v_mul_f32 v46, v46, v35\n\t" \
"v_accvgpr_write_b32 a30, v46\n\t" \
"v_accvgpr_read_b32 v47, a31\n\t" \
"v_mul_f32 v47, v47, v35\n\t" \
"v_accvgpr_write_b32 a31, v47\n\t" \
"v_accvgpr_read_b32 v40, a32\n\t" \
"v_mul_f32 v40, v40, v34\n\t" \
"v_accvgpr_write_b32 a32, v40\n\t" \
"v_accvgpr_read_b32 v41, a33\n\t" \
"v_mul_f32 v41, v41, v34\n\t" \
"v_accvgpr_write_b32 a33, v41\n\t" \
"v_accvgpr_read_b32 v42, a34\n\t" \
"v_mul_f32 v42, v42, v34\n\t" \
"v_accvgpr_write_b32 a34, v42\n\t" \
"v_accvgpr_read_b32 v43, a35\n\t" \
"v_mul_f32 v43, v43, v34\n\t" \
"v_accvgpr_write_b32 a35, v43\n\t" \
"v_accvgpr_read_b32 v44, a36\n\t" \
"v_mul_f32 v44, v44, v34\n\t" \
"v_accvgpr_write_b32 a36, v44\n\t" \
"v_accvgpr_read_b32 v45, a37\n\t" \
"v_mul_f32 v45, v45, v34\n\t" \
"v_accvgpr_write_b32 a37, v45\n\t" \
"v_accvgpr_read_b32 v46, a38\n\t" \
"v_mul_f32 v46, v46, v34\n\t" \
"v_accvgpr_write_b32 a38, v46\n\t" \
"v_accvgpr_read_b32 v47, a39\n\t" \
"v_mul_f32 v47, v47, v34\n\t" \
"v_accvgpr_write_b32 a39, v47\n\t" \
"v_accvgpr_read_b32 v40, a40\n\t" \
"v_mul_f32 v40, v40, v34\n\t" \
"v_accvgpr_write_b32 a40, v40\n\t" \
"v_accvgpr_read_b32 v41, a41\n\t" \
"v_mul_f32 v41, v41, v34\n\t" \
"v_accvgpr_write_b32 a41, v41\n\t" \
"v_accvgpr_read_b32 v42, a42\n\t" \
"v_mul_f32 v42, v42, v34\n\t" \
"v_accvgpr_write_b32 a42, v42\n\t" \
"v_accvgpr_read_b32 v43, a43\n\t" \
"v_mul_f32 v43, v43, v34\n\t" \
"v_accvgpr_write_b32 a43, v43\n\t" \
"v_accvgpr_read_b32 v44, a44\n\t" \
"v_mul_f32 v44, v44, v34\n\t" \
"v_accvgpr_write_b32 a44, v44\n\t" \
"v_accvgpr_read_b32 v45, a45\n\t" \
"v_mul_f32 v45, v45, v34\n\t" \
"v_accvgpr_write_b32 a45, v45\n\t" \
"v_accvgpr_read_b32 v46, a46\n\t" \
"v_mul_f32 v46, v46, v34\n\t" \
"v_accvgpr_write_b32 a46, v46\n\t" \
"v_accvgpr_read_b32 v47, a47\n\t" \
"v_mul_f32 v47, v47, v34\n\t" \
"v_accvgpr_write_b32 a47, v47\n\t" \
"v_accvgpr_read_b32 v40, a48\n\t" \
"v_mul_f32 v40, v40, v35\n\t" \
"v_accvgpr_write_b32 a48, v40\n\t" \
"v_accvgpr_read_b32 v41, a49\n\t" \
"v_mul_f32 v41, v41, v35\n\t" \
"v_accvgpr_write_b32 a49, v41\n\t" \
"v_accvgpr_read_b32 v42, a50\n\t" \
"v_mul_f32 v42, v42, v35\n\t" \
"v_accvgpr_write_b32 a50, v42\n\t" \
"v_accvgpr_read_b32 v43, a51\n\t" \
"v_mul_f32 v43, v43, v35\n\t" \
"v_accvgpr_write_b32 a51, v43\n\t" \
"v_accvgpr_read_b32 v44, a52\n\t" \
"v_mul_f32 v44, v44, v35\n\t" \
"v_accvgpr_write_b32 a52, v44\n\t" \
"v_accvgpr_read_b32 v45, a53\n\t" \
"v_mul_f32 v45, v45, v35\n\t" \
"v_accvgpr_write_b32 a53, v45\n\t" \
"v_accvgpr_read_b32 v46, a54\n\t" \
"v_mul_f32 v46, v46, v35\n\t" \
"v_accvgpr_write_b32 a54, v46\n\t" \
"v_accvgpr_read_b32 v47, a55\n\t" \
"v_mul_f32 v47, v47, v35\n\t" \
"v_accvgpr_write_b32 a55, v47\n\t" \
"v_accvgpr_read_b32 v40, a56\n\t" \
"v_mul_f32 v40, v40, v35\n\t" \
"v_accvgpr_write_b32 a56, v40\n\t" \
"v_accvgpr_read_b32 v41, a57\n\t" \
"v_mul_f32 v41, v41, v35\n\t" \
"v_accvgpr_write_b32 a57, v41\n\t" \
"v_accvgpr_read_b32 v42, a58\n\t" \
"v_mul_f32 v42, v42, v35\n\t" \
"v_accvgpr_write_b32 a58, v42\n\t" \
"v_accvgpr_read_b32 v43, a59\n\t" \
"v_mul_f32 v43, v43, v35\n\t" \
"v_accvgpr_write_b32 a59, v43\n\t" \
"v_accvgpr_read_b32 v44, a60\n\t" \
"v_mul_f32 v44, v44, v35\n\t" \
"v_accvgpr_write_b32 a60, v44\n\t" \
"v_accvgpr_read_b32 v45, a61\n\t" \
"v_mul_f32 v45, v45, v35\n\t" \
"v_accvgpr_write_b32 a61, v45\n\t" \
"v_accvgpr_read_b32 v46, a62\n\t" \
"v_mul_f32 v46, v46, v35\n\t" \
"v_accvgpr_write_b32 a62, v46\n\t" \
"v_accvgpr_read_b32 v47, a63\n\t" \
"v_mul_f32 v47, v47, v35\n\t" \
"v_accvgpr_write_b32 a63, v47\n\t" \
"v_accvgpr_read_b32 v40, a64\n\t" \
"v_mul_f32 v40, v40, v34\n\t" \
"v_accvgpr_write_b32 a64, v40\n\t" \
"v_accvgpr_read_b32 v41, a65\n\t" \
"v_mul_f32 v41, v41, v34\n\t" \
"v_accvgpr_write_b32 a65, v41\n\t" \
"v_accvgpr_read_b32 v42, a66\n\t" \
"v_mul_f32 v42, v42, v34\n\t" \
"v_accvgpr_write_b32 a66, v42\n\t" \
"v_accvgpr_read_b32 v43, a67\n\t" \
"v_mul_f32 v43, v43, v34\n\t" \
"v_accvgpr_write_b32 a67, v43\n\t" \
"v_accvgpr_read_b32 v44, a68\n\t" \
"v_mul_f32 v44, v44, v34\n\t" \
"v_accvgpr_write_b32 a68, v44\n\t" \
"v_accvgpr_read_b32 v45, a69\n\t" \
"v_mul_f32 v45, v45, v34\n\t" \
"v_accvgpr_write_b32 a69, v45\n\t" \
"v_accvgpr_read_b32 v46, a70\n\t" \
"v_mul_f32 v46, v46, v34\n\t" \
"v_accvgpr_write_b32 a70, v46\n\t" \
"v_accvgpr_read_b32 v47, a71\n\t" \
"v_mul_f32 v47, v47, v34\n\t" \
"v_accvgpr_write_b32 a71, v47\n\t" \
"v_accvgpr_read_b32 v40, a72\n\t" \
"v_mul_f32 v40, v40, v34\n\t" \
"v_accvgpr_write_b32 a72, v40\n\t" \
"v_accvgpr_read_b32 v41, a73\n\t" \
"v_mul_f32 v41, v41, v34\n\t" \
"v_accvgpr_write_b32 a73, v41\n\t" \
"v_accvgpr_read_b32 v42, a74\n\t" \
"v_mul_f32 v42, v42, v34\n\t" \
"v_accvgpr_write_b32 a74, v42\n\t" \
"v_accvgpr_read_b32 v43, a75\n\t" \
"v_mul_f32 v43, v43, v34\n\t" \
"v_accvgpr_write_b32 a75, v43\n\t" \
"v_accvgpr_read_b32 v44, a76\n\t" \
"v_mul_f32 v44, v44, v34\n\t" \
"v_accvgpr_write_b32 a76, v44\n\t" \
"v_accvgpr_read_b32 v45, a77\n\t" \
"v_mul_f32 v45, v45, v34\n\t" \
"v_accvgpr_write_b32 a77, v45\n\t" \
"v_accvgpr_read_b32 v46, a78\n\t" \
"v_mul_f32 v46, v46, v34\n\t" \
"v_accvgpr_write_b32 a78, v46\n\t" \
"v_accvgpr_read_b32 v47, a79\n\t" \
"v_mul_f32 v47, v47, v34\n\t" \
"v_accvgpr_write_b32 a79, v47\n\t" \
"v_accvgpr_read_b32 v40, a80\n\t" \
"v_mul_f32 v40, v40, v35\n\t" \
"v_accvgpr_write_b32 a80, v40\n\t" \
"v_accvgpr_read_b32 v41, a81\n\t" \
"v_mul_f32 v41, v41, v35\n\t" \
"v_accvgpr_write_b32 a81, v41\n\t" \
"v_accvgpr_read_b32 v42, a82\n\t" \
"v_mul_f32 v42, v42, v35\n\t" \
"v_accvgpr_write_b32 a82, v42\n\t" \
"v_accvgpr_read_b32 v43, a83\n\t" \
"v_mul_f32 v43, v43, v35\n\t" \
"v_accvgpr_write_b32 a83, v43\n\t" \
"v_accvgpr_read_b32 v44, a84\n\t" \
"v_mul_f32 v44, v44, v35\n\t" \
"v_accvgpr_write_b32 a84, v44\n\t" \
"v_accvgpr_read_b32 v45, a85\n\t" \
"v_mul_f32 v45, v45, v35\n\t" \
"v_accvgpr_write_b32 a85, v45\n\t" \
"v_accvgpr_read_b32 v46, a86\n\t" \
"v_mul_f32 v46, v46, v35\n\t" \
"v_accvgpr_write_b32 a86, v46\n\t" \
"v_accvgpr_read_b32 v47, a87\n\t" \
"v_mul_f32 v47, v47, v35\n\t" \
"v_accvgpr_write_b32 a87, v47\n\t" \
"v_accvgpr_read_b32 v40, a88\n\t" \
"v_mul_f32 v40, v40, v35\n\t" \
"v_accvgpr_write_b32 a88, v40\n\t" \
"v_accvgpr_read_b32 v41, a89\n\t" \
"v_mul_f32 v41, v41, v35\n\t" \
"v_accvgpr_write_b32 a89, v41\n\t" \
"v_accvgpr_read_b32 v42, a90\n\t" \
"v_mul_f32 v42, v42, v35\n\t" \
"v_accvgpr_write_b32 a90, v42\n\t" \
"v_accvgpr_read_b32 v43, a91\n\t" \
"v_mul_f32 v43, v43, v35\n\t" \
"v_accvgpr_write_b32 a91, v43\n\t" \
"v_accvgpr_read_b32 v44, a92\n\t" \
"v_mul_f32 v44, v44, v35\n\t" \
"v_accvgpr_write_b32 a92, v44\n\t" \
"v_accvgpr_read_b32 v45, a93\n\t" \
"v_mul_f32 v45, v45, v35\n\t" \
"v_accvgpr_write_b32 a93, v45\n\t" \
"v_accvgpr_read_b32 v46, a94\n\t" \
"v_mul_f32 v46, v46, v35\n\t" \
"v_accvgpr_write_b32 a94, v46\n\t" \
"v_accvgpr_read_b32 v47, a95\n\t" \
"v_mul_f32 v47, v47, v35\n\t" \
"v_accvgpr_write_b32 a95, v47\n\t" \
"v_accvgpr_read_b32 v40, a96\n\t" \
"v_mul_f32 v40, v40, v34\n\t" \
"v_accvgpr_write_b32 a96, v40\n\t" \
"v_accvgpr_read_b32 v41, a97\n\t" \
"v_mul_f32 v41, v41, v34\n\t" \
"v_accvgpr_write_b32 a97, v41\n\t" \
"v_accvgpr_read_b32 v42, a98\n\t" \
"v_mul_f32 v42, v42, v34\n\t" \
"v_accvgpr_write_b32 a98, v42\n\t" \
"v_accvgpr_read_b32 v43, a99\n\t" \
"v_mul_f32 v43, v43, v34\n\t" \
"v_accvgpr_write_b32 a99, v43\n\t" \
"v_accvgpr_read_b32 v44, a100\n\t" \
"v_mul_f32 v44, v44, v34\n\t" \
"v_accvgpr_write_b32 a100, v44\n\t" \
"v_accvgpr_read_b32 v45, a101\n\t" \
"v_mul_f32 v45, v45, v34\n\t" \
"v_accvgpr_write_b32 a101, v45\n\t" \
"v_accvgpr_read_b32 v46, a102\n\t" \
"v_mul_f32 v46, v46, v34\n\t" \
"v_accvgpr_write_b32 a102, v46\n\t" \
"v_accvgpr_read_b32 v47, a103\n\t" \
"v_mul_f32 v47, v47, v34\n\t" \
"v_accvgpr_write_b32 a103, v47\n\t" \
"v_accvgpr_read_b32 v40, a104\n\t" \
"v_mul_f32 v40, v40, v34\n\t" \
"v_accvgpr_write_b32 a104, v40\n\t" \
"v_accvgpr_read_b32 v41, a105\n\t" \
"v_mul_f32 v41, v41, v34\n\t" \
"v_accvgpr_write_b32 a105, v41\n\t" \
"v_accvgpr_read_b32 v42, a106\n\t" \
"v_mul_f32 v42, v42, v34\n\t" \
"v_accvgpr_write_b32 a106, v42\n\t" \
"v_accvgpr_read_b32 v43, a107\n\t" \
"v_mul_f32 v43, v43, v34\n\t" \
"v_accvgpr_write_b32 a107, v43\n\t" \
"v_accvgpr_read_b32 v44, a108\n\t" \
"v_mul_f32 v44, v44, v34\n\t" \
"v_accvgpr_write_b32 a108, v44\n\t" \
"v_accvgpr_read_b32 v45, a109\n\t" \
"v_mul_f32 v45, v45, v34\n\t" \
"v_accvgpr_write_b32 a109, v45\n\t" \
"v_accvgpr_read_b32 v46, a110\n\t" \
"v_mul_f32 v46, v46, v34\n\t" \
"v_accvgpr_write_b32 a110, v46\n\t" \
"v_accvgpr_read_b32 v47, a111\n\t" \
"v_mul_f32 v47, v47, v34\n\t" \
"v_accvgpr_write_b32 a111, v47\n\t" \
"v_accvgpr_read_b32 v40, a112\n\t" \
"v_mul_f32 v40, v40, v35\n\t" \
"v_accvgpr_write_b32 a112, v40\n\t" \
"v_accvgpr_read_b32 v41, a113\n\t" \
"v_mul_f32 v41, v41, v35\n\t" \
"v_accvgpr_write_b32 a113, v41\n\t" \
"v_accvgpr_read_b32 v42, a114\n\t" \
"v_mul_f32 v42, v42, v35\n\t" \
"v_accvgpr_write_b32 a114, v42\n\t" \
"v_accvgpr_read_b32 v43, a115\n\t" \
"v_mul_f32 v43, v43, v35\n\t" \
"v_accvgpr_write_b32 a115, v43\n\t" \
"v_accvgpr_read_b32 v44, a116\n\t" \
"v_mul_f32 v44, v44, v35\n\t" \
"v_accvgpr_write_b32 a116, v44\n\t" \
"v_accvgpr_read_b32 v45, a117\n\t" \
"v_mul_f32 v45, v45, v35\n\t" \
"v_accvgpr_write_b32 a117, v45\n\t" \
"v_accvgpr_read_b32 v46, a118\n\t" \
"v_mul_f32 v46, v46, v35\n\t" \
"v_accvgpr_write_b32 a118, v46\n\t" \
"v_accvgpr_read_b32 v47, a119\n\t" \
"v_mul_f32 v47, v47, v35\n\t" \
"v_accvgpr_write_b32 a119, v47\n\t" \
"v_accvgpr_read_b32 v40, a120\n\t" \
"v_mul_f32 v40, v40, v35\n\t" \
"v_accvgpr_write_b32 a120, v40\n\t" \
"v_accvgpr_read_b32 v41, a121\n\t" \
"v_mul_f32 v41, v41, v35\n\t" \
"v_accvgpr_write_b32 a121, v41\n\t" \
"v_accvgpr_read_b32 v42, a122\n\t" \
"v_mul_f32 v42, v42, v35\n\t" \
"v_accvgpr_write_b32 a122, v42\n\t" \
"v_accvgpr_read_b32 v43, a123\n\t" \
"v_mul_f32 v43, v43, v35\n\t" \
"v_accvgpr_write_b32 a123, v43\n\t" \
"v_accvgpr_read_b32 v44, a124\n\t" \
"v_mul_f32 v44, v44, v35\n\t" \
"v_accvgpr_write_b32 a124, v44\n\t" \
"v_accvgpr_read_b32 v45, a125\n\t" \
"v_mul_f32 v45, v45, v35\n\t" \
"v_accvgpr_write_b32 a125, v45\n\t" \
"v_accvgpr_read_b32 v46, a126\n\t" \
"v_mul_f32 v46, v46, v35\n\t" \
"v_accvgpr_write_b32 a126, v46\n\t" \
"v_accvgpr_read_b32 v47, a127\n\t" \
"v_mul_f32 v47, v47, v35\n\t" \
"v_accvgpr_write_b32 a127, v47\n\t" \
"s_mov_b32 s50, 0\n\t" \
"s_nop 1\n\t" \
"s_branch RESCB_p1_%=\n\t" \
"RESC_l0_%=:\n\t" \
"s_nop 15\n\t" \
"v_accvgpr_read_b32 v40, a0\n\t" \
"v_mul_f32 v40, v40, v34\n\t" \
"v_accvgpr_write_b32 a0, v40\n\t" \
"v_accvgpr_read_b32 v41, a1\n\t" \
"v_mul_f32 v41, v41, v34\n\t" \
"v_accvgpr_write_b32 a1, v41\n\t" \
"v_accvgpr_read_b32 v42, a2\n\t" \
"v_mul_f32 v42, v42, v34\n\t" \
"v_accvgpr_write_b32 a2, v42\n\t" \
"v_accvgpr_read_b32 v43, a3\n\t" \
"v_mul_f32 v43, v43, v34\n\t" \
"v_accvgpr_write_b32 a3, v43\n\t" \
"v_accvgpr_read_b32 v44, a4\n\t" \
"v_mul_f32 v44, v44, v34\n\t" \
"v_accvgpr_write_b32 a4, v44\n\t" \
"v_accvgpr_read_b32 v45, a5\n\t" \
"v_mul_f32 v45, v45, v34\n\t" \
"v_accvgpr_write_b32 a5, v45\n\t" \
"v_accvgpr_read_b32 v46, a6\n\t" \
"v_mul_f32 v46, v46, v34\n\t" \
"v_accvgpr_write_b32 a6, v46\n\t" \
"v_accvgpr_read_b32 v47, a7\n\t" \
"v_mul_f32 v47, v47, v34\n\t" \
"v_accvgpr_write_b32 a7, v47\n\t" \
"v_accvgpr_read_b32 v40, a8\n\t" \
"v_mul_f32 v40, v40, v34\n\t" \
"v_accvgpr_write_b32 a8, v40\n\t" \
"v_accvgpr_read_b32 v41, a9\n\t" \
"v_mul_f32 v41, v41, v34\n\t" \
"v_accvgpr_write_b32 a9, v41\n\t" \
"v_accvgpr_read_b32 v42, a10\n\t" \
"v_mul_f32 v42, v42, v34\n\t" \
"v_accvgpr_write_b32 a10, v42\n\t" \
"v_accvgpr_read_b32 v43, a11\n\t" \
"v_mul_f32 v43, v43, v34\n\t" \
"v_accvgpr_write_b32 a11, v43\n\t" \
"v_accvgpr_read_b32 v44, a12\n\t" \
"v_mul_f32 v44, v44, v34\n\t" \
"v_accvgpr_write_b32 a12, v44\n\t" \
"v_accvgpr_read_b32 v45, a13\n\t" \
"v_mul_f32 v45, v45, v34\n\t" \
"v_accvgpr_write_b32 a13, v45\n\t" \
"v_accvgpr_read_b32 v46, a14\n\t" \
"v_mul_f32 v46, v46, v34\n\t" \
"v_accvgpr_write_b32 a14, v46\n\t" \
"v_accvgpr_read_b32 v47, a15\n\t" \
"v_mul_f32 v47, v47, v34\n\t" \
"v_accvgpr_write_b32 a15, v47\n\t" \
"v_accvgpr_read_b32 v40, a16\n\t" \
"v_mul_f32 v40, v40, v35\n\t" \
"v_accvgpr_write_b32 a16, v40\n\t" \
"v_accvgpr_read_b32 v41, a17\n\t" \
"v_mul_f32 v41, v41, v35\n\t" \
"v_accvgpr_write_b32 a17, v41\n\t" \
"v_accvgpr_read_b32 v42, a18\n\t" \
"v_mul_f32 v42, v42, v35\n\t" \
"v_accvgpr_write_b32 a18, v42\n\t" \
"v_accvgpr_read_b32 v43, a19\n\t" \
"v_mul_f32 v43, v43, v35\n\t" \
"v_accvgpr_write_b32 a19, v43\n\t" \
"v_accvgpr_read_b32 v44, a20\n\t" \
"v_mul_f32 v44, v44, v35\n\t" \
"v_accvgpr_write_b32 a20, v44\n\t" \
"v_accvgpr_read_b32 v45, a21\n\t" \
"v_mul_f32 v45, v45, v35\n\t" \
"v_accvgpr_write_b32 a21, v45\n\t" \
"v_accvgpr_read_b32 v46, a22\n\t" \
"v_mul_f32 v46, v46, v35\n\t" \
"v_accvgpr_write_b32 a22, v46\n\t" \
"v_accvgpr_read_b32 v47, a23\n\t" \
"v_mul_f32 v47, v47, v35\n\t" \
"v_accvgpr_write_b32 a23, v47\n\t" \
"v_accvgpr_read_b32 v40, a24\n\t" \
"v_mul_f32 v40, v40, v35\n\t" \
"v_accvgpr_write_b32 a24, v40\n\t" \
"v_accvgpr_read_b32 v41, a25\n\t" \
"v_mul_f32 v41, v41, v35\n\t" \
"v_accvgpr_write_b32 a25, v41\n\t" \
"v_accvgpr_read_b32 v42, a26\n\t" \
"v_mul_f32 v42, v42, v35\n\t" \
"v_accvgpr_write_b32 a26, v42\n\t" \
"v_accvgpr_read_b32 v43, a27\n\t" \
"v_mul_f32 v43, v43, v35\n\t" \
"v_accvgpr_write_b32 a27, v43\n\t" \
"v_accvgpr_read_b32 v44, a28\n\t" \
"v_mul_f32 v44, v44, v35\n\t" \
"v_accvgpr_write_b32 a28, v44\n\t" \
"v_accvgpr_read_b32 v45, a29\n\t" \
"v_mul_f32 v45, v45, v35\n\t" \
"v_accvgpr_write_b32 a29, v45\n\t" \
"v_accvgpr_read_b32 v46, a30\n\t" \
"v_mul_f32 v46, v46, v35\n\t" \
"v_accvgpr_write_b32 a30, v46\n\t" \
"v_accvgpr_read_b32 v47, a31\n\t" \
"v_mul_f32 v47, v47, v35\n\t" \
"v_accvgpr_write_b32 a31, v47\n\t" \
"v_accvgpr_read_b32 v40, a32\n\t" \
"v_mul_f32 v40, v40, v34\n\t" \
"v_accvgpr_write_b32 a32, v40\n\t" \
"v_accvgpr_read_b32 v41, a33\n\t" \
"v_mul_f32 v41, v41, v34\n\t" \
"v_accvgpr_write_b32 a33, v41\n\t" \
"v_accvgpr_read_b32 v42, a34\n\t" \
"v_mul_f32 v42, v42, v34\n\t" \
"v_accvgpr_write_b32 a34, v42\n\t" \
"v_accvgpr_read_b32 v43, a35\n\t" \
"v_mul_f32 v43, v43, v34\n\t" \
"v_accvgpr_write_b32 a35, v43\n\t" \
"v_accvgpr_read_b32 v44, a36\n\t" \
"v_mul_f32 v44, v44, v34\n\t" \
"v_accvgpr_write_b32 a36, v44\n\t" \
"v_accvgpr_read_b32 v45, a37\n\t" \
"v_mul_f32 v45, v45, v34\n\t" \
"v_accvgpr_write_b32 a37, v45\n\t" \
"v_accvgpr_read_b32 v46, a38\n\t" \
"v_mul_f32 v46, v46, v34\n\t" \
"v_accvgpr_write_b32 a38, v46\n\t" \
"v_accvgpr_read_b32 v47, a39\n\t" \
"v_mul_f32 v47, v47, v34\n\t" \
"v_accvgpr_write_b32 a39, v47\n\t" \
"v_accvgpr_read_b32 v40, a40\n\t" \
"v_mul_f32 v40, v40, v34\n\t" \
"v_accvgpr_write_b32 a40, v40\n\t" \
"v_accvgpr_read_b32 v41, a41\n\t" \
"v_mul_f32 v41, v41, v34\n\t" \
"v_accvgpr_write_b32 a41, v41\n\t" \
"v_accvgpr_read_b32 v42, a42\n\t" \
"v_mul_f32 v42, v42, v34\n\t" \
"v_accvgpr_write_b32 a42, v42\n\t" \
"v_accvgpr_read_b32 v43, a43\n\t" \
"v_mul_f32 v43, v43, v34\n\t" \
"v_accvgpr_write_b32 a43, v43\n\t" \
"v_accvgpr_read_b32 v44, a44\n\t" \
"v_mul_f32 v44, v44, v34\n\t" \
"v_accvgpr_write_b32 a44, v44\n\t" \
"v_accvgpr_read_b32 v45, a45\n\t" \
"v_mul_f32 v45, v45, v34\n\t" \
"v_accvgpr_write_b32 a45, v45\n\t" \
"v_accvgpr_read_b32 v46, a46\n\t" \
"v_mul_f32 v46, v46, v34\n\t" \
"v_accvgpr_write_b32 a46, v46\n\t" \
"v_accvgpr_read_b32 v47, a47\n\t" \
"v_mul_f32 v47, v47, v34\n\t" \
"v_accvgpr_write_b32 a47, v47\n\t" \
"v_accvgpr_read_b32 v40, a48\n\t" \
"v_mul_f32 v40, v40, v35\n\t" \
"v_accvgpr_write_b32 a48, v40\n\t" \
"v_accvgpr_read_b32 v41, a49\n\t" \
"v_mul_f32 v41, v41, v35\n\t" \
"v_accvgpr_write_b32 a49, v41\n\t" \
"v_accvgpr_read_b32 v42, a50\n\t" \
"v_mul_f32 v42, v42, v35\n\t" \
"v_accvgpr_write_b32 a50, v42\n\t" \
"v_accvgpr_read_b32 v43, a51\n\t" \
"v_mul_f32 v43, v43, v35\n\t" \
"v_accvgpr_write_b32 a51, v43\n\t" \
"v_accvgpr_read_b32 v44, a52\n\t" \
"v_mul_f32 v44, v44, v35\n\t" \
"v_accvgpr_write_b32 a52, v44\n\t" \
"v_accvgpr_read_b32 v45, a53\n\t" \
"v_mul_f32 v45, v45, v35\n\t" \
"v_accvgpr_write_b32 a53, v45\n\t" \
"v_accvgpr_read_b32 v46, a54\n\t" \
"v_mul_f32 v46, v46, v35\n\t" \
"v_accvgpr_write_b32 a54, v46\n\t" \
"v_accvgpr_read_b32 v47, a55\n\t" \
"v_mul_f32 v47, v47, v35\n\t" \
"v_accvgpr_write_b32 a55, v47\n\t" \
"v_accvgpr_read_b32 v40, a56\n\t" \
"v_mul_f32 v40, v40, v35\n\t" \
"v_accvgpr_write_b32 a56, v40\n\t" \
"v_accvgpr_read_b32 v41, a57\n\t" \
"v_mul_f32 v41, v41, v35\n\t" \
"v_accvgpr_write_b32 a57, v41\n\t" \
"v_accvgpr_read_b32 v42, a58\n\t" \
"v_mul_f32 v42, v42, v35\n\t" \
"v_accvgpr_write_b32 a58, v42\n\t" \
"v_accvgpr_read_b32 v43, a59\n\t" \
"v_mul_f32 v43, v43, v35\n\t" \
"v_accvgpr_write_b32 a59, v43\n\t" \
"v_accvgpr_read_b32 v44, a60\n\t" \
"v_mul_f32 v44, v44, v35\n\t" \
"v_accvgpr_write_b32 a60, v44\n\t" \
"v_accvgpr_read_b32 v45, a61\n\t" \
"v_mul_f32 v45, v45, v35\n\t" \
"v_accvgpr_write_b32 a61, v45\n\t" \
"v_accvgpr_read_b32 v46, a62\n\t" \
"v_mul_f32 v46, v46, v35\n\t" \
"v_accvgpr_write_b32 a62, v46\n\t" \
"v_accvgpr_read_b32 v47, a63\n\t" \
"v_mul_f32 v47, v47, v35\n\t" \
"v_accvgpr_write_b32 a63, v47\n\t" \
"v_accvgpr_read_b32 v40, a64\n\t" \
"v_mul_f32 v40, v40, v34\n\t" \
"v_accvgpr_write_b32 a64, v40\n\t" \
"v_accvgpr_read_b32 v41, a65\n\t" \
"v_mul_f32 v41, v41, v34\n\t" \
"v_accvgpr_write_b32 a65, v41\n\t" \
"v_accvgpr_read_b32 v42, a66\n\t" \
"v_mul_f32 v42, v42, v34\n\t" \
"v_accvgpr_write_b32 a66, v42\n\t" \
"v_accvgpr_read_b32 v43, a67\n\t" \
"v_mul_f32 v43, v43, v34\n\t" \
"v_accvgpr_write_b32 a67, v43\n\t" \
"v_accvgpr_read_b32 v44, a68\n\t" \
"v_mul_f32 v44, v44, v34\n\t" \
"v_accvgpr_write_b32 a68, v44\n\t" \
"v_accvgpr_read_b32 v45, a69\n\t" \
"v_mul_f32 v45, v45, v34\n\t" \
"v_accvgpr_write_b32 a69, v45\n\t" \
"v_accvgpr_read_b32 v46, a70\n\t" \
"v_mul_f32 v46, v46, v34\n\t" \
"v_accvgpr_write_b32 a70, v46\n\t" \
"v_accvgpr_read_b32 v47, a71\n\t" \
"v_mul_f32 v47, v47, v34\n\t" \
"v_accvgpr_write_b32 a71, v47\n\t" \
"v_accvgpr_read_b32 v40, a72\n\t" \
"v_mul_f32 v40, v40, v34\n\t" \
"v_accvgpr_write_b32 a72, v40\n\t" \
"v_accvgpr_read_b32 v41, a73\n\t" \
"v_mul_f32 v41, v41, v34\n\t" \
"v_accvgpr_write_b32 a73, v41\n\t" \
"v_accvgpr_read_b32 v42, a74\n\t" \
"v_mul_f32 v42, v42, v34\n\t" \
"v_accvgpr_write_b32 a74, v42\n\t" \
"v_accvgpr_read_b32 v43, a75\n\t" \
"v_mul_f32 v43, v43, v34\n\t" \
"v_accvgpr_write_b32 a75, v43\n\t" \
"v_accvgpr_read_b32 v44, a76\n\t" \
"v_mul_f32 v44, v44, v34\n\t" \
"v_accvgpr_write_b32 a76, v44\n\t" \
"v_accvgpr_read_b32 v45, a77\n\t" \
"v_mul_f32 v45, v45, v34\n\t" \
"v_accvgpr_write_b32 a77, v45\n\t" \
"v_accvgpr_read_b32 v46, a78\n\t" \
"v_mul_f32 v46, v46, v34\n\t" \
"v_accvgpr_write_b32 a78, v46\n\t" \
"v_accvgpr_read_b32 v47, a79\n\t" \
"v_mul_f32 v47, v47, v34\n\t" \
"v_accvgpr_write_b32 a79, v47\n\t" \
"v_accvgpr_read_b32 v40, a80\n\t" \
"v_mul_f32 v40, v40, v35\n\t" \
"v_accvgpr_write_b32 a80, v40\n\t" \
"v_accvgpr_read_b32 v41, a81\n\t" \
"v_mul_f32 v41, v41, v35\n\t" \
"v_accvgpr_write_b32 a81, v41\n\t" \
"v_accvgpr_read_b32 v42, a82\n\t" \
"v_mul_f32 v42, v42, v35\n\t" \
"v_accvgpr_write_b32 a82, v42\n\t" \
"v_accvgpr_read_b32 v43, a83\n\t" \
"v_mul_f32 v43, v43, v35\n\t" \
"v_accvgpr_write_b32 a83, v43\n\t" \
"v_accvgpr_read_b32 v44, a84\n\t" \
"v_mul_f32 v44, v44, v35\n\t" \
"v_accvgpr_write_b32 a84, v44\n\t" \
"v_accvgpr_read_b32 v45, a85\n\t" \
"v_mul_f32 v45, v45, v35\n\t" \
"v_accvgpr_write_b32 a85, v45\n\t" \
"v_accvgpr_read_b32 v46, a86\n\t" \
"v_mul_f32 v46, v46, v35\n\t" \
"v_accvgpr_write_b32 a86, v46\n\t" \
"v_accvgpr_read_b32 v47, a87\n\t" \
"v_mul_f32 v47, v47, v35\n\t" \
"v_accvgpr_write_b32 a87, v47\n\t" \
"v_accvgpr_read_b32 v40, a88\n\t" \
"v_mul_f32 v40, v40, v35\n\t" \
"v_accvgpr_write_b32 a88, v40\n\t" \
"v_accvgpr_read_b32 v41, a89\n\t" \
"v_mul_f32 v41, v41, v35\n\t" \
"v_accvgpr_write_b32 a89, v41\n\t" \
"v_accvgpr_read_b32 v42, a90\n\t" \
"v_mul_f32 v42, v42, v35\n\t" \
"v_accvgpr_write_b32 a90, v42\n\t" \
"v_accvgpr_read_b32 v43, a91\n\t" \
"v_mul_f32 v43, v43, v35\n\t" \
"v_accvgpr_write_b32 a91, v43\n\t" \
"v_accvgpr_read_b32 v44, a92\n\t" \
"v_mul_f32 v44, v44, v35\n\t" \
"v_accvgpr_write_b32 a92, v44\n\t" \
"v_accvgpr_read_b32 v45, a93\n\t" \
"v_mul_f32 v45, v45, v35\n\t" \
"v_accvgpr_write_b32 a93, v45\n\t" \
"v_accvgpr_read_b32 v46, a94\n\t" \
"v_mul_f32 v46, v46, v35\n\t" \
"v_accvgpr_write_b32 a94, v46\n\t" \
"v_accvgpr_read_b32 v47, a95\n\t" \
"v_mul_f32 v47, v47, v35\n\t" \
"v_accvgpr_write_b32 a95, v47\n\t" \
"v_accvgpr_read_b32 v40, a96\n\t" \
"v_mul_f32 v40, v40, v34\n\t" \
"v_accvgpr_write_b32 a96, v40\n\t" \
"v_accvgpr_read_b32 v41, a97\n\t" \
"v_mul_f32 v41, v41, v34\n\t" \
"v_accvgpr_write_b32 a97, v41\n\t" \
"v_accvgpr_read_b32 v42, a98\n\t" \
"v_mul_f32 v42, v42, v34\n\t" \
"v_accvgpr_write_b32 a98, v42\n\t" \
"v_accvgpr_read_b32 v43, a99\n\t" \
"v_mul_f32 v43, v43, v34\n\t" \
"v_accvgpr_write_b32 a99, v43\n\t" \
"v_accvgpr_read_b32 v44, a100\n\t" \
"v_mul_f32 v44, v44, v34\n\t" \
"v_accvgpr_write_b32 a100, v44\n\t" \
"v_accvgpr_read_b32 v45, a101\n\t" \
"v_mul_f32 v45, v45, v34\n\t" \
"v_accvgpr_write_b32 a101, v45\n\t" \
"v_accvgpr_read_b32 v46, a102\n\t" \
"v_mul_f32 v46, v46, v34\n\t" \
"v_accvgpr_write_b32 a102, v46\n\t" \
"v_accvgpr_read_b32 v47, a103\n\t" \
"v_mul_f32 v47, v47, v34\n\t" \
"v_accvgpr_write_b32 a103, v47\n\t" \
"v_accvgpr_read_b32 v40, a104\n\t" \
"v_mul_f32 v40, v40, v34\n\t" \
"v_accvgpr_write_b32 a104, v40\n\t" \
"v_accvgpr_read_b32 v41, a105\n\t" \
"v_mul_f32 v41, v41, v34\n\t" \
"v_accvgpr_write_b32 a105, v41\n\t" \
"v_accvgpr_read_b32 v42, a106\n\t" \
"v_mul_f32 v42, v42, v34\n\t" \
"v_accvgpr_write_b32 a106, v42\n\t" \
"v_accvgpr_read_b32 v43, a107\n\t" \
"v_mul_f32 v43, v43, v34\n\t" \
"v_accvgpr_write_b32 a107, v43\n\t" \
"v_accvgpr_read_b32 v44, a108\n\t" \
"v_mul_f32 v44, v44, v34\n\t" \
"v_accvgpr_write_b32 a108, v44\n\t" \
"v_accvgpr_read_b32 v45, a109\n\t" \
"v_mul_f32 v45, v45, v34\n\t" \
"v_accvgpr_write_b32 a109, v45\n\t" \
"v_accvgpr_read_b32 v46, a110\n\t" \
"v_mul_f32 v46, v46, v34\n\t" \
"v_accvgpr_write_b32 a110, v46\n\t" \
"v_accvgpr_read_b32 v47, a111\n\t" \
"v_mul_f32 v47, v47, v34\n\t" \
"v_accvgpr_write_b32 a111, v47\n\t" \
"v_accvgpr_read_b32 v40, a112\n\t" \
"v_mul_f32 v40, v40, v35\n\t" \
"v_accvgpr_write_b32 a112, v40\n\t" \
"v_accvgpr_read_b32 v41, a113\n\t" \
"v_mul_f32 v41, v41, v35\n\t" \
"v_accvgpr_write_b32 a113, v41\n\t" \
"v_accvgpr_read_b32 v42, a114\n\t" \
"v_mul_f32 v42, v42, v35\n\t" \
"v_accvgpr_write_b32 a114, v42\n\t" \
"v_accvgpr_read_b32 v43, a115\n\t" \
"v_mul_f32 v43, v43, v35\n\t" \
"v_accvgpr_write_b32 a115, v43\n\t" \
"v_accvgpr_read_b32 v44, a116\n\t" \
"v_mul_f32 v44, v44, v35\n\t" \
"v_accvgpr_write_b32 a116, v44\n\t" \
"v_accvgpr_read_b32 v45, a117\n\t" \
"v_mul_f32 v45, v45, v35\n\t" \
"v_accvgpr_write_b32 a117, v45\n\t" \
"v_accvgpr_read_b32 v46, a118\n\t" \
"v_mul_f32 v46, v46, v35\n\t" \
"v_accvgpr_write_b32 a118, v46\n\t" \
"v_accvgpr_read_b32 v47, a119\n\t" \
"v_mul_f32 v47, v47, v35\n\t" \
"v_accvgpr_write_b32 a119, v47\n\t" \
"v_accvgpr_read_b32 v40, a120\n\t" \
"v_mul_f32 v40, v40, v35\n\t" \
"v_accvgpr_write_b32 a120, v40\n\t" \
"v_accvgpr_read_b32 v41, a121\n\t" \
"v_mul_f32 v41, v41, v35\n\t" \
"v_accvgpr_write_b32 a121, v41\n\t" \
"v_accvgpr_read_b32 v42, a122\n\t" \
"v_mul_f32 v42, v42, v35\n\t" \
"v_accvgpr_write_b32 a122, v42\n\t" \
"v_accvgpr_read_b32 v43, a123\n\t" \
"v_mul_f32 v43, v43, v35\n\t" \
"v_accvgpr_write_b32 a123, v43\n\t" \
"v_accvgpr_read_b32 v44, a124\n\t" \
"v_mul_f32 v44, v44, v35\n\t" \
"v_accvgpr_write_b32 a124, v44\n\t" \
"v_accvgpr_read_b32 v45, a125\n\t" \
"v_mul_f32 v45, v45, v35\n\t" \
"v_accvgpr_write_b32 a125, v45\n\t" \
"v_accvgpr_read_b32 v46, a126\n\t" \
"v_mul_f32 v46, v46, v35\n\t" \
"v_accvgpr_write_b32 a126, v46\n\t" \
"v_accvgpr_read_b32 v47, a127\n\t" \
"v_mul_f32 v47, v47, v35\n\t" \
"v_accvgpr_write_b32 a127, v47\n\t" \
"s_mov_b32 s50, 0\n\t" \
"s_nop 1\n\t" \
"s_branch RESCB_l0_%=\n\t" \
"RESC_l1_%=:\n\t" \
"s_nop 15\n\t" \
"v_accvgpr_read_b32 v40, a0\n\t" \
"v_mul_f32 v40, v40, v34\n\t" \
"v_accvgpr_write_b32 a0, v40\n\t" \
"v_accvgpr_read_b32 v41, a1\n\t" \
"v_mul_f32 v41, v41, v34\n\t" \
"v_accvgpr_write_b32 a1, v41\n\t" \
"v_accvgpr_read_b32 v42, a2\n\t" \
"v_mul_f32 v42, v42, v34\n\t" \
"v_accvgpr_write_b32 a2, v42\n\t" \
"v_accvgpr_read_b32 v43, a3\n\t" \
"v_mul_f32 v43, v43, v34\n\t" \
"v_accvgpr_write_b32 a3, v43\n\t" \
"v_accvgpr_read_b32 v44, a4\n\t" \
"v_mul_f32 v44, v44, v34\n\t" \
"v_accvgpr_write_b32 a4, v44\n\t" \
"v_accvgpr_read_b32 v45, a5\n\t" \
"v_mul_f32 v45, v45, v34\n\t" \
"v_accvgpr_write_b32 a5, v45\n\t" \
"v_accvgpr_read_b32 v46, a6\n\t" \
"v_mul_f32 v46, v46, v34\n\t" \
"v_accvgpr_write_b32 a6, v46\n\t" \
"v_accvgpr_read_b32 v47, a7\n\t" \
"v_mul_f32 v47, v47, v34\n\t" \
"v_accvgpr_write_b32 a7, v47\n\t" \
"v_accvgpr_read_b32 v40, a8\n\t" \
"v_mul_f32 v40, v40, v34\n\t" \
"v_accvgpr_write_b32 a8, v40\n\t" \
"v_accvgpr_read_b32 v41, a9\n\t" \
"v_mul_f32 v41, v41, v34\n\t" \
"v_accvgpr_write_b32 a9, v41\n\t" \
"v_accvgpr_read_b32 v42, a10\n\t" \
"v_mul_f32 v42, v42, v34\n\t" \
"v_accvgpr_write_b32 a10, v42\n\t" \
"v_accvgpr_read_b32 v43, a11\n\t" \
"v_mul_f32 v43, v43, v34\n\t" \
"v_accvgpr_write_b32 a11, v43\n\t" \
"v_accvgpr_read_b32 v44, a12\n\t" \
"v_mul_f32 v44, v44, v34\n\t" \
"v_accvgpr_write_b32 a12, v44\n\t" \
"v_accvgpr_read_b32 v45, a13\n\t" \
"v_mul_f32 v45, v45, v34\n\t" \
"v_accvgpr_write_b32 a13, v45\n\t" \
"v_accvgpr_read_b32 v46, a14\n\t" \
"v_mul_f32 v46, v46, v34\n\t" \
"v_accvgpr_write_b32 a14, v46\n\t" \
"v_accvgpr_read_b32 v47, a15\n\t" \
"v_mul_f32 v47, v47, v34\n\t" \
"v_accvgpr_write_b32 a15, v47\n\t" \
"v_accvgpr_read_b32 v40, a16\n\t" \
"v_mul_f32 v40, v40, v35\n\t" \
"v_accvgpr_write_b32 a16, v40\n\t" \
"v_accvgpr_read_b32 v41, a17\n\t" \
"v_mul_f32 v41, v41, v35\n\t" \
"v_accvgpr_write_b32 a17, v41\n\t" \
"v_accvgpr_read_b32 v42, a18\n\t" \
"v_mul_f32 v42, v42, v35\n\t" \
"v_accvgpr_write_b32 a18, v42\n\t" \
"v_accvgpr_read_b32 v43, a19\n\t" \
"v_mul_f32 v43, v43, v35\n\t" \
"v_accvgpr_write_b32 a19, v43\n\t" \
"v_accvgpr_read_b32 v44, a20\n\t" \
"v_mul_f32 v44, v44, v35\n\t" \
"v_accvgpr_write_b32 a20, v44\n\t" \
"v_accvgpr_read_b32 v45, a21\n\t" \
"v_mul_f32 v45, v45, v35\n\t" \
"v_accvgpr_write_b32 a21, v45\n\t" \
"v_accvgpr_read_b32 v46, a22\n\t" \
"v_mul_f32 v46, v46, v35\n\t" \
"v_accvgpr_write_b32 a22, v46\n\t" \
"v_accvgpr_read_b32 v47, a23\n\t" \
"v_mul_f32 v47, v47, v35\n\t" \
"v_accvgpr_write_b32 a23, v47\n\t" \
"v_accvgpr_read_b32 v40, a24\n\t" \
"v_mul_f32 v40, v40, v35\n\t" \
"v_accvgpr_write_b32 a24, v40\n\t" \
"v_accvgpr_read_b32 v41, a25\n\t" \
"v_mul_f32 v41, v41, v35\n\t" \
"v_accvgpr_write_b32 a25, v41\n\t" \
"v_accvgpr_read_b32 v42, a26\n\t" \
"v_mul_f32 v42, v42, v35\n\t" \
"v_accvgpr_write_b32 a26, v42\n\t" \
"v_accvgpr_read_b32 v43, a27\n\t" \
"v_mul_f32 v43, v43, v35\n\t" \
"v_accvgpr_write_b32 a27, v43\n\t" \
"v_accvgpr_read_b32 v44, a28\n\t" \
"v_mul_f32 v44, v44, v35\n\t" \
"v_accvgpr_write_b32 a28, v44\n\t" \
"v_accvgpr_read_b32 v45, a29\n\t" \
"v_mul_f32 v45, v45, v35\n\t" \
"v_accvgpr_write_b32 a29, v45\n\t" \
"v_accvgpr_read_b32 v46, a30\n\t" \
"v_mul_f32 v46, v46, v35\n\t" \
"v_accvgpr_write_b32 a30, v46\n\t" \
"v_accvgpr_read_b32 v47, a31\n\t" \
"v_mul_f32 v47, v47, v35\n\t" \
"v_accvgpr_write_b32 a31, v47\n\t" \
"v_accvgpr_read_b32 v40, a32\n\t" \
"v_mul_f32 v40, v40, v34\n\t" \
"v_accvgpr_write_b32 a32, v40\n\t" \
"v_accvgpr_read_b32 v41, a33\n\t" \
"v_mul_f32 v41, v41, v34\n\t" \
"v_accvgpr_write_b32 a33, v41\n\t" \
"v_accvgpr_read_b32 v42, a34\n\t" \
"v_mul_f32 v42, v42, v34\n\t" \
"v_accvgpr_write_b32 a34, v42\n\t" \
"v_accvgpr_read_b32 v43, a35\n\t" \
"v_mul_f32 v43, v43, v34\n\t" \
"v_accvgpr_write_b32 a35, v43\n\t" \
"v_accvgpr_read_b32 v44, a36\n\t" \
"v_mul_f32 v44, v44, v34\n\t" \
"v_accvgpr_write_b32 a36, v44\n\t" \
"v_accvgpr_read_b32 v45, a37\n\t" \
"v_mul_f32 v45, v45, v34\n\t" \
"v_accvgpr_write_b32 a37, v45\n\t" \
"v_accvgpr_read_b32 v46, a38\n\t" \
"v_mul_f32 v46, v46, v34\n\t" \
"v_accvgpr_write_b32 a38, v46\n\t" \
"v_accvgpr_read_b32 v47, a39\n\t" \
"v_mul_f32 v47, v47, v34\n\t" \
"v_accvgpr_write_b32 a39, v47\n\t" \
"v_accvgpr_read_b32 v40, a40\n\t" \
"v_mul_f32 v40, v40, v34\n\t" \
"v_accvgpr_write_b32 a40, v40\n\t" \
"v_accvgpr_read_b32 v41, a41\n\t" \
"v_mul_f32 v41, v41, v34\n\t" \
"v_accvgpr_write_b32 a41, v41\n\t" \
"v_accvgpr_read_b32 v42, a42\n\t" \
"v_mul_f32 v42, v42, v34\n\t" \
"v_accvgpr_write_b32 a42, v42\n\t" \
"v_accvgpr_read_b32 v43, a43\n\t" \
"v_mul_f32 v43, v43, v34\n\t" \
"v_accvgpr_write_b32 a43, v43\n\t" \
"v_accvgpr_read_b32 v44, a44\n\t" \
"v_mul_f32 v44, v44, v34\n\t" \
"v_accvgpr_write_b32 a44, v44\n\t" \
"v_accvgpr_read_b32 v45, a45\n\t" \
"v_mul_f32 v45, v45, v34\n\t" \
"v_accvgpr_write_b32 a45, v45\n\t" \
"v_accvgpr_read_b32 v46, a46\n\t" \
"v_mul_f32 v46, v46, v34\n\t" \
"v_accvgpr_write_b32 a46, v46\n\t" \
"v_accvgpr_read_b32 v47, a47\n\t" \
"v_mul_f32 v47, v47, v34\n\t" \
"v_accvgpr_write_b32 a47, v47\n\t" \
"v_accvgpr_read_b32 v40, a48\n\t" \
"v_mul_f32 v40, v40, v35\n\t" \
"v_accvgpr_write_b32 a48, v40\n\t" \
"v_accvgpr_read_b32 v41, a49\n\t" \
"v_mul_f32 v41, v41, v35\n\t" \
"v_accvgpr_write_b32 a49, v41\n\t" \
"v_accvgpr_read_b32 v42, a50\n\t" \
"v_mul_f32 v42, v42, v35\n\t" \
"v_accvgpr_write_b32 a50, v42\n\t" \
"v_accvgpr_read_b32 v43, a51\n\t" \
"v_mul_f32 v43, v43, v35\n\t" \
"v_accvgpr_write_b32 a51, v43\n\t" \
"v_accvgpr_read_b32 v44, a52\n\t" \
"v_mul_f32 v44, v44, v35\n\t" \
"v_accvgpr_write_b32 a52, v44\n\t" \
"v_accvgpr_read_b32 v45, a53\n\t" \
"v_mul_f32 v45, v45, v35\n\t" \
"v_accvgpr_write_b32 a53, v45\n\t" \
"v_accvgpr_read_b32 v46, a54\n\t" \
"v_mul_f32 v46, v46, v35\n\t" \
"v_accvgpr_write_b32 a54, v46\n\t" \
"v_accvgpr_read_b32 v47, a55\n\t" \
"v_mul_f32 v47, v47, v35\n\t" \
"v_accvgpr_write_b32 a55, v47\n\t" \
"v_accvgpr_read_b32 v40, a56\n\t" \
"v_mul_f32 v40, v40, v35\n\t" \
"v_accvgpr_write_b32 a56, v40\n\t" \
"v_accvgpr_read_b32 v41, a57\n\t" \
"v_mul_f32 v41, v41, v35\n\t" \
"v_accvgpr_write_b32 a57, v41\n\t" \
"v_accvgpr_read_b32 v42, a58\n\t" \
"v_mul_f32 v42, v42, v35\n\t" \
"v_accvgpr_write_b32 a58, v42\n\t" \
"v_accvgpr_read_b32 v43, a59\n\t" \
"v_mul_f32 v43, v43, v35\n\t" \
"v_accvgpr_write_b32 a59, v43\n\t" \
"v_accvgpr_read_b32 v44, a60\n\t" \
"v_mul_f32 v44, v44, v35\n\t" \
"v_accvgpr_write_b32 a60, v44\n\t" \
"v_accvgpr_read_b32 v45, a61\n\t" \
"v_mul_f32 v45, v45, v35\n\t" \
"v_accvgpr_write_b32 a61, v45\n\t" \
"v_accvgpr_read_b32 v46, a62\n\t" \
"v_mul_f32 v46, v46, v35\n\t" \
"v_accvgpr_write_b32 a62, v46\n\t" \
"v_accvgpr_read_b32 v47, a63\n\t" \
"v_mul_f32 v47, v47, v35\n\t" \
"v_accvgpr_write_b32 a63, v47\n\t" \
"v_accvgpr_read_b32 v40, a64\n\t" \
"v_mul_f32 v40, v40, v34\n\t" \
"v_accvgpr_write_b32 a64, v40\n\t" \
"v_accvgpr_read_b32 v41, a65\n\t" \
"v_mul_f32 v41, v41, v34\n\t" \
"v_accvgpr_write_b32 a65, v41\n\t" \
"v_accvgpr_read_b32 v42, a66\n\t" \
"v_mul_f32 v42, v42, v34\n\t" \
"v_accvgpr_write_b32 a66, v42\n\t" \
"v_accvgpr_read_b32 v43, a67\n\t" \
"v_mul_f32 v43, v43, v34\n\t" \
"v_accvgpr_write_b32 a67, v43\n\t" \
"v_accvgpr_read_b32 v44, a68\n\t" \
"v_mul_f32 v44, v44, v34\n\t" \
"v_accvgpr_write_b32 a68, v44\n\t" \
"v_accvgpr_read_b32 v45, a69\n\t" \
"v_mul_f32 v45, v45, v34\n\t" \
"v_accvgpr_write_b32 a69, v45\n\t" \
"v_accvgpr_read_b32 v46, a70\n\t" \
"v_mul_f32 v46, v46, v34\n\t" \
"v_accvgpr_write_b32 a70, v46\n\t" \
"v_accvgpr_read_b32 v47, a71\n\t" \
"v_mul_f32 v47, v47, v34\n\t" \
"v_accvgpr_write_b32 a71, v47\n\t" \
"v_accvgpr_read_b32 v40, a72\n\t" \
"v_mul_f32 v40, v40, v34\n\t" \
"v_accvgpr_write_b32 a72, v40\n\t" \
"v_accvgpr_read_b32 v41, a73\n\t" \
"v_mul_f32 v41, v41, v34\n\t" \
"v_accvgpr_write_b32 a73, v41\n\t" \
"v_accvgpr_read_b32 v42, a74\n\t" \
"v_mul_f32 v42, v42, v34\n\t" \
"v_accvgpr_write_b32 a74, v42\n\t" \
"v_accvgpr_read_b32 v43, a75\n\t" \
"v_mul_f32 v43, v43, v34\n\t" \
"v_accvgpr_write_b32 a75, v43\n\t" \
"v_accvgpr_read_b32 v44, a76\n\t" \
"v_mul_f32 v44, v44, v34\n\t" \
"v_accvgpr_write_b32 a76, v44\n\t" \
"v_accvgpr_read_b32 v45, a77\n\t" \
"v_mul_f32 v45, v45, v34\n\t" \
"v_accvgpr_write_b32 a77, v45\n\t" \
"v_accvgpr_read_b32 v46, a78\n\t" \
"v_mul_f32 v46, v46, v34\n\t" \
"v_accvgpr_write_b32 a78, v46\n\t" \
"v_accvgpr_read_b32 v47, a79\n\t" \
"v_mul_f32 v47, v47, v34\n\t" \
"v_accvgpr_write_b32 a79, v47\n\t" \
"v_accvgpr_read_b32 v40, a80\n\t" \
"v_mul_f32 v40, v40, v35\n\t" \
"v_accvgpr_write_b32 a80, v40\n\t" \
"v_accvgpr_read_b32 v41, a81\n\t" \
"v_mul_f32 v41, v41, v35\n\t" \
"v_accvgpr_write_b32 a81, v41\n\t" \
"v_accvgpr_read_b32 v42, a82\n\t" \
"v_mul_f32 v42, v42, v35\n\t" \
"v_accvgpr_write_b32 a82, v42\n\t" \
"v_accvgpr_read_b32 v43, a83\n\t" \
"v_mul_f32 v43, v43, v35\n\t" \
"v_accvgpr_write_b32 a83, v43\n\t" \
"v_accvgpr_read_b32 v44, a84\n\t" \
"v_mul_f32 v44, v44, v35\n\t" \
"v_accvgpr_write_b32 a84, v44\n\t" \
"v_accvgpr_read_b32 v45, a85\n\t" \
"v_mul_f32 v45, v45, v35\n\t" \
"v_accvgpr_write_b32 a85, v45\n\t" \
"v_accvgpr_read_b32 v46, a86\n\t" \
"v_mul_f32 v46, v46, v35\n\t" \
"v_accvgpr_write_b32 a86, v46\n\t" \
"v_accvgpr_read_b32 v47, a87\n\t" \
"v_mul_f32 v47, v47, v35\n\t" \
"v_accvgpr_write_b32 a87, v47\n\t" \
"v_accvgpr_read_b32 v40, a88\n\t" \
"v_mul_f32 v40, v40, v35\n\t" \
"v_accvgpr_write_b32 a88, v40\n\t" \
"v_accvgpr_read_b32 v41, a89\n\t" \
"v_mul_f32 v41, v41, v35\n\t" \
"v_accvgpr_write_b32 a89, v41\n\t" \
"v_accvgpr_read_b32 v42, a90\n\t" \
"v_mul_f32 v42, v42, v35\n\t" \
"v_accvgpr_write_b32 a90, v42\n\t" \
"v_accvgpr_read_b32 v43, a91\n\t" \
"v_mul_f32 v43, v43, v35\n\t" \
"v_accvgpr_write_b32 a91, v43\n\t" \
"v_accvgpr_read_b32 v44, a92\n\t" \
"v_mul_f32 v44, v44, v35\n\t" \
"v_accvgpr_write_b32 a92, v44\n\t" \
"v_accvgpr_read_b32 v45, a93\n\t" \
"v_mul_f32 v45, v45, v35\n\t" \
"v_accvgpr_write_b32 a93, v45\n\t" \
"v_accvgpr_read_b32 v46, a94\n\t" \
"v_mul_f32 v46, v46, v35\n\t" \
"v_accvgpr_write_b32 a94, v46\n\t" \
"v_accvgpr_read_b32 v47, a95\n\t" \
"v_mul_f32 v47, v47, v35\n\t" \
"v_accvgpr_write_b32 a95, v47\n\t" \
"v_accvgpr_read_b32 v40, a96\n\t" \
"v_mul_f32 v40, v40, v34\n\t" \
"v_accvgpr_write_b32 a96, v40\n\t" \
"v_accvgpr_read_b32 v41, a97\n\t" \
"v_mul_f32 v41, v41, v34\n\t" \
"v_accvgpr_write_b32 a97, v41\n\t" \
"v_accvgpr_read_b32 v42, a98\n\t" \
"v_mul_f32 v42, v42, v34\n\t" \
"v_accvgpr_write_b32 a98, v42\n\t" \
"v_accvgpr_read_b32 v43, a99\n\t" \
"v_mul_f32 v43, v43, v34\n\t" \
"v_accvgpr_write_b32 a99, v43\n\t" \
"v_accvgpr_read_b32 v44, a100\n\t" \
"v_mul_f32 v44, v44, v34\n\t" \
"v_accvgpr_write_b32 a100, v44\n\t" \
"v_accvgpr_read_b32 v45, a101\n\t" \
"v_mul_f32 v45, v45, v34\n\t" \
"v_accvgpr_write_b32 a101, v45\n\t" \
"v_accvgpr_read_b32 v46, a102\n\t" \
"v_mul_f32 v46, v46, v34\n\t" \
"v_accvgpr_write_b32 a102, v46\n\t" \
"v_accvgpr_read_b32 v47, a103\n\t" \
"v_mul_f32 v47, v47, v34\n\t" \
"v_accvgpr_write_b32 a103, v47\n\t" \
"v_accvgpr_read_b32 v40, a104\n\t" \
"v_mul_f32 v40, v40, v34\n\t" \
"v_accvgpr_write_b32 a104, v40\n\t" \
"v_accvgpr_read_b32 v41, a105\n\t" \
"v_mul_f32 v41, v41, v34\n\t" \
"v_accvgpr_write_b32 a105, v41\n\t" \
"v_accvgpr_read_b32 v42, a106\n\t" \
"v_mul_f32 v42, v42, v34\n\t" \
"v_accvgpr_write_b32 a106, v42\n\t" \
"v_accvgpr_read_b32 v43, a107\n\t" \
"v_mul_f32 v43, v43, v34\n\t" \
"v_accvgpr_write_b32 a107, v43\n\t" \
"v_accvgpr_read_b32 v44, a108\n\t" \
"v_mul_f32 v44, v44, v34\n\t" \
"v_accvgpr_write_b32 a108, v44\n\t" \
"v_accvgpr_read_b32 v45, a109\n\t" \
"v_mul_f32 v45, v45, v34\n\t" \
"v_accvgpr_write_b32 a109, v45\n\t" \
"v_accvgpr_read_b32 v46, a110\n\t" \
"v_mul_f32 v46, v46, v34\n\t" \
"v_accvgpr_write_b32 a110, v46\n\t" \
"v_accvgpr_read_b32 v47, a111\n\t" \
"v_mul_f32 v47, v47, v34\n\t" \
"v_accvgpr_write_b32 a111, v47\n\t" \
"v_accvgpr_read_b32 v40, a112\n\t" \
"v_mul_f32 v40, v40, v35\n\t" \
"v_accvgpr_write_b32 a112, v40\n\t" \
"v_accvgpr_read_b32 v41, a113\n\t" \
"v_mul_f32 v41, v41, v35\n\t" \
"v_accvgpr_write_b32 a113, v41\n\t" \
"v_accvgpr_read_b32 v42, a114\n\t" \
"v_mul_f32 v42, v42, v35\n\t" \
"v_accvgpr_write_b32 a114, v42\n\t" \
"v_accvgpr_read_b32 v43, a115\n\t" \
"v_mul_f32 v43, v43, v35\n\t" \
"v_accvgpr_write_b32 a115, v43\n\t" \
"v_accvgpr_read_b32 v44, a116\n\t" \
"v_mul_f32 v44, v44, v35\n\t" \
"v_accvgpr_write_b32 a116, v44\n\t" \
"v_accvgpr_read_b32 v45, a117\n\t" \
"v_mul_f32 v45, v45, v35\n\t" \
"v_accvgpr_write_b32 a117, v45\n\t" \
"v_accvgpr_read_b32 v46, a118\n\t" \
"v_mul_f32 v46, v46, v35\n\t" \
"v_accvgpr_write_b32 a118, v46\n\t" \
"v_accvgpr_read_b32 v47, a119\n\t" \
"v_mul_f32 v47, v47, v35\n\t" \
"v_accvgpr_write_b32 a119, v47\n\t" \
"v_accvgpr_read_b32 v40, a120\n\t" \
"v_mul_f32 v40, v40, v35\n\t" \
"v_accvgpr_write_b32 a120, v40\n\t" \
"v_accvgpr_read_b32 v41, a121\n\t" \
"v_mul_f32 v41, v41, v35\n\t" \
"v_accvgpr_write_b32 a121, v41\n\t" \
"v_accvgpr_read_b32 v42, a122\n\t" \
"v_mul_f32 v42, v42, v35\n\t" \
"v_accvgpr_write_b32 a122, v42\n\t" \
"v_accvgpr_read_b32 v43, a123\n\t" \
"v_mul_f32 v43, v43, v35\n\t" \
"v_accvgpr_write_b32 a123, v43\n\t" \
"v_accvgpr_read_b32 v44, a124\n\t" \
"v_mul_f32 v44, v44, v35\n\t" \
"v_accvgpr_write_b32 a124, v44\n\t" \
"v_accvgpr_read_b32 v45, a125\n\t" \
"v_mul_f32 v45, v45, v35\n\t" \
"v_accvgpr_write_b32 a125, v45\n\t" \
"v_accvgpr_read_b32 v46, a126\n\t" \
"v_mul_f32 v46, v46, v35\n\t" \
"v_accvgpr_write_b32 a126, v46\n\t" \
"v_accvgpr_read_b32 v47, a127\n\t" \
"v_mul_f32 v47, v47, v35\n\t" \
"v_accvgpr_write_b32 a127, v47\n\t" \
"s_mov_b32 s50, 0\n\t" \
"s_nop 1\n\t" \
"s_branch RESCB_l1_%=\n\t" \
"DONE_%=:\n\t"
#define ATTN_ASM_CLOBBERS "v16", "v17", "v18", "v19", "v20", "v21", "v22", "v23", "v24", "v25", "v26", "v27", "v28", "v29", "v30", "v31", "v32", "v33", "v34", "v35", "v36", "v37", "v38", "v39", "v40", "v41", "v42", "v43", "v44", "v45", "v46", "v47", "v48", "v49", "v50", "v51", "v52", "v53", "v54", "v60", "v61", "v64", "v65", "v66", "v67", "v68", "v69", "v70", "v71", "v72", "v73", "v74", "v75", "v76", "v77", "v78", "v79", "v80", "v81", "v82", "v83", "v84", "v85", "v86", "v87", "v88", "v89", "v90", "v91", "v92", "v93", "v94", "v95", "v96", "v97", "v98", "v99", "v100", "v101", "v102", "v103", "v104", "v105", "v106", "v107", "v108", "v109", "v110", "v111", "v112", "v113", "v114", "v115", "v116", "v117", "v118", "v119", "v120", "v121", "v122", "v123", "v124", "v125", "v126", "v127", "v128", "v129", "v130", "v131", "v132", "v133", "v134", "v135", "v136", "v137", "v138", "v139", "v140", "v141", "v142", "v143", "v144", "v145", "v146", "v147", "v148", "v149", "v150", "v151", "v152", "v153", "v154", "v155", "v156", "v157", "v158", "v159", "v160", "v161", "v162", "v163", "v164", "v165", "v166", "v167", "v168", "v169", "v170", "v171", "v172", "v173", "v174", "v175", "v176", "v177", "v178", "v179", "v180", "v181", "v182", "v183", "v184", "v185", "v186", "v187", "v188", "v189", "v190", "v191", "v192", "v193", "v194", "v195", "v196", "v197", "v198", "v199", "v200", "v201", "v202", "v203", "v204", "v205", "v206", "v207", "v208", "v209", "v210", "v211", "v212", "v213", "v214", "v215", "v216", "v217", "v218", "v219", "v220", "v221", "v222", "v223", "v224", "v225", "v226", "v227", "v228", "v229", "v230", "v231", "v232", "v233", "v234", "v235", "v236", "v237", "v238", "v239", "v240", "v241", "v242", "v243", "v244", "v245", "v246", "v247", "v248", "v249", "v250", "v251", "v252", "v253", "v254", "v255", "a0", "a1", "a2", "a3", "a4", "a5", "a6", "a7", "a8", "a9", "a10", "a11", "a12", "a13", "a14", "a15", "a16", "a17", "a18", "a19", "a20", "a21", "a22", "a23", "a24", "a25", "a26", "a27", "a28", "a29", "a30", "a31", "a32", "a33", "a34", "a35", "a36", "a37", "a38", "a39", "a40", "a41", "a42", "a43", "a44", "a45", "a46", "a47", "a48", "a49", "a50", "a51", "a52", "a53", "a54", "a55", "a56", "a57", "a58", "a59", "a60", "a61", "a62", "a63", "a64", "a65", "a66", "a67", "a68", "a69", "a70", "a71", "a72", "a73", "a74", "a75", "a76", "a77", "a78", "a79", "a80", "a81", "a82", "a83", "a84", "a85", "a86", "a87", "a88", "a89", "a90", "a91", "a92", "a93", "a94", "a95", "a96", "a97", "a98", "a99", "a100", "a101", "a102", "a103", "a104", "a105", "a106", "a107", "a108", "a109", "a110", "a111", "a112", "a113", "a114", "a115", "a116", "a117", "a118", "a119", "a120", "a121", "a122", "a123", "a124", "a125", "a126", "a127", "a128", "a129", "a130", "a131", "a132", "a133", "a134", "a135", "a136", "a137", "a138", "a139", "a140", "a141", "a142", "a143", "a144", "a145", "a146", "a147", "a148", "a149", "a150", "a151", "a152", "a153", "a154", "a155", "a156", "a157", "a158", "a159", "a160", "a161", "a162", "a163", "a164", "a165", "a166", "a167", "a168", "a169", "a170", "a171", "a172", "a173", "a174", "a175", "a176", "a177", "a178", "a179", "a180", "a181", "a182", "a183", "a184", "a185", "a186", "a187", "a188", "a189", "a190", "a191", "a192", "a193", "a194", "a195", "a196", "a197", "a198", "a199", "a200", "a201", "a202", "a203", "a204", "a205", "a206", "a207", "a208", "a209", "a210", "a211", "a212", "a213", "a214", "a215", "a216", "a217", "a218", "a219", "a220", "a221", "a222", "a223", "a224", "a225", "a226", "a227", "a228", "a229", "a230", "a231", "a232", "a233", "a234", "a235", "a236", "a237", "a238", "a239", "a240", "a241", "a242", "a243", "a244", "a245", "a246", "a247", "a248", "a249", "a250", "a251", "a252", "a253", "a254", "a255", "s40", "s41", "s42", "s43", "s44", "s45", "s46", "s47", "s48", "s49", "s50", "s51", "s52", "s53", "s54", "s55", "s56", "s57", "s58", "s59", "s60", "s62", "s63", "s70", "s71", "s72", "s73", "s74", "s75", "s76", "s77", "s78", "s79", "s80", "s81", "s82", "s83", "m0", "vcc", "memory", "scc"

struct AttnArgs { char* ws; const bf16_t* xa; const bf16_t* wq; const bf16_t* wqkv; const bf16_t* ckv; const bf16_t* wuk; const bf16_t* wuv; const bf16_t* wo; };

__global__ __launch_bounds__(256, 1) void attn_kernel(AttnArgs p) {
    extern __shared__ __attribute__((aligned(16))) char smem_[];
    const int tid = threadIdx.x, wid = __builtin_amdgcn_readfirstlane(tid >> 6);
    const int xcd = blockIdx.x & 7, idx = blockIdx.x >> 3, h = xcd * 4 + (idx >> 3), pr = idx & 7;
    const int rr = wid & 1, c = wid >> 1;

    {
        const u32x4 srd = make_srd(p.ws, ATTN_SRD_BYTES);
        const u32x4 srdX = make_srd(p.xa, SEQ * DM * 2), srdWq = make_srd(p.wq + h * HD, (512 * 4096 - h * HD) * 2), srdWb = make_srd(p.wqkv + h * 16, (512 * 1536 - h * 16) * 2);
        const u32x4 srdC = make_srd(p.ckv, SEQ * DM * 2), srdWk = make_srd(p.wuk + h * HD, (512 * 4096 - h * HD) * 2), srdWv = make_srd(p.wuv + h * HD, (512 * 4096 - h * HD) * 2);
        const u32x4 srdWo = make_srd(p.wo, 4096 * 512 * 2);
        const unsigned ldsb = (unsigned)(size_t)smem_;
        const unsigned i_kb = (unsigned)(WS_K + (size_t)h * 32 * 16384 + wid * 8192), i_vb = (unsigned)(WS_V + (size_t)h * 32 * 16384 + wid * 8192);
        const unsigned i_ab = (unsigned)(WS_A + (size_t)h * 8 * 1024 + (size_t)(rr * 2 + c) * NK16 * 1024);
        const unsigned i_gb = (unsigned)(WS_G + (rr * 64 + c * 32) * 4);
        const unsigned i_T1 = 16 - pr, i_T2 = pr + 1, i_ldsw = ldsb + wid * 8192;
        const int lrb = (wid < 2 ? (15 - pr) : pr) * 4 + (wid & 1) * 2;
        const unsigned i_kvf = (unsigned)(WS_KVF + (size_t)h * KVF_STRIDE), i_pq = (unsigned)(WS_PQ + (size_t)(h * 64 + lrb) * 1024);
        const unsigned i_g2 = (unsigned)(WS_G + lrb * 128), i_al = (unsigned)(WS_A + ((size_t)lrb * NK16 + 256 + h) * 1024);
        const int ln = threadIdx.x & 63, l31 = ln & 31, hh = ln >> 5;
        const unsigned i_lane16 = ln * 16;
        const float i_fthr = ((float)(l31 - 4 * hh + 64 * (rr - c)) + 0.5f) * 1.2676506002282294e30f;
        unsigned voffA[2], voffW[2], voffB[2];
#pragma unroll
        for (int e = 0; e < 2; ++e) {
            const int pc = 2 * wid + e;
            voffA[e] = (unsigned)((8 * pc + (ln >> 3)) * 1024 + (((ln & 7) ^ ((4 * e + (ln >> 4)) & 7)) * 16));
            voffW[e] = (unsigned)((4 * pc + (ln >> 4)) * 8192 + (((ln & 15) ^ (((ln >> 4) << 2) | (pc & 3))) * 16));
            const int ch = (ln & 7) ^ (((ln >> 4) & 1) << 2), sec = ch >> 1;
            voffB[e] = (unsigned)((8 * pc + (ln >> 3)) * 3072 + (sec < 2 ? 0 : (sec - 1) * 1024) + (ch & 1) * 16);
        }
        const int tq = (ln & 15) >> 2, tp = ln & 3, tb = (ln >> 4) & 1;
        const unsigned i_abase = ldsb + (unsigned)(l31 * 128 + ((hh ^ ((l31 >> 1) & 7)) * 16));
        const unsigned i_wraw = ldsb + (unsigned)((8 * hh + tq) * 256 + (((2 * tb + (tp >> 1)) ^ ((tq << 2) | (2 * hh))) * 16) + 8 * (tp & 1));
        asm volatile(ATTN_ASM_TEXT
                     :
                     : "s"(srd), "s"(i_kb), "s"(i_vb), "s"(i_ab), "s"(i_gb), "s"(i_T1), "s"(i_T2), "s"(i_ldsw), "s"(ldsb),
                       "s"(i_kvf), "s"(i_pq), "s"(i_g2), "s"(i_al), "s"(srdX), "s"(srdWq), "s"(srdWb),
                       "v"(i_lane16), "v"(i_fthr), "v"(voffA[0]), "v"(voffA[1]), "v"(voffW[0]), "v"(voffW[1]), "v"(voffB[0]), "v"(voffB[1]), "v"(i_abase), "v"(i_wraw), "s"(srdC), "s"(srdWk), "s"(srdWv),
                       "s"(srdWo)
                     : ATTN_ASM_CLOBBERS);
    }
}

#define OUT_ASM_TEXT \
"s_mov_b32 s51, 0x20000\n\t" \
"s_mov_b32 s52, 0x400000\n\t" \
"s_mov_b32 s53, 0x80000\n\t" \
"v_xor_b32 v80, 0, %12\n\t" \
"v_xor_b32 v81, 16, %12\n\t" \
"v_xor_b32 v82, 32, %12\n\t" \
"v_xor_b32 v83, 48, %12\n\t" \
"s_add_u32 m0, %9, 0\n\t" \
"s_add_u32 s40, %7, 0\n\t" \
"buffer_load_dwordx4 %11, %4, s40 offen lds\n\t" \
"s_add_u32 m0, %9, 1024\n\t" \
"s_add_u32 s41, %7, 1024\n\t" \
"buffer_load_dwordx4 %11, %4, s41 offen lds\n\t" \
"s_add_u32 m0, %9, 2048\n\t" \
"s_add_u32 s40, %7, 2048\n\t" \
"buffer_load_dwordx4 %11, %4, s40 offen lds\n\t" \
"s_add_u32 m0, %9, 3072\n\t" \
"s_add_u32 s41, %7, 3072\n\t" \
"buffer_load_dwordx4 %11, %4, s41 offen lds\n\t" \
"s_cmp_ge_u32 0, %10\n\t" \
"s_cselect_b64 s[48:49], %6, %5\n\t" \
"s_cselect_b32 s50, s53, s52\n\t" \
"s_cselect_b32 s44, 0x400000, 0\n\t" \
"s_sub_u32 s44, %8, s44\n\t" \
"s_add_u32 m0, %9, 16384\n\t" \
"s_add_u32 s42, s44, 0\n\t" \
"buffer_load_dwordx4 v80, s[48:51], s42 offen lds\n\t" \
"s_add_u32 m0, %9, 17408\n\t" \
"s_add_u32 s43, s44, 4096\n\t" \
"buffer_load_dwordx4 v81, s[48:51], s43 offen lds\n\t" \
"s_add_u32 m0, %9, 18432\n\t" \
"s_add_u32 s42, s44, 8192\n\t" \
"buffer_load_dwordx4 v82, s[48:51], s42 offen lds\n\t" \
"s_add_u32 m0, %9, 19456\n\t" \
"s_add_u32 s43, s44, 12288\n\t" \
"buffer_load_dwordx4 v83, s[48:51], s43 offen lds\n\t" \
"v_add_u32 v84, 0, %13\n\t" \
"v_xor_b32 v88, 0, %14\n\t" \
"v_xor_b32 v89, 32, %14\n\t" \
"v_xor_b32 v90, 64, %14\n\t" \
"v_xor_b32 v91, 96, %14\n\t" \
"v_add_u32 v85, 65536, %13\n\t" \
"v_xor_b32 v92, 0, %14\n\t" \
"v_add_u32 v92, 65536, v92\n\t" \
"v_xor_b32 v93, 32, %14\n\t" \
"v_add_u32 v93, 65536, v93\n\t" \
"v_xor_b32 v94, 64, %14\n\t" \
"v_add_u32 v94, 65536, v94\n\t" \
"v_xor_b32 v95, 96, %14\n\t" \
"v_add_u32 v95, 65536, v95\n\t" \
"v_add_u32 v86, 131072, %13\n\t" \
"v_xor_b32 v96, 0, %14\n\t" \
"v_add_u32 v96, 131072, v96\n\t" \
"v_xor_b32 v97, 32, %14\n\t" \
"v_add_u32 v97, 131072, v97\n\t" \
"v_xor_b32 v98, 64, %14\n\t" \
"v_add_u32 v98, 131072, v98\n\t" \
"v_xor_b32 v99, 96, %14\n\t" \
"v_add_u32 v99, 131072, v99\n\t" \
"s_waitcnt vmcnt(0)\n\t" \
"s_barrier\n\t" \
"ds_read_b64_tr_b16 v[40:41], v88 offset:0\n\t" \
"ds_read_b64_tr_b16 v[42:43], v89 offset:2048\n\t" \
"ds_read_b128 v[32:35], v84 offset:0\n\t" \
"ds_read_b128 v[36:39], v84 offset:4096\n\t" \
"ds_read_b64_tr_b16 v[44:45], v90 offset:0\n\t" \
"ds_read_b64_tr_b16 v[46:47], v91 offset:2048\n\t" \
"ds_read_b64_tr_b16 v[56:57], v88 offset:4096\n\t" \
"ds_read_b64_tr_b16 v[58:59], v89 offset:6144\n\t" \
"ds_read_b128 v[48:51], v84 offset:1024\n\t" \
"ds_read_b128 v[52:55], v84 offset:5120\n\t" \
"ds_read_b64_tr_b16 v[60:61], v90 offset:4096\n\t" \
"ds_read_b64_tr_b16 v[62:63], v91 offset:6144\n\t" \
"s_waitcnt lgkmcnt(9)\n\t" \
"v_mfma_f32_32x32x16_bf16 %0, v[40:43], v[32:35], 0\n\t" \
"ds_read_b64_tr_b16 v[72:73], v88 offset:8192\n\t" \
"ds_read_b64_tr_b16 v[74:75], v89 offset:10240\n\t" \
"ds_read_b128 v[64:67], v84 offset:2048\n\t" \
"ds_read_b128 v[68:71], v84 offset:6144\n\t" \
"s_waitcnt lgkmcnt(12)\n\t" \
"v_mfma_f32_32x32x16_bf16 %1, v[40:43], v[36:39], 0\n\t" \
"ds_read_b64_tr_b16 v[76:77], v90 offset:8192\n\t" \
"ds_read_b64_tr_b16 v[78:79], v91 offset:10240\n\t" \
"s_add_u32 m0, %9, 32768\n\t" \
"s_add_u32 s40, %7, 4096\n\t" \
"buffer_load_dwordx4 %11, %4, s40 offen lds\n\t" \
"s_add_u32 m0, %9, 33792\n\t" \
"s_add_u32 s41, %7, 5120\n\t" \
"buffer_load_dwordx4 %11, %4, s41 offen lds\n\t" \
"s_waitcnt lgkmcnt(12)\n\t" \
"v_mfma_f32_32x32x16_bf16 %2, v[44:47], v[32:35], 0\n\t" \
"s_add_u32 m0, %9, 34816\n\t" \
"s_add_u32 s40, %7, 6144\n\t" \
"buffer_load_dwordx4 %11, %4, s40 offen lds\n\t" \
"s_add_u32 m0, %9, 35840\n\t" \
"s_add_u32 s41, %7, 7168\n\t" \
"buffer_load_dwordx4 %11, %4, s41 offen lds\n\t" \
"s_cmp_ge_u32 1, %10\n\t" \
"s_cselect_b64 s[48:49], %6, %5\n\t" \
"s_cselect_b32 s50, s53, s52\n\t" \
"s_cselect_b32 s44, 0x400000, 0\n\t" \
"s_sub_u32 s44, %8, s44\n\t" \
"s_add_u32 m0, %9, 49152\n\t" \
"s_add_u32 s42, s44, 65536\n\t" \
"buffer_load_dwordx4 v80, s[48:51], s42 offen lds\n\t" \
"v_mfma_f32_32x32x16_bf16 %3, v[44:47], v[36:39], 0\n\t" \
"s_add_u32 m0, %9, 50176\n\t" \
"s_add_u32 s43, s44, 69632\n\t" \
"buffer_load_dwordx4 v81, s[48:51], s43 offen lds\n\t" \
"s_add_u32 m0, %9, 51200\n\t" \
"s_add_u32 s42, s44, 73728\n\t" \
"buffer_load_dwordx4 v82, s[48:51], s42 offen lds\n\t" \
"s_add_u32 m0, %9, 52224\n\t" \
"s_add_u32 s43, s44, 77824\n\t" \
"buffer_load_dwordx4 v83, s[48:51], s43 offen lds\n\t" \
"s_waitcnt lgkmcnt(9)\n\t" \
"v_mfma_f32_32x32x16_bf16 %0, v[56:59], v[48:51], %0\n\t" \
"ds_read_b64_tr_b16 v[40:41], v88 offset:12288\n\t" \
"ds_read_b64_tr_b16 v[42:43], v89 offset:14336\n\t" \
"ds_read_b128 v[32:35], v84 offset:3072\n\t" \
"ds_read_b128 v[36:39], v84 offset:7168\n\t" \
"s_waitcnt lgkmcnt(12)\n\t" \
"v_mfma_f32_32x32x16_bf16 %1, v[56:59], v[52:55], %1\n\t" \
"ds_read_b64_tr_b16 v[44:45], v90 offset:12288\n\t" \
"ds_read_b64_tr_b16 v[46:47], v91 offset:14336\n\t" \
"s_add_u32 m0, %9, 65536\n\t" \
"s_add_u32 s40, %7, 8192\n\t" \
"buffer_load_dwordx4 %11, %4, s40 offen lds\n\t" \
"s_add_u32 m0, %9, 66560\n\t" \
"s_add_u32 s41, %7, 9216\n\t" \
"buffer_load_dwordx4 %11, %4, s41 offen lds\n\t" \
"s_waitcnt lgkmcnt(12)\n\t" \
"v_mfma_f32_32x32x16_bf16 %2, v[60:63], v[48:51], %2\n\t" \
"s_add_u32 m0, %9, 67584\n\t" \
"s_add_u32 s40, %7, 10240\n\t" \
"buffer_load_dwordx4 %11, %4, s40 offen lds\n\t" \
"s_add_u32 m0, %9, 68608\n\t" \
"s_add_u32 s41, %7, 11264\n\t" \
"buffer_load_dwordx4 %11, %4, s41 offen lds\n\t" \
"s_cmp_ge_u32 2, %10\n\t" \
"s_cselect_b64 s[48:49], %6, %5\n\t" \
"s_cselect_b32 s50, s53, s52\n\t" \
"s_cselect_b32 s44, 0x400000, 0\n\t" \
"s_sub_u32 s44, %8, s44\n\t" \
"s_add_u32 m0, %9, 81920\n\t" \
"s_add_u32 s42, s44, 131072\n\t" \
"buffer_load_dwordx4 v80, s[48:51], s42 offen lds\n\t" \
"v_mfma_f32_32x32x16_bf16 %3, v[60:63], v[52:55], %3\n\t" \
"s_add_u32 m0, %9, 82944\n\t" \
"s_add_u32 s43, s44, 135168\n\t" \
"buffer_load_dwordx4 v81, s[48:51], s43 offen lds\n\t" \
"s_add_u32 m0, %9, 83968\n\t" \
"s_add_u32 s42, s44, 139264\n\t" \
"buffer_load_dwordx4 v82, s[48:51], s42 offen lds\n\t" \
"s_add_u32 m0, %9, 84992\n\t" \
"s_add_u32 s43, s44, 143360\n\t" \
"buffer_load_dwordx4 v83, s[48:51], s43 offen lds\n\t" \
"s_waitcnt lgkmcnt(9)\n\t" \
"v_mfma_f32_32x32x16_bf16 %0, v[72:75], v[64:67], %0\n\t" \
"s_waitcnt vmcnt(8)\n\t" \
"s_barrier\n\t" \
"s_waitcnt lgkmcnt(8)\n\t" \
"v_mfma_f32_32x32x16_bf16 %1, v[72:75], v[68:71], %1\n\t" \
"ds_read_b64_tr_b16 v[56:57], v88 offset:32768\n\t" \
"ds_read_b64_tr_b16 v[58:59], v89 offset:34816\n\t" \
"ds_read_b128 v[48:51], v84 offset:32768\n\t" \
"ds_read_b128 v[52:55], v84 offset:36864\n\t" \
"ds_read_b64_tr_b16 v[60:61], v90 offset:32768\n\t" \
"s_waitcnt lgkmcnt(11)\n\t" \
"v_mfma_f32_32x32x16_bf16 %2, v[76:79], v[64:67], %2\n\t" \
"ds_read_b64_tr_b16 v[62:63], v91 offset:34816\n\t" \
"s_add_u32 m0, %9, 98304\n\t" \
"s_add_u32 s40, %7, 12288\n\t" \
"buffer_load_dwordx4 %11, %4, s40 offen lds\n\t" \
"s_add_u32 m0, %9, 99328\n\t" \
"s_add_u32 s41, %7, 13312\n\t" \
"buffer_load_dwordx4 %11, %4, s41 offen lds\n\t" \
"s_add_u32 m0, %9, 100352\n\t" \
"s_add_u32 s40, %7, 14336\n\t" \
"buffer_load_dwordx4 %11, %4, s40 offen lds\n\t" \
"s_add_u32 m0, %9, 101376\n\t" \
"s_add_u32 s41, %7, 15360\n\t" \
"buffer_load_dwordx4 %11, %4, s41 offen lds\n\t" \
"v_mfma_f32_32x32x16_bf16 %3, v[76:79], v[68:71], %3\n\t" \
"s_cmp_ge_u32 3, %10\n\t" \
"s_cselect_b64 s[48:49], %6, %5\n\t" \
"s_cselect_b32 s50, s53, s52\n\t" \
"s_cselect_b32 s44, 0x400000, 0\n\t" \
"s_sub_u32 s44, %8, s44\n\t" \
"s_add_u32 m0, %9, 114688\n\t" \
"s_add_u32 s42, s44, 196608\n\t" \
"buffer_load_dwordx4 v80, s[48:51], s42 offen lds\n\t" \
"s_add_u32 m0, %9, 115712\n\t" \
"s_add_u32 s43, s44, 200704\n\t" \
"buffer_load_dwordx4 v81, s[48:51], s43 offen lds\n\t" \
"s_add_u32 m0, %9, 116736\n\t" \
"s_add_u32 s42, s44, 204800\n\t" \
"buffer_load_dwordx4 v82, s[48:51], s42 offen lds\n\t" \
"s_add_u32 m0, %9, 117760\n\t" \
"s_add_u32 s43, s44, 208896\n\t" \
"buffer_load_dwordx4 v83, s[48:51], s43 offen lds\n\t" \
"s_waitcnt lgkmcnt(9)\n\t" \
"v_mfma_f32_32x32x16_bf16 %0, v[40:43], v[32:35], %0\n\t" \
"ds_read_b64_tr_b16 v[72:73], v88 offset:36864\n\t" \
"ds_read_b64_tr_b16 v[74:75], v89 offset:38912\n\t" \
"ds_read_b128 v[64:67], v84 offset:33792\n\t" \
"ds_read_b128 v[68:71], v84 offset:37888\n\t" \
"s_waitcnt lgkmcnt(12)\n\t" \
"v_mfma_f32_32x32x16_bf16 %1, v[40:43], v[36:39], %1\n\t" \
"ds_read_b64_tr_b16 v[76:77], v90 offset:36864\n\t" \
"ds_read_b64_tr_b16 v[78:79], v91 offset:38912\n\t" \
"s_add_u32 m0, %9, 131072\n\t" \
"s_add_u32 s40, %7, 16384\n\t" \
"buffer_load_dwordx4 %11, %4, s40 offen lds\n\t" \
"s_add_u32 m0, %9, 132096\n\t" \
"s_add_u32 s41, %7, 17408\n\t" \
"buffer_load_dwordx4 %11, %4, s41 offen lds\n\t" \
"s_waitcnt lgkmcnt(12)\n\t" \
"v_mfma_f32_32x32x16_bf16 %2, v[44:47], v[32:35], %2\n\t" \
"s_add_u32 m0, %9, 133120\n\t" \
"s_add_u32 s40, %7, 18432\n\t" \
"buffer_load_dwordx4 %11, %4, s40 offen lds\n\t" \
"s_add_u32 m0, %9, 134144\n\t" \
"s_add_u32 s41, %7, 19456\n\t" \
"buffer_load_dwordx4 %11, %4, s41 offen lds\n\t" \
"s_cmp_ge_u32 4, %10\n\t" \
"s_cselect_b64 s[48:49], %6, %5\n\t" \
"s_cselect_b32 s50, s53, s52\n\t" \
"s_cselect_b32 s44, 0x400000, 0\n\t" \
"s_sub_u32 s44, %8, s44\n\t" \
"s_add_u32 m0, %9, 147456\n\t" \
"s_add_u32 s42, s44, 262144\n\t" \
"buffer_load_dwordx4 v80, s[48:51], s42 offen lds\n\t" \
"v_mfma_f32_32x32x16_bf16 %3, v[44:47], v[36:39], %3\n\t" \
"s_add_u32 m0, %9, 148480\n\t" \
"s_add_u32 s43, s44, 266240\n\t" \
"buffer_load_dwordx4 v81, s[48:51], s43 offen lds\n\t" \
"s_add_u32 m0, %9, 149504\n\t" \
"s_add_u32 s42, s44, 270336\n\t" \
"buffer_load_dwordx4 v82, s[48:51], s42 offen lds\n\t" \
"s_add_u32 m0, %9, 150528\n\t" \
"s_add_u32 s43, s44, 274432\n\t" \
"buffer_load_dwordx4 v83, s[48:51], s43 offen lds\n\t" \
"s_waitcnt lgkmcnt(9)\n\t" \
"v_mfma_f32_32x32x16_bf16 %0, v[56:59], v[48:51], %0\n\t" \
"ds_read_b64_tr_b16 v[40:41], v88 offset:40960\n\t" \
"ds_read_b64_tr_b16 v[42:43], v89 offset:43008\n\t" \
"s_waitcnt lgkmcnt(10)\n\t" \
"v_mfma_f32_32x32x16_bf16 %1, v[56:59], v[52:55], %1\n\t" \
"ds_read_b128 v[32:35], v84 offset:34816\n\t" \
"s_waitcnt lgkmcnt(9)\n\t" \
"v_mfma_f32_32x32x16_bf16 %2, v[60:63], v[48:51], %2\n\t" \
"ds_read_b128 v[36:39], v84 offset:38912\n\t" \
"ds_read_b64_tr_b16 v[44:45], v90 offset:40960\n\t" \
"v_mfma_f32_32x32x16_bf16 %3, v[60:63], v[52:55], %3\n\t" \
"ds_read_b64_tr_b16 v[46:47], v91 offset:43008\n\t" \
"s_waitcnt lgkmcnt(9)\n\t" \
"v_mfma_f32_32x32x16_bf16 %0, v[72:75], v[64:67], %0\n\t" \
"ds_read_b64_tr_b16 v[56:57], v88 offset:45056\n\t" \
"ds_read_b64_tr_b16 v[58:59], v89 offset:47104\n\t" \
"s_waitcnt lgkmcnt(10)\n\t" \
"v_mfma_f32_32x32x16_bf16 %1, v[72:75], v[68:71], %1\n\t" \
"ds_read_b128 v[48:51], v84 offset:35840\n\t" \
"s_waitcnt lgkmcnt(9)\n\t" \
"v_mfma_f32_32x32x16_bf16 %2, v[76:79], v[64:67], %2\n\t" \
"ds_read_b128 v[52:55], v84 offset:39936\n\t" \
"ds_read_b64_tr_b16 v[60:61], v90 offset:45056\n\t" \
"v_mfma_f32_32x32x16_bf16 %3, v[76:79], v[68:71], %3\n\t" \
"ds_read_b64_tr_b16 v[62:63], v91 offset:47104\n\t" \
"s_waitcnt lgkmcnt(9)\n\t" \
"v_mfma_f32_32x32x16_bf16 %0, v[40:43], v[32:35], %0\n\t" \
"s_waitcnt vmcnt(16)\n\t" \
"s_barrier\n\t" \
"s_waitcnt lgkmcnt(8)\n\t" \
"v_mfma_f32_32x32x16_bf16 %1, v[40:43], v[36:39], %1\n\t" \
"ds_read_b64_tr_b16 v[72:73], v92 offset:0\n\t" \
"ds_read_b64_tr_b16 v[74:75], v93 offset:2048\n\t" \
"ds_read_b128 v[64:67], v85 offset:0\n\t" \
"s_waitcnt lgkmcnt(9)\n\t" \
"v_mfma_f32_32x32x16_bf16 %2, v[44:47], v[32:35], %2\n\t" \
"ds_read_b128 v[68:71], v85 offset:4096\n\t" \
"ds_read_b64_tr_b16 v[76:77], v94 offset:0\n\t" \
"ds_read_b64_tr_b16 v[78:79], v95 offset:2048\n\t" \
"v_mfma_f32_32x32x16_bf16 %3, v[44:47], v[36:39], %3\n\t" \
"s_add_u32 m0, %9, 0\n\t" \
"s_add_u32 s40, %7, 20480\n\t" \
"buffer_load_dwordx4 %11, %4, s40 offen lds\n\t" \
"s_add_u32 m0, %9, 1024\n\t" \
"s_add_u32 s41, %7, 21504\n\t" \
"buffer_load_dwordx4 %11, %4, s41 offen lds\n\t" \
"s_add_u32 m0, %9, 2048\n\t" \
"s_add_u32 s40, %7, 22528\n\t" \
"buffer_load_dwordx4 %11, %4, s40 offen lds\n\t" \
"s_waitcnt lgkmcnt(9)\n\t" \
"v_mfma_f32_32x32x16_bf16 %0, v[56:59], v[48:51], %0\n\t" \
"ds_read_b64_tr_b16 v[40:41], v92 offset:4096\n\t" \
"ds_read_b64_tr_b16 v[42:43], v93 offset:6144\n\t" \
"ds_read_b128 v[32:35], v85 offset:1024\n\t" \
"s_waitcnt lgkmcnt(11)\n\t" \
"v_mfma_f32_32x32x16_bf16 %1, v[56:59], v[52:55], %1\n\t" \
"ds_read_b128 v[36:39], v85 offset:5120\n\t" \
"ds_read_b64_tr_b16 v[44:45], v94 offset:4096\n\t" \
"s_waitcnt lgkmcnt(11)\n\t" \
"v_mfma_f32_32x32x16_bf16 %2, v[60:63], v[48:51], %2\n\t" \
"ds_read_b64_tr_b16 v[46:47], v95 offset:6144\n\t" \
"s_add_u32 m0, %9, 3072\n\t" \
"s_add_u32 s41, %7, 23552\n\t" \
"buffer_load_dwordx4 %11, %4, s41 offen lds\n\t" \
"v_mfma_f32_32x32x16_bf16 %3, v[60:63], v[52:55], %3\n\t" \
"s_cmp_ge_u32 5, %10\n\t" \
"s_cselect_b64 s[48:49], %6, %5\n\t" \
"s_cselect_b32 s50, s53, s52\n\t" \
"s_cselect_b32 s44, 0x400000, 0\n\t" \
"s_sub_u32 s44, %8, s44\n\t" \
"s_add_u32 m0, %9, 16384\n\t" \
"s_add_u32 s42, s44, 327680\n\t" \
"buffer_load_dwordx4 v80, s[48:51], s42 offen lds\n\t" \
"s_waitcnt lgkmcnt(9)\n\t" \
"v_mfma_f32_32x32x16_bf16 %0, v[72:75], v[64:67], %0\n\t" \
"ds_read_b64_tr_b16 v[56:57], v92 offset:8192\n\t" \
"ds_read_b64_tr_b16 v[58:59], v93 offset:10240\n\t" \
"ds_read_b128 v[48:51], v85 offset:2048\n\t" \
"s_waitcnt lgkmcnt(11)\n\t" \
"v_mfma_f32_32x32x16_bf16 %1, v[72:75], v[68:71], %1\n\t" \
"ds_read_b128 v[52:55], v85 offset:6144\n\t" \
"ds_read_b64_tr_b16 v[60:61], v94 offset:8192\n\t" \
"s_waitcnt lgkmcnt(11)\n\t" \
"v_mfma_f32_32x32x16_bf16 %2, v[76:79], v[64:67], %2\n\t" \
"ds_read_b64_tr_b16 v[62:63], v95 offset:10240\n\t" \
"s_add_u32 m0, %9, 17408\n\t" \
"s_add_u32 s43, s44, 331776\n\t" \
"buffer_load_dwordx4 v81, s[48:51], s43 offen lds\n\t" \
"v_mfma_f32_32x32x16_bf16 %3, v[76:79], v[68:71], %3\n\t" \
"s_add_u32 m0, %9, 18432\n\t" \
"s_add_u32 s42, s44, 335872\n\t" \
"buffer_load_dwordx4 v82, s[48:51], s42 offen lds\n\t" \
"s_add_u32 m0, %9, 19456\n\t" \
"s_add_u32 s43, s44, 339968\n\t" \
"buffer_load_dwordx4 v83, s[48:51], s43 offen lds\n\t" \
"s_waitcnt lgkmcnt(9)\n\t" \
"v_mfma_f32_32x32x16_bf16 %0, v[40:43], v[32:35], %0\n\t" \
"ds_read_b64_tr_b16 v[72:73], v92 offset:12288\n\t" \
"ds_read_b64_tr_b16 v[74:75], v93 offset:14336\n\t" \
"s_waitcnt lgkmcnt(10)\n\t" \
"v_mfma_f32_32x32x16_bf16 %1, v[40:43], v[36:39], %1\n\t" \
"ds_read_b128 v[64:67], v85 offset:3072\n\t" \
"s_waitcnt lgkmcnt(9)\n\t" \
"v_mfma_f32_32x32x16_bf16 %2, v[44:47], v[32:35], %2\n\t" \
"ds_read_b128 v[68:71], v85 offset:7168\n\t" \
"ds_read_b64_tr_b16 v[76:77], v94 offset:12288\n\t" \
"v_mfma_f32_32x32x16_bf16 %3, v[44:47], v[36:39], %3\n\t" \
"ds_read_b64_tr_b16 v[78:79], v95 offset:14336\n\t" \
"s_waitcnt lgkmcnt(9)\n\t" \
"v_mfma_f32_32x32x16_bf16 %0, v[56:59], v[48:51], %0\n\t" \
"s_waitcnt vmcnt(16)\n\t" \
"s_barrier\n\t" \
"s_waitcnt lgkmcnt(8)\n\t" \
"v_mfma_f32_32x32x16_bf16 %1, v[56:59], v[52:55], %1\n\t" \
"ds_read_b64_tr_b16 v[40:41], v92 offset:32768\n\t" \
"ds_read_b64_tr_b16 v[42:43], v93 offset:34816\n\t" \
"ds_read_b128 v[32:35], v85 offset:32768\n\t" \
"s_waitcnt lgkmcnt(9)\n\t" \
"v_mfma_f32_32x32x16_bf16 %2, v[60:63], v[48:51], %2\n\t" \
"ds_read_b128 v[36:39], v85 offset:36864\n\t" \
"ds_read_b64_tr_b16 v[44:45], v94 offset:32768\n\t" \
"ds_read_b64_tr_b16 v[46:47], v95 offset:34816\n\t" \
"v_mfma_f32_32x32x16_bf16 %3, v[60:63], v[52:55], %3\n\t" \
"s_add_u32 m0, %9, 32768\n\t" \
"s_add_u32 s40, %7, 24576\n\t" \
"buffer_load_dwordx4 %11, %4, s40 offen lds\n\t" \
"s_add_u32 m0, %9, 33792\n\t" \
"s_add_u32 s41, %7, 25600\n\t" \
"buffer_load_dwordx4 %11, %4, s41 offen lds\n\t" \
"s_add_u32 m0, %9, 34816\n\t" \
"s_add_u32 s40, %7, 26624\n\t" \
"buffer_load_dwordx4 %11, %4, s40 offen lds\n\t" \
"s_waitcnt lgkmcnt(9)\n\t" \
"v_mfma_f32_32x32x16_bf16 %0, v[72:75], v[64:67], %0\n\t" \
"ds_read_b64_tr_b16 v[56:57], v92 offset:36864\n\t" \
"ds_read_b64_tr_b16 v[58:59], v93 offset:38912\n\t" \
"ds_read_b128 v[48:51], v85 offset:33792\n\t" \
"s_waitcnt lgkmcnt(11)\n\t" \
"v_mfma_f32_32x32x16_bf16 %1, v[72:75], v[68:71], %1\n\t" \
"ds_read_b128 v[52:55], v85 offset:37888\n\t" \
"ds_read_b64_tr_b16 v[60:61], v94 offset:36864\n\t" \
"s_waitcnt lgkmcnt(11)\n\t" \
"v_mfma_f32_32x32x16_bf16 %2, v[76:79], v[64:67], %2\n\t" \
"ds_read_b64_tr_b16 v[62:63], v95 offset:38912\n\t" \
"s_add_u32 m0, %9, 35840\n\t" \
"s_add_u32 s41, %7, 27648\n\t" \
"buffer_load_dwordx4 %11, %4, s41 offen lds\n\t" \
"v_mfma_f32_32x32x16_bf16 %3, v[76:79], v[68:71], %3\n\t" \
"s_cmp_ge_u32 6, %10\n\t" \
"s_cselect_b64 s[48:49], %6, %5\n\t" \
"s_cselect_b32 s50, s53, s52\n\t" \
"s_cselect_b32 s44, 0x400000, 0\n\t" \
"s_sub_u32 s44, %8, s44\n\t" \
"s_add_u32 m0, %9, 49152\n\t" \
"s_add_u32 s42, s44, 393216\n\t" \
"buffer_load_dwordx4 v80, s[48:51], s42 offen lds\n\t" \
"s_waitcnt lgkmcnt(9)\n\t" \
"v_mfma_f32_32x32x16_bf16 %0, v[40:43], v[32:35], %0\n\t" \
"ds_read_b64_tr_b16 v[72:73], v92 offset:40960\n\t" \
"ds_read_b64_tr_b16 v[74:75], v93 offset:43008\n\t" \
"ds_read_b128 v[64:67], v85 offset:34816\n\t" \
"s_waitcnt lgkmcnt(11)\n\t" \
"v_mfma_f32_32x32x16_bf16 %1, v[40:43], v[36:39], %1\n\t" \
"ds_read_b128 v[68:71], v85 offset:38912\n\t" \
"ds_read_b64_tr_b16 v[76:77], v94 offset:40960\n\t" \
"s_waitcnt lgkmcnt(11)\n\t" \
"v_mfma_f32_32x32x16_bf16 %2, v[44:47], v[32:35], %2\n\t" \
"ds_read_b64_tr_b16 v[78:79], v95 offset:43008\n\t" \
"s_add_u32 m0, %9, 50176\n\t" \
"s_add_u32 s43, s44, 397312\n\t" \
"buffer_load_dwordx4 v81, s[48:51], s43 offen lds\n\t" \
"v_mfma_f32_32x32x16_bf16 %3, v[44:47], v[36:39], %3\n\t" \
"s_add_u32 m0, %9, 51200\n\t" \
"s_add_u32 s42, s44, 401408\n\t" \
"buffer_load_dwordx4 v82, s[48:51], s42 offen lds\n\t" \
"s_add_u32 m0, %9, 52224\n\t" \
"s_add_u32 s43, s44, 405504\n\t" \
"buffer_load_dwordx4 v83, s[48:51], s43 offen lds\n\t" \
"s_waitcnt lgkmcnt(9)\n\t" \
"v_mfma_f32_32x32x16_bf16 %0, v[56:59], v[48:51], %0\n\t" \
"ds_read_b64_tr_b16 v[40:41], v92 offset:45056\n\t" \
"ds_read_b64_tr_b16 v[42:43], v93 offset:47104\n\t" \
"s_waitcnt lgkmcnt(10)\n\t" \
"v_mfma_f32_32x32x16_bf16 %1, v[56:59], v[52:55], %1\n\t" \
"ds_read_b128 v[32:35], v85 offset:35840\n\t" \
"s_waitcnt lgkmcnt(9)\n\t" \
"v_mfma_f32_32x32x16_bf16 %2, v[60:63], v[48:51], %2\n\t" \
"ds_read_b128 v[36:39], v85 offset:39936\n\t" \
"ds_read_b64_tr_b16 v[44:45], v94 offset:45056\n\t" \
"v_mfma_f32_32x32x16_bf16 %3, v[60:63], v[52:55], %3\n\t" \
"ds_read_b64_tr_b16 v[46:47], v95 offset:47104\n\t" \
"s_waitcnt lgkmcnt(9)\n\t" \
"v_mfma_f32_32x32x16_bf16 %0, v[72:75], v[64:67], %0\n\t" \
"s_waitcnt vmcnt(16)\n\t" \
"s_barrier\n\t" \
"s_waitcnt lgkmcnt(8)\n\t" \
"v_mfma_f32_32x32x16_bf16 %1, v[72:75], v[68:71], %1\n\t" \
"ds_read_b64_tr_b16 v[56:57], v96 offset:0\n\t" \
"ds_read_b64_tr_b16 v[58:59], v97 offset:2048\n\t" \
"ds_read_b128 v[48:51], v86 offset:0\n\t" \
"s_waitcnt lgkmcnt(9)\n\t" \
"v_mfma_f32_32x32x16_bf16 %2, v[76:79], v[64:67], %2\n\t" \
"ds_read_b128 v[52:55], v86 offset:4096\n\t" \
"ds_read_b64_tr_b16 v[60:61], v98 offset:0\n\t" \
"ds_read_b64_tr_b16 v[62:63], v99 offset:2048\n\t" \
"v_mfma_f32_32x32x16_bf16 %3, v[76:79], v[68:71], %3\n\t" \
"s_add_u32 m0, %9, 65536\n\t" \
"s_add_u32 s40, %7, 28672\n\t" \
"buffer_load_dwordx4 %11, %4, s40 offen lds\n\t" \
"s_add_u32 m0, %9, 66560\n\t" \
"s_add_u32 s41, %7, 29696\n\t" \
"buffer_load_dwordx4 %11, %4, s41 offen lds\n\t" \
"s_add_u32 m0, %9, 67584\n\t" \
"s_add_u32 s40, %7, 30720\n\t" \
"buffer_load_dwordx4 %11, %4, s40 offen lds\n\t" \
"s_waitcnt lgkmcnt(9)\n\t" \
"v_mfma_f32_32x32x16_bf16 %0, v[40:43], v[32:35], %0\n\t" \
"ds_read_b64_tr_b16 v[72:73], v96 offset:4096\n\t" \
"ds_read_b64_tr_b16 v[74:75], v97 offset:6144\n\t" \
"ds_read_b128 v[64:67], v86 offset:1024\n\t" \
"s_waitcnt lgkmcnt(11)\n\t" \
"v_mfma_f32_32x32x16_bf16 %1, v[40:43], v[36:39], %1\n\t" \
"ds_read_b128 v[68:71], v86 offset:5120\n\t" \
"ds_read_b64_tr_b16 v[76:77], v98 offset:4096\n\t" \
"s_waitcnt lgkmcnt(11)\n\t" \
"v_mfma_f32_32x32x16_bf16 %2, v[44:47], v[32:35], %2\n\t" \
"ds_read_b64_tr_b16 v[78:79], v99 offset:6144\n\t" \
"s_add_u32 m0, %9, 68608\n\t" \
"s_add_u32 s41, %7, 31744\n\t" \
"buffer_load_dwordx4 %11, %4, s41 offen lds\n\t" \
"v_mfma_f32_32x32x16_bf16 %3, v[44:47], v[36:39], %3\n\t" \
"s_cmp_ge_u32 7, %10\n\t" \
"s_cselect_b64 s[48:49], %6, %5\n\t" \
"s_cselect_b32 s50, s53, s52\n\t" \
"s_cselect_b32 s44, 0x400000, 0\n\t" \
"s_sub_u32 s44, %8, s44\n\t" \
"s_add_u32 m0, %9, 81920\n\t" \
"s_add_u32 s42, s44, 458752\n\t" \
"buffer_load_dwordx4 v80, s[48:51], s42 offen lds\n\t" \
"s_waitcnt lgkmcnt(9)\n\t" \
"v_mfma_f32_32x32x16_bf16 %0, v[56:59], v[48:51], %0\n\t" \
"ds_read_b64_tr_b16 v[40:41], v96 offset:8192\n\t" \
"ds_read_b64_tr_b16 v[42:43], v97 offset:10240\n\t" \
"ds_read_b128 v[32:35], v86 offset:2048\n\t" \
"s_waitcnt lgkmcnt(11)\n\t" \
"v_mfma_f32_32x32x16_bf16 %1, v[56:59], v[52:55], %1\n\t" \
"ds_read_b128 v[36:39], v86 offset:6144\n\t" \
"ds_read_b64_tr_b16 v[44:45], v98 offset:8192\n\t" \
"s_waitcnt lgkmcnt(11)\n\t" \
"v_mfma_f32_32x32x16_bf16 %2, v[60:63], v[48:51], %2\n\t" \
"ds_read_b64_tr_b16 v[46:47], v99 offset:10240\n\t" \
"s_add_u32 m0, %9, 82944\n\t" \
"s_add_u32 s43, s44, 462848\n\t" \
"buffer_load_dwordx4 v81, s[48:51], s43 offen lds\n\t" \
"v_mfma_f32_32x32x16_bf16 %3, v[60:63], v[52:55], %3\n\t" \
"s_add_u32 m0, %9, 83968\n\t" \
"s_add_u32 s42, s44, 466944\n\t" \
"buffer_load_dwordx4 v82, s[48:51], s42 offen lds\n\t" \
"s_add_u32 m0, %9, 84992\n\t" \
"s_add_u32 s43, s44, 471040\n\t" \
"buffer_load_dwordx4 v83, s[48:51], s43 offen lds\n\t" \
"s_waitcnt lgkmcnt(9)\n\t" \
"v_mfma_f32_32x32x16_bf16 %0, v[72:75], v[64:67], %0\n\t" \
"ds_read_b64_tr_b16 v[56:57], v96 offset:12288\n\t" \
"ds_read_b64_tr_b16 v[58:59], v97 offset:14336\n\t" \
"s_waitcnt lgkmcnt(10)\n\t" \
"v_mfma_f32_32x32x16_bf16 %1, v[72:75], v[68:71], %1\n\t" \
"ds_read_b128 v[48:51], v86 offset:3072\n\t" \
"s_waitcnt lgkmcnt(9)\n\t" \
"v_mfma_f32_32x32x16_bf16 %2, v[76:79], v[64:67], %2\n\t" \
"ds_read_b128 v[52:55], v86 offset:7168\n\t" \
"ds_read_b64_tr_b16 v[60:61], v98 offset:12288\n\t" \
"v_mfma_f32_32x32x16_bf16 %3, v[76:79], v[68:71], %3\n\t" \
"ds_read_b64_tr_b16 v[62:63], v99 offset:14336\n\t" \
"s_waitcnt lgkmcnt(9)\n\t" \
"v_mfma_f32_32x32x16_bf16 %0, v[40:43], v[32:35], %0\n\t" \
"s_waitcnt vmcnt(16)\n\t" \
"s_barrier\n\t" \
"s_waitcnt lgkmcnt(8)\n\t" \
"v_mfma_f32_32x32x16_bf16 %1, v[40:43], v[36:39], %1\n\t" \
"ds_read_b64_tr_b16 v[72:73], v88 offset:0\n\t" \
"ds_read_b64_tr_b16 v[74:75], v89 offset:2048\n\t" \
"ds_read_b128 v[64:67], v84 offset:0\n\t" \
"s_waitcnt lgkmcnt(9)\n\t" \
"v_mfma_f32_32x32x16_bf16 %2, v[44:47], v[32:35], %2\n\t" \
"ds_read_b128 v[68:71], v84 offset:4096\n\t" \
"ds_read_b64_tr_b16 v[76:77], v90 offset:0\n\t" \
"ds_read_b64_tr_b16 v[78:79], v91 offset:2048\n\t" \
"v_mfma_f32_32x32x16_bf16 %3, v[44:47], v[36:39], %3\n\t" \
"s_add_u32 m0, %9, 98304\n\t" \
"s_add_u32 s40, %7, 32768\n\t" \
"buffer_load_dwordx4 %11, %4, s40 offen lds\n\t" \
"s_add_u32 m0, %9, 99328\n\t" \
"s_add_u32 s41, %7, 33792\n\t" \
"buffer_load_dwordx4 %11, %4, s41 offen lds\n\t" \
"s_add_u32 m0, %9, 100352\n\t" \
"s_add_u32 s40, %7, 34816\n\t" \
"buffer_load_dwordx4 %11, %4, s40 offen lds\n\t" \
"s_waitcnt lgkmcnt(9)\n\t" \
"v_mfma_f32_32x32x16_bf16 %0, v[56:59], v[48:51], %0\n\t" \
"ds_read_b64_tr_b16 v[40:41], v88 offset:4096\n\t" \
"ds_read_b64_tr_b16 v[42:43], v89 offset:6144\n\t" \
"ds_read_b128 v[32:35], v84 offset:1024\n\t" \
"s_waitcnt lgkmcnt(11)\n\t" \
"v_mfma_f32_32x32x16_bf16 %1, v[56:59], v[52:55], %1\n\t" \
"ds_read_b128 v[36:39], v84 offset:5120\n\t" \
"ds_read_b64_tr_b16 v[44:45], v90 offset:4096\n\t" \
"s_waitcnt lgkmcnt(11)\n\t" \
"v_mfma_f32_32x32x16_bf16 %2, v[60:63], v[48:51], %2\n\t" \
"ds_read_b64_tr_b16 v[46:47], v91 offset:6144\n\t" \
"s_add_u32 m0, %9, 101376\n\t" \
"s_add_u32 s41, %7, 35840\n\t" \
"buffer_load_dwordx4 %11, %4, s41 offen lds\n\t" \
"v_mfma_f32_32x32x16_bf16 %3, v[60:63], v[52:55], %3\n\t" \
"s_cmp_ge_u32 8, %10\n\t" \
"s_cselect_b64 s[48:49], %6, %5\n\t" \
"s_cselect_b32 s50, s53, s52\n\t" \
"s_cselect_b32 s44, 0x400000, 0\n\t" \
"s_sub_u32 s44, %8, s44\n\t" \
"s_add_u32 m0, %9, 114688\n\t" \
"s_add_u32 s42, s44, 524288\n\t" \
"buffer_load_dwordx4 v80, s[48:51], s42 offen lds\n\t" \
"s_waitcnt lgkmcnt(9)\n\t" \
"v_mfma_f32_32x32x16_bf16 %0, v[72:75], v[64:67], %0\n\t" \
"ds_read_b64_tr_b16 v[56:57], v88 offset:8192\n\t" \
"ds_read_b64_tr_b16 v[58:59], v89 offset:10240\n\t" \
"ds_read_b128 v[48:51], v84 offset:2048\n\t" \
"s_waitcnt lgkmcnt(11)\n\t" \
"v_mfma_f32_32x32x16_bf16 %1, v[72:75], v[68:71], %1\n\t" \
"ds_read_b128 v[52:55], v84 offset:6144\n\t" \
"ds_read_b64_tr_b16 v[60:61], v90 offset:8192\n\t" \
"s_waitcnt lgkmcnt(11)\n\t" \
"v_mfma_f32_32x32x16_bf16 %2, v[76:79], v[64:67], %2\n\t" \
"ds_read_b64_tr_b16 v[62:63], v91 offset:10240\n\t" \
"s_add_u32 m0, %9, 115712\n\t" \
"s_add_u32 s43, s44, 528384\n\t" \
"buffer_load_dwordx4 v81, s[48:51], s43 offen lds\n\t" \
"v_mfma_f32_32x32x16_bf16 %3, v[76:79], v[68:71], %3\n\t" \
"s_add_u32 m0, %9, 116736\n\t" \
"s_add_u32 s42, s44, 532480\n\t" \
"buffer_load_dwordx4 v82, s[48:51], s42 offen lds\n\t" \
"s_add_u32 m0, %9, 117760\n\t" \
"s_add_u32 s43, s44, 536576\n\t" \
"buffer_load_dwordx4 v83, s[48:51], s43 offen lds\n\t" \
"s_waitcnt lgkmcnt(9)\n\t" \
"v_mfma_f32_32x32x16_bf16 %0, v[40:43], v[32:35], %0\n\t" \
"ds_read_b64_tr_b16 v[72:73], v88 offset:12288\n\t" \
"ds_read_b64_tr_b16 v[74:75], v89 offset:14336\n\t" \
"s_waitcnt lgkmcnt(10)\n\t" \
"v_mfma_f32_32x32x16_bf16 %1, v[40:43], v[36:39], %1\n\t" \
"ds_read_b128 v[64:67], v84 offset:3072\n\t" \
"s_waitcnt lgkmcnt(9)\n\t" \
"v_mfma_f32_32x32x16_bf16 %2, v[44:47], v[32:35], %2\n\t" \
"ds_read_b128 v[68:71], v84 offset:7168\n\t" \
"ds_read_b64_tr_b16 v[76:77], v90 offset:12288\n\t" \
"v_mfma_f32_32x32x16_bf16 %3, v[44:47], v[36:39], %3\n\t" \
"ds_read_b64_tr_b16 v[78:79], v91 offset:14336\n\t" \
"s_waitcnt lgkmcnt(9)\n\t" \
"v_mfma_f32_32x32x16_bf16 %0, v[56:59], v[48:51], %0\n\t" \
"s_waitcnt vmcnt(16)\n\t" \
"s_barrier\n\t" \
"s_waitcnt lgkmcnt(8)\n\t" \
"v_mfma_f32_32x32x16_bf16 %1, v[56:59], v[52:55], %1\n\t" \
"ds_read_b64_tr_b16 v[40:41], v88 offset:32768\n\t" \
"ds_read_b64_tr_b16 v[42:43], v89 offset:34816\n\t" \
"ds_read_b128 v[32:35], v84 offset:32768\n\t" \
"s_waitcnt lgkmcnt(9)\n\t" \
"v_mfma_f32_32x32x16_bf16 %2, v[60:63], v[48:51], %2\n\t" \
"ds_read_b128 v[36:39], v84 offset:36864\n\t" \
"ds_read_b64_tr_b16 v[44:45], v90 offset:32768\n\t" \
"ds_read_b64_tr_b16 v[46:47], v91 offset:34816\n\t" \
"v_mfma_f32_32x32x16_bf16 %3, v[60:63], v[52:55], %3\n\t" \
"s_add_u32 m0, %9, 131072\n\t" \
"s_add_u32 s40, %7, 36864\n\t" \
"buffer_load_dwordx4 %11, %4, s40 offen lds\n\t" \
"s_add_u32 m0, %9, 132096\n\t" \
"s_add_u32 s41, %7, 37888\n\t" \
"buffer_load_dwordx4 %11, %4, s41 offen lds\n\t" \
"s_add_u32 m0, %9, 133120\n\t" \
"s_add_u32 s40, %7, 38912\n\t" \
"buffer_load_dwordx4 %11, %4, s40 offen lds\n\t" \
"s_waitcnt lgkmcnt(9)\n\t" \
"v_mfma_f32_32x32x16_bf16 %0, v[72:75], v[64:67], %0\n\t" \
"ds_read_b64_tr_b16 v[56:57], v88 offset:36864\n\t" \
"ds_read_b64_tr_b16 v[58:59], v89 offset:38912\n\t" \
"ds_read_b128 v[48:51], v84 offset:33792\n\t" \
"s_waitcnt lgkmcnt(11)\n\t" \
"v_mfma_f32_32x32x16_bf16 %1, v[72:75], v[68:71], %1\n\t" \
"ds_read_b128 v[52:55], v84 offset:37888\n\t" \
"ds_read_b64_tr_b16 v[60:61], v90 offset:36864\n\t" \
"s_waitcnt lgkmcnt(11)\n\t" \
"v_mfma_f32_32x32x16_bf16 %2, v[76:79], v[64:67], %2\n\t" \
"ds_read_b64_tr_b16 v[62:63], v91 offset:38912\n\t" \
"s_add_u32 m0, %9, 134144\n\t" \
"s_add_u32 s41, %7, 39936\n\t" \
"buffer_load_dwordx4 %11, %4, s41 offen lds\n\t" \
"v_mfma_f32_32x32x16_bf16 %3, v[76:79], v[68:71], %3\n\t" \
"s_cmp_ge_u32 9, %10\n\t" \
"s_cselect_b64 s[48:49], %6, %5\n\t" \
"s_cselect_b32 s50, s53, s52\n\t" \
"s_cselect_b32 s44, 0x400000, 0\n\t" \
"s_sub_u32 s44, %8, s44\n\t" \
"s_add_u32 m0, %9, 147456\n\t" \
"s_add_u32 s42, s44, 589824\n\t" \
"buffer_load_dwordx4 v80, s[48:51], s42 offen lds\n\t" \
"s_waitcnt lgkmcnt(9)\n\t" \
"v_mfma_f32_32x32x16_bf16 %0, v[40:43], v[32:35], %0\n\t" \
"ds_read_b64_tr_b16 v[72:73], v88 offset:40960\n\t" \
"ds_read_b64_tr_b16 v[74:75], v89 offset:43008\n\t" \
"ds_read_b128 v[64:67], v84 offset:34816\n\t" \
"s_waitcnt lgkmcnt(11)\n\t" \
"v_mfma_f32_32x32x16_bf16 %1, v[40:43], v[36:39], %1\n\t" \
"ds_read_b128 v[68:71], v84 offset:38912\n\t" \
"ds_read_b64_tr_b16 v[76:77], v90 offset:40960\n\t" \
"s_waitcnt lgkmcnt(11)\n\t" \
"v_mfma_f32_32x32x16_bf16 %2, v[44:47], v[32:35], %2\n\t" \
"ds_read_b64_tr_b16 v[78:79], v91 offset:43008\n\t" \
"s_add_u32 m0, %9, 148480\n\t" \
"s_add_u32 s43, s44, 593920\n\t" \
"buffer_load_dwordx4 v81, s[48:51], s43 offen lds\n\t" \
"v_mfma_f32_32x32x16_bf16 %3, v[44:47], v[36:39], %3\n\t" \
"s_add_u32 m0, %9, 149504\n\t" \
"s_add_u32 s42, s44, 598016\n\t" \
"buffer_load_dwordx4 v82, s[48:51], s42 offen lds\n\t" \
"s_add_u32 m0, %9, 150528\n\t" \
"s_add_u32 s43, s44, 602112\n\t" \
"buffer_load_dwordx4 v83, s[48:51], s43 offen lds\n\t" \
"s_waitcnt lgkmcnt(9)\n\t" \
"v_mfma_f32_32x32x16_bf16 %0, v[56:59], v[48:51], %0\n\t" \
"ds_read_b64_tr_b16 v[40:41], v88 offset:45056\n\t" \
"ds_read_b64_tr_b16 v[42:43], v89 offset:47104\n\t" \
"s_waitcnt lgkmcnt(10)\n\t" \
"v_mfma_f32_32x32x16_bf16 %1, v[56:59], v[52:55], %1\n\t" \
"ds_read_b128 v[32:35], v84 offset:35840\n\t" \
"s_waitcnt lgkmcnt(9)\n\t" \
"v_mfma_f32_32x32x16_bf16 %2, v[60:63], v[48:51], %2\n\t" \
"ds_read_b128 v[36:39], v84 offset:39936\n\t" \
"ds_read_b64_tr_b16 v[44:45], v90 offset:45056\n\t" \
"v_mfma_f32_32x32x16_bf16 %3, v[60:63], v[52:55], %3\n\t" \
"ds_read_b64_tr_b16 v[46:47], v91 offset:47104\n\t" \
"s_waitcnt lgkmcnt(9)\n\t" \
"v_mfma_f32_32x32x16_bf16 %0, v[72:75], v[64:67], %0\n\t" \
"s_waitcnt vmcnt(16)\n\t" \
"s_barrier\n\t" \
"s_waitcnt lgkmcnt(8)\n\t" \
"v_mfma_f32_32x32x16_bf16 %1, v[72:75], v[68:71], %1\n\t" \
"ds_read_b64_tr_b16 v[56:57], v92 offset:0\n\t" \
"ds_read_b64_tr_b16 v[58:59], v93 offset:2048\n\t" \
"ds_read_b128 v[48:51], v85 offset:0\n\t" \
"s_waitcnt lgkmcnt(9)\n\t" \
"v_mfma_f32_32x32x16_bf16 %2, v[76:79], v[64:67], %2\n\t" \
"ds_read_b128 v[52:55], v85 offset:4096\n\t" \
"ds_read_b64_tr_b16 v[60:61], v94 offset:0\n\t" \
"ds_read_b64_tr_b16 v[62:63], v95 offset:2048\n\t" \
"v_mfma_f32_32x32x16_bf16 %3, v[76:79], v[68:71], %3\n\t" \
"s_add_u32 m0, %9, 0\n\t" \
"s_add_u32 s40, %7, 40960\n\t" \
"buffer_load_dwordx4 %11, %4, s40 offen lds\n\t" \
"s_add_u32 m0, %9, 1024\n\t" \
"s_add_u32 s41, %7, 41984\n\t" \
"buffer_load_dwordx4 %11, %4, s41 offen lds\n\t" \
"s_add_u32 m0, %9, 2048\n\t" \
"s_add_u32 s40, %7, 43008\n\t" \
"buffer_load_dwordx4 %11, %4, s40 offen lds\n\t" \
"s_waitcnt lgkmcnt(9)\n\t" \
"v_mfma_f32_32x32x16_bf16 %0, v[40:43], v[32:35], %0\n\t" \
"ds_read_b64_tr_b16 v[72:73], v92 offset:4096\n\t" \
"ds_read_b64_tr_b16 v[74:75], v93 offset:6144\n\t" \
"ds_read_b128 v[64:67], v85 offset:1024\n\t" \
"s_waitcnt lgkmcnt(11)\n\t" \
"v_mfma_f32_32x32x16_bf16 %1, v[40:43], v[36:39], %1\n\t" \
"ds_read_b128 v[68:71], v85 offset:5120\n\t" \
"ds_read_b64_tr_b16 v[76:77], v94 offset:4096\n\t" \
"s_waitcnt lgkmcnt(11)\n\t" \
"v_mfma_f32_32x32x16_bf16 %2, v[44:47], v[32:35], %2\n\t" \
"ds_read_b64_tr_b16 v[78:79], v95 offset:6144\n\t" \
"s_add_u32 m0, %9, 3072\n\t" \
"s_add_u32 s41, %7, 44032\n\t" \
"buffer_load_dwordx4 %11, %4, s41 offen lds\n\t" \
"v_mfma_f32_32x32x16_bf16 %3, v[44:47], v[36:39], %3\n\t" \
"s_cmp_ge_u32 10, %10\n\t" \
"s_cselect_b64 s[48:49], %6, %5\n\t" \
"s_cselect_b32 s50, s53, s52\n\t" \
"s_cselect_b32 s44, 0x400000, 0\n\t" \
"s_sub_u32 s44, %8, s44\n\t" \
"s_add_u32 m0, %9, 16384\n\t" \
"s_add_u32 s42, s44, 655360\n\t" \
"buffer_load_dwordx4 v80, s[48:51], s42 offen lds\n\t" \
"s_waitcnt lgkmcnt(9)\n\t" \
"v_mfma_f32_32x32x16_bf16 %0, v[56:59], v[48:51], %0\n\t" \
"ds_read_b64_tr_b16 v[40:41], v92 offset:8192\n\t" \
"ds_read_b64_tr_b16 v[42:43], v93 offset:10240\n\t" \
"ds_read_b128 v[32:35], v85 offset:2048\n\t" \
"s_waitcnt lgkmcnt(11)\n\t" \
"v_mfma_f32_32x32x16_bf16 %1, v[56:59], v[52:55], %1\n\t" \
"ds_read_b128 v[36:39], v85 offset:6144\n\t" \
"ds_read_b64_tr_b16 v[44:45], v94 offset:8192\n\t" \
"s_waitcnt lgkmcnt(11)\n\t" \
"v_mfma_f32_32x32x16_bf16 %2, v[60:63], v[48:51], %2\n\t" \
"ds_read_b64_tr_b16 v[46:47], v95 offset:10240\n\t" \
"s_add_u32 m0, %9, 17408\n\t" \
"s_add_u32 s43, s44, 659456\n\t" \
"buffer_load_dwordx4 v81, s[48:51], s43 offen lds\n\t" \
"v_mfma_f32_32x32x16_bf16 %3, v[60:63], v[52:55], %3\n\t" \
"s_add_u32 m0, %9, 18432\n\t" \
"s_add_u32 s42, s44, 663552\n\t" \
"buffer_load_dwordx4 v82, s[48:51], s42 offen lds\n\t" \
"s_add_u32 m0, %9, 19456\n\t" \
"s_add_u32 s43, s44, 667648\n\t" \
"buffer_load_dwordx4 v83, s[48:51], s43 offen lds\n\t" \
"s_waitcnt lgkmcnt(9)\n\t" \
"v_mfma_f32_32x32x16_bf16 %0, v[72:75], v[64:67], %0\n\t" \
"ds_read_b64_tr_b16 v[56:57], v92 offset:12288\n\t" \
"ds_read_b64_tr_b16 v[58:59], v93 offset:14336\n\t" \
"s_waitcnt lgkmcnt(10)\n\t" \
"v_mfma_f32_32x32x16_bf16 %1, v[72:75], v[68:71], %1\n\t" \
"ds_read_b128 v[48:51], v85 offset:3072\n\t" \
"s_waitcnt lgkmcnt(9)\n\t" \
"v_mfma_f32_32x32x16_bf16 %2, v[76:79], v[64:67], %2\n\t" \
"ds_read_b128 v[52:55], v85 offset:7168\n\t" \
"ds_read_b64_tr_b16 v[60:61], v94 offset:12288\n\t" \
"v_mfma_f32_32x32x16_bf16 %3, v[76:79], v[68:71], %3\n\t" \
"ds_read_b64_tr_b16 v[62:63], v95 offset:14336\n\t" \
"s_waitcnt lgkmcnt(9)\n\t" \
"v_mfma_f32_32x32x16_bf16 %0, v[40:43], v[32:35], %0\n\t" \
"s_waitcnt vmcnt(16)\n\t" \
"s_barrier\n\t" \
"s_waitcnt lgkmcnt(8)\n\t" \
"v_mfma_f32_32x32x16_bf16 %1, v[40:43], v[36:39], %1\n\t" \
"ds_read_b64_tr_b16 v[72:73], v92 offset:32768\n\t" \
"ds_read_b64_tr_b16 v[74:75], v93 offset:34816\n\t" \
"ds_read_b128 v[64:67], v85 offset:32768\n\t" \
"s_waitcnt lgkmcnt(9)\n\t" \
"v_mfma_f32_32x32x16_bf16 %2, v[44:47], v[32:35], %2\n\t" \
"ds_read_b128 v[68:71], v85 offset:36864\n\t" \
"ds_read_b64_tr_b16 v[76:77], v94 offset:32768\n\t" \
"ds_read_b64_tr_b16 v[78:79], v95 offset:34816\n\t" \
"v_mfma_f32_32x32x16_bf16 %3, v[44:47], v[36:39], %3\n\t" \
"s_add_u32 m0, %9, 32768\n\t" \
"s_add_u32 s40, %7, 45056\n\t" \
"buffer_load_dwordx4 %11, %4, s40 offen lds\n\t" \
"s_add_u32 m0, %9, 33792\n\t" \
"s_add_u32 s41, %7, 46080\n\t" \
"buffer_load_dwordx4 %11, %4, s41 offen lds\n\t" \
"s_add_u32 m0, %9, 34816\n\t" \
"s_add_u32 s40, %7, 47104\n\t" \
"buffer_load_dwordx4 %11, %4, s40 offen lds\n\t" \
"s_waitcnt lgkmcnt(9)\n\t" \
"v_mfma_f32_32x32x16_bf16 %0, v[56:59], v[48:51], %0\n\t" \
"ds_read_b64_tr_b16 v[40:41], v92 offset:36864\n\t" \
"ds_read_b64_tr_b16 v[42:43], v93 offset:38912\n\t" \
"ds_read_b128 v[32:35], v85 offset:33792\n\t" \
"s_waitcnt lgkmcnt(11)\n\t" \
"v_mfma_f32_32x32x16_bf16 %1, v[56:59], v[52:55], %1\n\t" \
"ds_read_b128 v[36:39], v85 offset:37888\n\t" \
"ds_read_b64_tr_b16 v[44:45], v94 offset:36864\n\t" \
"s_waitcnt lgkmcnt(11)\n\t" \
"v_mfma_f32_32x32x16_bf16 %2, v[60:63], v[48:51], %2\n\t" \
"ds_read_b64_tr_b16 v[46:47], v95 offset:38912\n\t" \
"s_add_u32 m0, %9, 35840\n\t" \
"s_add_u32 s41, %7, 48128\n\t" \
"buffer_load_dwordx4 %11, %4, s41 offen lds\n\t" \
"v_mfma_f32_32x32x16_bf16 %3, v[60:63], v[52:55], %3\n\t" \
"s_cmp_ge_u32 11, %10\n\t" \
"s_cselect_b64 s[48:49], %6, %5\n\t" \
"s_cselect_b32 s50, s53, s52\n\t" \
"s_cselect_b32 s44, 0x400000, 0\n\t" \
"s_sub_u32 s44, %8, s44\n\t" \
"s_add_u32 m0, %9, 49152\n\t" \
"s_add_u32 s42, s44, 720896\n\t" \
"buffer_load_dwordx4 v80, s[48:51], s42 offen lds\n\t" \
"s_waitcnt lgkmcnt(9)\n\t" \
"v_mfma_f32_32x32x16_bf16 %0, v[72:75], v[64:67], %0\n\t" \
"ds_read_b64_tr_b16 v[56:57], v92 offset:40960\n\t" \
"ds_read_b64_tr_b16 v[58:59], v93 offset:43008\n\t" \
"ds_read_b128 v[48:51], v85 offset:34816\n\t" \
"s_waitcnt lgkmcnt(11)\n\t" \
"v_mfma_f32_32x32x16_bf16 %1, v[72:75], v[68:71], %1\n\t" \
"ds_read_b128 v[52:55], v85 offset:38912\n\t" \
"ds_read_b64_tr_b16 v[60:61], v94 offset:40960\n\t" \
"s_waitcnt lgkmcnt(11)\n\t" \
"v_mfma_f32_32x32x16_bf16 %2, v[76:79], v[64:67], %2\n\t" \
"ds_read_b64_tr_b16 v[62:63], v95 offset:43008\n\t" \
"s_add_u32 m0, %9, 50176\n\t" \
"s_add_u32 s43, s44, 724992\n\t" \
"buffer_load_dwordx4 v81, s[48:51], s43 offen lds\n\t" \
"v_mfma_f32_32x32x16_bf16 %3, v[76:79], v[68:71], %3\n\t" \
"s_add_u32 m0, %9, 51200\n\t" \
"s_add_u32 s42, s44, 729088\n\t" \
"buffer_load_dwordx4 v82, s[48:51], s42 offen lds\n\t" \
"s_add_u32 m0, %9, 52224\n\t" \
"s_add_u32 s43, s44, 733184\n\t" \
"buffer_load_dwordx4 v83, s[48:51], s43 offen lds\n\t" \
"s_waitcnt lgkmcnt(9)\n\t" \
"v_mfma_f32_32x32x16_bf16 %0, v[40:43], v[32:35], %0\n\t" \
"ds_read_b64_tr_b16 v[72:73], v92 offset:45056\n\t" \
"ds_read_b64_tr_b16 v[74:75], v93 offset:47104\n\t" \
"s_waitcnt lgkmcnt(10)\n\t" \
"v_mfma_f32_32x32x16_bf16 %1, v[40:43], v[36:39], %1\n\t" \
"ds_read_b128 v[64:67], v85 offset:35840\n\t" \
"s_waitcnt lgkmcnt(9)\n\t" \
"v_mfma_f32_32x32x16_bf16 %2, v[44:47], v[32:35], %2\n\t" \
"ds_read_b128 v[68:71], v85 offset:39936\n\t" \
"ds_read_b64_tr_b16 v[76:77], v94 offset:45056\n\t" \
"v_mfma_f32_32x32x16_bf16 %3, v[44:47], v[36:39], %3\n\t" \
"ds_read_b64_tr_b16 v[78:79], v95 offset:47104\n\t" \
"s_waitcnt lgkmcnt(9)\n\t" \
"v_mfma_f32_32x32x16_bf16 %0, v[56:59], v[48:51], %0\n\t" \
"s_waitcnt vmcnt(16)\n\t" \
"s_barrier\n\t" \
"s_waitcnt lgkmcnt(8)\n\t" \
"v_mfma_f32_32x32x16_bf16 %1, v[56:59], v[52:55], %1\n\t" \
"ds_read_b64_tr_b16 v[40:41], v96 offset:0\n\t" \
"ds_read_b64_tr_b16 v[42:43], v97 offset:2048\n\t" \
"ds_read_b128 v[32:35], v86 offset:0\n\t" \
"s_waitcnt lgkmcnt(9)\n\t" \
"v_mfma_f32_32x32x16_bf16 %2, v[60:63], v[48:51], %2\n\t" \
"ds_read_b128 v[36:39], v86 offset:4096\n\t" \
"ds_read_b64_tr_b16 v[44:45], v98 offset:0\n\t" \
"ds_read_b64_tr_b16 v[46:47], v99 offset:2048\n\t" \
"v_mfma_f32_32x32x16_bf16 %3, v[60:63], v[52:55], %3\n\t" \
"s_add_u32 m0, %9, 65536\n\t" \
"s_add_u32 s40, %7, 49152\n\t" \
"buffer_load_dwordx4 %11, %4, s40 offen lds\n\t" \
"s_add_u32 m0, %9, 66560\n\t" \
"s_add_u32 s41, %7, 50176\n\t" \
"buffer_load_dwordx4 %11, %4, s41 offen lds\n\t" \
"s_add_u32 m0, %9, 67584\n\t" \
"s_add_u32 s40, %7, 51200\n\t" \
"buffer_load_dwordx4 %11, %4, s40 offen lds\n\t" \
"s_waitcnt lgkmcnt(9)\n\t" \
"v_mfma_f32_32x32x16_bf16 %0, v[72:75], v[64:67], %0\n\t" \
"ds_read_b64_tr_b16 v[56:57], v96 offset:4096\n\t" \
"ds_read_b64_tr_b16 v[58:59], v97 offset:6144\n\t" \
"ds_read_b128 v[48:51], v86 offset:1024\n\t" \
"s_waitcnt lgkmcnt(11)\n\t" \
"v_mfma_f32_32x32x16_bf16 %1, v[72:75], v[68:71], %1\n\t" \
"ds_read_b128 v[52:55], v86 offset:5120\n\t" \
"ds_read_b64_tr_b16 v[60:61], v98 offset:4096\n\t" \
"s_waitcnt lgkmcnt(11)\n\t" \
"v_mfma_f32_32x32x16_bf16 %2, v[76:79], v[64:67], %2\n\t" \
"ds_read_b64_tr_b16 v[62:63], v99 offset:6144\n\t" \
"s_add_u32 m0, %9, 68608\n\t" \
"s_add_u32 s41, %7, 52224\n\t" \
"buffer_load_dwordx4 %11, %4, s41 offen lds\n\t" \
"v_mfma_f32_32x32x16_bf16 %3, v[76:79], v[68:71], %3\n\t" \
"s_cmp_ge_u32 12, %10\n\t" \
"s_cselect_b64 s[48:49], %6, %5\n\t" \
"s_cselect_b32 s50, s53, s52\n\t" \
"s_cselect_b32 s44, 0x400000, 0\n\t" \
"s_sub_u32 s44, %8, s44\n\t" \
"s_add_u32 m0, %9, 81920\n\t" \
"s_add_u32 s42, s44, 786432\n\t" \
"buffer_load_dwordx4 v80, s[48:51], s42 offen lds\n\t" \
"s_waitcnt lgkmcnt(9)\n\t" \
"v_mfma_f32_32x32x16_bf16 %0, v[40:43], v[32:35], %0\n\t" \
"ds_read_b64_tr_b16 v[72:73], v96 offset:8192\n\t" \
"ds_read_b64_tr_b16 v[74:75], v97 offset:10240\n\t" \
"ds_read_b128 v[64:67], v86 offset:2048\n\t" \
"s_waitcnt lgkmcnt(11)\n\t" \
"v_mfma_f32_32x32x16_bf16 %1, v[40:43], v[36:39], %1\n\t" \
"ds_read_b128 v[68:71], v86 offset:6144\n\t" \
"ds_read_b64_tr_b16 v[76:77], v98 offset:8192\n\t" \
"s_waitcnt lgkmcnt(11)\n\t" \
"v_mfma_f32_32x32x16_bf16 %2, v[44:47], v[32:35], %2\n\t" \
"ds_read_b64_tr_b16 v[78:79], v99 offset:10240\n\t" \
"s_add_u32 m0, %9, 82944\n\t" \
"s_add_u32 s43, s44, 790528\n\t" \
"buffer_load_dwordx4 v81, s[48:51], s43 offen lds\n\t" \
"v_mfma_f32_32x32x16_bf16 %3, v[44:47], v[36:39], %3\n\t" \
"s_add_u32 m0, %9, 83968\n\t" \
"s_add_u32 s42, s44, 794624\n\t" \
"buffer_load_dwordx4 v82, s[48:51], s42 offen lds\n\t" \
"s_add_u32 m0, %9, 84992\n\t" \
"s_add_u32 s43, s44, 798720\n\t" \
"buffer_load_dwordx4 v83, s[48:51], s43 offen lds\n\t" \
"s_waitcnt lgkmcnt(9)\n\t" \
"v_mfma_f32_32x32x16_bf16 %0, v[56:59], v[48:51], %0\n\t" \
"ds_read_b64_tr_b16 v[40:41], v96 offset:12288\n\t" \
"ds_read_b64_tr_b16 v[42:43], v97 offset:14336\n\t" \
"s_waitcnt lgkmcnt(10)\n\t" \
"v_mfma_f32_32x32x16_bf16 %1, v[56:59], v[52:55], %1\n\t" \
"ds_read_b128 v[32:35], v86 offset:3072\n\t" \
"s_waitcnt lgkmcnt(9)\n\t" \
"v_mfma_f32_32x32x16_bf16 %2, v[60:63], v[48:51], %2\n\t" \
"ds_read_b128 v[36:39], v86 offset:7168\n\t" \
"ds_read_b64_tr_b16 v[44:45], v98 offset:12288\n\t" \
"v_mfma_f32_32x32x16_bf16 %3, v[60:63], v[52:55], %3\n\t" \
"ds_read_b64_tr_b16 v[46:47], v99 offset:14336\n\t" \
"s_waitcnt lgkmcnt(9)\n\t" \
"v_mfma_f32_32x32x16_bf16 %0, v[72:75], v[64:67], %0\n\t" \
"s_waitcnt vmcnt(16)\n\t" \
"s_barrier\n\t" \
"s_waitcnt lgkmcnt(8)\n\t" \
"v_mfma_f32_32x32x16_bf16 %1, v[72:75], v[68:71], %1\n\t" \
"ds_read_b64_tr_b16 v[56:57], v88 offset:0\n\t" \
"ds_read_b64_tr_b16 v[58:59], v89 offset:2048\n\t" \
"ds_read_b128 v[48:51], v84 offset:0\n\t" \
"s_waitcnt lgkmcnt(9)\n\t" \
"v_mfma_f32_32x32x16_bf16 %2, v[76:79], v[64:67], %2\n\t" \
"ds_read_b128 v[52:55], v84 offset:4096\n\t" \
"ds_read_b64_tr_b16 v[60:61], v90 offset:0\n\t" \
"ds_read_b64_tr_b16 v[62:63], v91 offset:2048\n\t" \
"v_mfma_f32_32x32x16_bf16 %3, v[76:79], v[68:71], %3\n\t" \
"s_add_u32 m0, %9, 98304\n\t" \
"s_add_u32 s40, %7, 53248\n\t" \
"buffer_load_dwordx4 %11, %4, s40 offen lds\n\t" \
"s_add_u32 m0, %9, 99328\n\t" \
"s_add_u32 s41, %7, 54272\n\t" \
"buffer_load_dwordx4 %11, %4, s41 offen lds\n\t" \
"s_add_u32 m0, %9, 100352\n\t" \
"s_add_u32 s40, %7, 55296\n\t" \
"buffer_load_dwordx4 %11, %4, s40 offen lds\n\t" \
"s_waitcnt lgkmcnt(9)\n\t" \
"v_mfma_f32_32x32x16_bf16 %0, v[40:43], v[32:35], %0\n\t" \
"ds_read_b64_tr_b16 v[72:73], v88 offset:4096\n\t" \
"ds_read_b64_tr_b16 v[74:75], v89 offset:6144\n\t" \
"ds_read_b128 v[64:67], v84 offset:1024\n\t" \
"s_waitcnt lgkmcnt(11)\n\t" \
"v_mfma_f32_32x32x16_bf16 %1, v[40:43], v[36:39], %1\n\t" \
"ds_read_b128 v[68:71], v84 offset:5120\n\t" \
"ds_read_b64_tr_b16 v[76:77], v90 offset:4096\n\t" \
"s_waitcnt lgkmcnt(11)\n\t" \
"v_mfma_f32_32x32x16_bf16 %2, v[44:47], v[32:35], %2\n\t" \
"ds_read_b64_tr_b16 v[78:79], v91 offset:6144\n\t" \
"s_add_u32 m0, %9, 101376\n\t" \
"s_add_u32 s41, %7, 56320\n\t" \
"buffer_load_dwordx4 %11, %4, s41 offen lds\n\t" \
"v_mfma_f32_32x32x16_bf16 %3, v[44:47], v[36:39], %3\n\t" \
"s_cmp_ge_u32 13, %10\n\t" \
"s_cselect_b64 s[48:49], %6, %5\n\t" \
"s_cselect_b32 s50, s53, s52\n\t" \
"s_cselect_b32 s44, 0x400000, 0\n\t" \
"s_sub_u32 s44, %8, s44\n\t" \
"s_add_u32 m0, %9, 114688\n\t" \
"s_add_u32 s42, s44, 851968\n\t" \
"buffer_load_dwordx4 v80, s[48:51], s42 offen lds\n\t" \
"s_waitcnt lgkmcnt(9)\n\t" \
"v_mfma_f32_32x32x16_bf16 %0, v[56:59], v[48:51], %0\n\t" \
"ds_read_b64_tr_b16 v[40:41], v88 offset:8192\n\t" \
"ds_read_b64_tr_b16 v[42:43], v89 offset:10240\n\t" \
"ds_read_b128 v[32:35], v84 offset:2048\n\t" \
"s_waitcnt lgkmcnt(11)\n\t" \
"v_mfma_f32_32x32x16_bf16 %1, v[56:59], v[52:55], %1\n\t" \
"ds_read_b128 v[36:39], v84 offset:6144\n\t" \
"ds_read_b64_tr_b16 v[44:45], v90 offset:8192\n\t" \
"s_waitcnt lgkmcnt(11)\n\t" \
"v_mfma_f32_32x32x16_bf16 %2, v[60:63], v[48:51], %2\n\t" \
"ds_read_b64_tr_b16 v[46:47], v91 offset:10240\n\t" \
"s_add_u32 m0, %9, 115712\n\t" \
"s_add_u32 s43, s44, 856064\n\t" \
"buffer_load_dwordx4 v81, s[48:51], s43 offen lds\n\t" \
"v_mfma_f32_32x32x16_bf16 %3, v[60:63], v[52:55], %3\n\t" \
"s_add_u32 m0, %9, 116736\n\t" \
"s_add_u32 s42, s44, 860160\n\t" \
"buffer_load_dwordx4 v82, s[48:51], s42 offen lds\n\t" \
"s_add_u32 m0, %9, 117760\n\t" \
"s_add_u32 s43, s44, 864256\n\t" \
"buffer_load_dwordx4 v83, s[48:51], s43 offen lds\n\t" \
"s_waitcnt lgkmcnt(9)\n\t" \
"v_mfma_f32_32x32x16_bf16 %0, v[72:75], v[64:67], %0\n\t" \
"ds_read_b64_tr_b16 v[56:57], v88 offset:12288\n\t" \
"ds_read_b64_tr_b16 v[58:59], v89 offset:14336\n\t" \
"s_waitcnt lgkmcnt(10)\n\t" \
"v_mfma_f32_32x32x16_bf16 %1, v[72:75], v[68:71], %1\n\t" \
"ds_read_b128 v[48:51], v84 offset:3072\n\t" \
"s_waitcnt lgkmcnt(9)\n\t" \
"v_mfma_f32_32x32x16_bf16 %2, v[76:79], v[64:67], %2\n\t" \
"ds_read_b128 v[52:55], v84 offset:7168\n\t" \
"ds_read_b64_tr_b16 v[60:61], v90 offset:12288\n\t" \
"v_mfma_f32_32x32x16_bf16 %3, v[76:79], v[68:71], %3\n\t" \
"ds_read_b64_tr_b16 v[62:63], v91 offset:14336\n\t" \
"s_waitcnt lgkmcnt(9)\n\t" \
"v_mfma_f32_32x32x16_bf16 %0, v[40:43], v[32:35], %0\n\t" \
"s_waitcnt vmcnt(16)\n\t" \
"s_barrier\n\t" \
"s_waitcnt lgkmcnt(8)\n\t" \
"v_mfma_f32_32x32x16_bf16 %1, v[40:43], v[36:39], %1\n\t" \
"ds_read_b64_tr_b16 v[72:73], v88 offset:32768\n\t" \
"ds_read_b64_tr_b16 v[74:75], v89 offset:34816\n\t" \
"ds_read_b128 v[64:67], v84 offset:32768\n\t" \
"s_waitcnt lgkmcnt(9)\n\t" \
"v_mfma_f32_32x32x16_bf16 %2, v[44:47], v[32:35], %2\n\t" \
"ds_read_b128 v[68:71], v84 offset:36864\n\t" \
"ds_read_b64_tr_b16 v[76:77], v90 offset:32768\n\t" \
"ds_read_b64_tr_b16 v[78:79], v91 offset:34816\n\t" \
"v_mfma_f32_32x32x16_bf16 %3, v[44:47], v[36:39], %3\n\t" \
"s_add_u32 m0, %9, 131072\n\t" \
"s_add_u32 s40, %7, 57344\n\t" \
"buffer_load_dwordx4 %11, %4, s40 offen lds\n\t" \
"s_add_u32 m0, %9, 132096\n\t" \
"s_add_u32 s41, %7, 58368\n\t" \
"buffer_load_dwordx4 %11, %4, s41 offen lds\n\t" \
"s_add_u32 m0, %9, 133120\n\t" \
"s_add_u32 s40, %7, 59392\n\t" \
"buffer_load_dwordx4 %11, %4, s40 offen lds\n\t" \
"s_waitcnt lgkmcnt(9)\n\t" \
"v_mfma_f32_32x32x16_bf16 %0, v[56:59], v[48:51], %0\n\t" \
"ds_read_b64_tr_b16 v[40:41], v88 offset:36864\n\t" \
"ds_read_b64_tr_b16 v[42:43], v89 offset:38912\n\t" \
"ds_read_b128 v[32:35], v84 offset:33792\n\t" \
"s_waitcnt lgkmcnt(11)\n\t" \
"v_mfma_f32_32x32x16_bf16 %1, v[56:59], v[52:55], %1\n\t" \
"ds_read_b128 v[36:39], v84 offset:37888\n\t" \
"ds_read_b64_tr_b16 v[44:45], v90 offset:36864\n\t" \
"s_waitcnt lgkmcnt(11)\n\t" \
"v_mfma_f32_32x32x16_bf16 %2, v[60:63], v[48:51], %2\n\t" \
"ds_read_b64_tr_b16 v[46:47], v91 offset:38912\n\t" \
"s_add_u32 m0, %9, 134144\n\t" \
"s_add_u32 s41, %7, 60416\n\t" \
"buffer_load_dwordx4 %11, %4, s41 offen lds\n\t" \
"v_mfma_f32_32x32x16_bf16 %3, v[60:63], v[52:55], %3\n\t" \
"s_cmp_ge_u32 14, %10\n\t" \
"s_cselect_b64 s[48:49], %6, %5\n\t" \
"s_cselect_b32 s50, s53, s52\n\t" \
"s_cselect_b32 s44, 0x400000, 0\n\t" \
"s_sub_u32 s44, %8, s44\n\t" \
"s_add_u32 m0, %9, 147456\n\t" \
"s_add_u32 s42, s44, 917504\n\t" \
"buffer_load_dwordx4 v80, s[48:51], s42 offen lds\n\t" \
"s_waitcnt lgkmcnt(9)\n\t" \
"v_mfma_f32_32x32x16_bf16 %0, v[72:75], v[64:67], %0\n\t" \
"ds_read_b64_tr_b16 v[56:57], v88 offset:40960\n\t" \
"ds_read_b64_tr_b16 v[58:59], v89 offset:43008\n\t" \
"ds_read_b128 v[48:51], v84 offset:34816\n\t" \
"s_waitcnt lgkmcnt(11)\n\t" \
"v_mfma_f32_32x32x16_bf16 %1, v[72:75], v[68:71], %1\n\t" \
"ds_read_b128 v[52:55], v84 offset:38912\n\t" \
"ds_read_b64_tr_b16 v[60:61], v90 offset:40960\n\t" \
"s_waitcnt lgkmcnt(11)\n\t" \
"v_mfma_f32_32x32x16_bf16 %2, v[76:79], v[64:67], %2\n\t" \
"ds_read_b64_tr_b16 v[62:63], v91 offset:43008\n\t" \
"s_add_u32 m0, %9, 148480\n\t" \
"s_add_u32 s43, s44, 921600\n\t" \
"buffer_load_dwordx4 v81, s[48:51], s43 offen lds\n\t" \
"v_mfma_f32_32x32x16_bf16 %3, v[76:79], v[68:71], %3\n\t" \
"s_add_u32 m0, %9, 149504\n\t" \
"s_add_u32 s42, s44, 925696\n\t" \
"buffer_load_dwordx4 v82, s[48:51], s42 offen lds\n\t" \
"s_add_u32 m0, %9, 150528\n\t" \
"s_add_u32 s43, s44, 929792\n\t" \
"buffer_load_dwordx4 v83, s[48:51], s43 offen lds\n\t" \
"s_waitcnt lgkmcnt(9)\n\t" \
"v_mfma_f32_32x32x16_bf16 %0, v[40:43], v[32:35], %0\n\t" \
"ds_read_b64_tr_b16 v[72:73], v88 offset:45056\n\t" \
"ds_read_b64_tr_b16 v[74:75], v89 offset:47104\n\t" \
"s_waitcnt lgkmcnt(10)\n\t" \
"v_mfma_f32_32x32x16_bf16 %1, v[40:43], v[36:39], %1\n\t" \
"ds_read_b128 v[64:67], v84 offset:35840\n\t" \
"s_waitcnt lgkmcnt(9)\n\t" \
"v_mfma_f32_32x32x16_bf16 %2, v[44:47], v[32:35], %2\n\t" \
"ds_read_b128 v[68:71], v84 offset:39936\n\t" \
"ds_read_b64_tr_b16 v[76:77], v90 offset:45056\n\t" \
"v_mfma_f32_32x32x16_bf16 %3, v[44:47], v[36:39], %3\n\t" \
"ds_read_b64_tr_b16 v[78:79], v91 offset:47104\n\t" \
"s_waitcnt lgkmcnt(9)\n\t" \
"v_mfma_f32_32x32x16_bf16 %0, v[56:59], v[48:51], %0\n\t" \
"s_waitcnt vmcnt(16)\n\t" \
"s_barrier\n\t" \
"s_waitcnt lgkmcnt(8)\n\t" \
"v_mfma_f32_32x32x16_bf16 %1, v[56:59], v[52:55], %1\n\t" \
"ds_read_b64_tr_b16 v[40:41], v92 offset:0\n\t" \
"ds_read_b64_tr_b16 v[42:43], v93 offset:2048\n\t" \
"ds_read_b128 v[32:35], v85 offset:0\n\t" \
"s_waitcnt lgkmcnt(9)\n\t" \
"v_mfma_f32_32x32x16_bf16 %2, v[60:63], v[48:51], %2\n\t" \
"ds_read_b128 v[36:39], v85 offset:4096\n\t" \
"ds_read_b64_tr_b16 v[44:45], v94 offset:0\n\t" \
"ds_read_b64_tr_b16 v[46:47], v95 offset:2048\n\t" \
"v_mfma_f32_32x32x16_bf16 %3, v[60:63], v[52:55], %3\n\t" \
"s_add_u32 m0, %9, 0\n\t" \
"s_add_u32 s40, %7, 61440\n\t" \
"buffer_load_dwordx4 %11, %4, s40 offen lds\n\t" \
"s_add_u32 m0, %9, 1024\n\t" \
"s_add_u32 s41, %7, 62464\n\t" \
"buffer_load_dwordx4 %11, %4, s41 offen lds\n\t" \
"s_add_u32 m0, %9, 2048\n\t" \
"s_add_u32 s40, %7, 63488\n\t" \
"buffer_load_dwordx4 %11, %4, s40 offen lds\n\t" \
"s_waitcnt lgkmcnt(9)\n\t" \
"v_mfma_f32_32x32x16_bf16 %0, v[72:75], v[64:67], %0\n\t" \
"ds_read_b64_tr_b16 v[56:57], v92 offset:4096\n\t" \
"ds_read_b64_tr_b16 v[58:59], v93 offset:6144\n\t" \
"ds_read_b128 v[48:51], v85 offset:1024\n\t" \
"s_waitcnt lgkmcnt(11)\n\t" \
"v_mfma_f32_32x32x16_bf16 %1, v[72:75], v[68:71], %1\n\t" \
"ds_read_b128 v[52:55], v85 offset:5120\n\t" \
"ds_read_b64_tr_b16 v[60:61], v94 offset:4096\n\t" \
"s_waitcnt lgkmcnt(11)\n\t" \
"v_mfma_f32_32x32x16_bf16 %2, v[76:79], v[64:67], %2\n\t" \
"ds_read_b64_tr_b16 v[62:63], v95 offset:6144\n\t" \
"s_add_u32 m0, %9, 3072\n\t" \
"s_add_u32 s41, %7, 64512\n\t" \
"buffer_load_dwordx4 %11, %4, s41 offen lds\n\t" \
"v_mfma_f32_32x32x16_bf16 %3, v[76:79], v[68:71], %3\n\t" \
"s_cmp_ge_u32 15, %10\n\t" \
"s_cselect_b64 s[48:49], %6, %5\n\t" \
"s_cselect_b32 s50, s53, s52\n\t" \
"s_cselect_b32 s44, 0x400000, 0\n\t" \
"s_sub_u32 s44, %8, s44\n\t" \
"s_add_u32 m0, %9, 16384\n\t" \
"s_add_u32 s42, s44, 983040\n\t" \
"buffer_load_dwordx4 v80, s[48:51], s42 offen lds\n\t" \
"s_waitcnt lgkmcnt(9)\n\t" \
"v_mfma_f32_32x32x16_bf16 %0, v[40:43], v[32:35], %0\n\t" \
"ds_read_b64_tr_b16 v[72:73], v92 offset:8192\n\t" \
"ds_read_b64_tr_b16 v[74:75], v93 offset:10240\n\t" \
"ds_read_b128 v[64:67], v85 offset:2048\n\t" \
"s_waitcnt lgkmcnt(11)\n\t" \
"v_mfma_f32_32x32x16_bf16 %1, v[40:43], v[36:39], %1\n\t" \
"ds_read_b128 v[68:71], v85 offset:6144\n\t" \
"ds_read_b64_tr_b16 v[76:77], v94 offset:8192\n\t" \
"s_waitcnt lgkmcnt(11)\n\t" \
"v_mfma_f32_32x32x16_bf16 %2, v[44:47], v[32:35], %2\n\t" \
"ds_read_b64_tr_b16 v[78:79], v95 offset:10240\n\t" \
"s_add_u32 m0, %9, 17408\n\t" \
"s_add_u32 s43, s44, 987136\n\t" \
"buffer_load_dwordx4 v81, s[48:51], s43 offen lds\n\t" \
"v_mfma_f32_32x32x16_bf16 %3, v[44:47], v[36:39], %3\n\t" \
"s_add_u32 m0, %9, 18432\n\t" \
"s_add_u32 s42, s44, 991232\n\t" \
"buffer_load_dwordx4 v82, s[48:51], s42 offen lds\n\t" \
"s_add_u32 m0, %9, 19456\n\t" \
"s_add_u32 s43, s44, 995328\n\t" \
"buffer_load_dwordx4 v83, s[48:51], s43 offen lds\n\t" \
"s_waitcnt lgkmcnt(9)\n\t" \
"v_mfma_f32_32x32x16_bf16 %0, v[56:59], v[48:51], %0\n\t" \
"ds_read_b64_tr_b16 v[40:41], v92 offset:12288\n\t" \
"ds_read_b64_tr_b16 v[42:43], v93 offset:14336\n\t" \
"s_waitcnt lgkmcnt(10)\n\t" \
"v_mfma_f32_32x32x16_bf16 %1, v[56:59], v[52:55], %1\n\t" \
"ds_read_b128 v[32:35], v85 offset:3072\n\t" \
"s_waitcnt lgkmcnt(9)\n\t" \
"v_mfma_f32_32x32x16_bf16 %2, v[60:63], v[48:51], %2\n\t" \
"ds_read_b128 v[36:39], v85 offset:7168\n\t" \
"ds_read_b64_tr_b16 v[44:45], v94 offset:12288\n\t" \
"v_mfma_f32_32x32x16_bf16 %3, v[60:63], v[52:55], %3\n\t" \
"ds_read_b64_tr_b16 v[46:47], v95 offset:14336\n\t" \
"s_waitcnt lgkmcnt(9)\n\t" \
"v_mfma_f32_32x32x16_bf16 %0, v[72:75], v[64:67], %0\n\t" \
"s_waitcnt vmcnt(16)\n\t" \
"s_barrier\n\t" \
"s_waitcnt lgkmcnt(8)\n\t" \
"v_mfma_f32_32x32x16_bf16 %1, v[72:75], v[68:71], %1\n\t" \
"ds_read_b64_tr_b16 v[56:57], v92 offset:32768\n\t" \
"ds_read_b64_tr_b16 v[58:59], v93 offset:34816\n\t" \
"ds_read_b128 v[48:51], v85 offset:32768\n\t" \
"s_waitcnt lgkmcnt(9)\n\t" \
"v_mfma_f32_32x32x16_bf16 %2, v[76:79], v[64:67], %2\n\t" \
"ds_read_b128 v[52:55], v85 offset:36864\n\t" \
"ds_read_b64_tr_b16 v[60:61], v94 offset:32768\n\t" \
"ds_read_b64_tr_b16 v[62:63], v95 offset:34816\n\t" \
"v_mfma_f32_32x32x16_bf16 %3, v[76:79], v[68:71], %3\n\t" \
"s_add_u32 m0, %9, 32768\n\t" \
"s_add_u32 s40, %7, 65536\n\t" \
"buffer_load_dwordx4 %11, %4, s40 offen lds\n\t" \
"s_add_u32 m0, %9, 33792\n\t" \
"s_add_u32 s41, %7, 66560\n\t" \
"buffer_load_dwordx4 %11, %4, s41 offen lds\n\t" \
"s_add_u32 m0, %9, 34816\n\t" \
"s_add_u32 s40, %7, 67584\n\t" \
"buffer_load_dwordx4 %11, %4, s40 offen lds\n\t" \
"s_waitcnt lgkmcnt(9)\n\t" \
"v_mfma_f32_32x32x16_bf16 %0, v[40:43], v[32:35], %0\n\t" \
"ds_read_b64_tr_b16 v[72:73], v92 offset:36864\n\t" \
"ds_read_b64_tr_b16 v[74:75], v93 offset:38912\n\t" \
"ds_read_b128 v[64:67], v85 offset:33792\n\t" \
"s_waitcnt lgkmcnt(11)\n\t" \
"v_mfma_f32_32x32x16_bf16 %1, v[40:43], v[36:39], %1\n\t" \
"ds_read_b128 v[68:71], v85 offset:37888\n\t" \
"ds_read_b64_tr_b16 v[76:77], v94 offset:36864\n\t" \
"s_waitcnt lgkmcnt(11)\n\t" \
"v_mfma_f32_32x32x16_bf16 %2, v[44:47], v[32:35], %2\n\t" \
"ds_read_b64_tr_b16 v[78:79], v95 offset:38912\n\t" \
"s_add_u32 m0, %9, 35840\n\t" \
"s_add_u32 s41, %7, 68608\n\t" \
"buffer_load_dwordx4 %11, %4, s41 offen lds\n\t" \
"v_mfma_f32_32x32x16_bf16 %3, v[44:47], v[36:39], %3\n\t" \
"s_cmp_ge_u32 16, %10\n\t" \
"s_cselect_b64 s[48:49], %6, %5\n\t" \
"s_cselect_b32 s50, s53, s52\n\t" \
"s_cselect_b32 s44, 0x400000, 0\n\t" \
"s_sub_u32 s44, %8, s44\n\t" \
"s_add_u32 m0, %9, 49152\n\t" \
"s_add_u32 s42, s44, 1048576\n\t" \
"buffer_load_dwordx4 v80, s[48:51], s42 offen lds\n\t" \
"s_waitcnt lgkmcnt(9)\n\t" \
"v_mfma_f32_32x32x16_bf16 %0, v[56:59], v[48:51], %0\n\t" \
"ds_read_b64_tr_b16 v[40:41], v92 offset:40960\n\t" \
"ds_read_b64_tr_b16 v[42:43], v93 offset:43008\n\t" \
"ds_read_b128 v[32:35], v85 offset:34816\n\t" \
"s_waitcnt lgkmcnt(11)\n\t" \
"v_mfma_f32_32x32x16_bf16 %1, v[56:59], v[52:55], %1\n\t" \
"ds_read_b128 v[36:39], v85 offset:38912\n\t" \
"ds_read_b64_tr_b16 v[44:45], v94 offset:40960\n\t" \
"s_waitcnt lgkmcnt(11)\n\t" \
"v_mfma_f32_32x32x16_bf16 %2, v[60:63], v[48:51], %2\n\t" \
"ds_read_b64_tr_b16 v[46:47], v95 offset:43008\n\t" \
"s_add_u32 m0, %9, 50176\n\t" \
"s_add_u32 s43, s44, 1052672\n\t" \
"buffer_load_dwordx4 v81, s[48:51], s43 offen lds\n\t" \
"v_mfma_f32_32x32x16_bf16 %3, v[60:63], v[52:55], %3\n\t" \
"s_add_u32 m0, %9, 51200\n\t" \
"s_add_u32 s42, s44, 1056768\n\t" \
"buffer_load_dwordx4 v82, s[48:51], s42 offen lds\n\t" \
"s_add_u32 m0, %9, 52224\n\t" \
"s_add_u32 s43, s44, 1060864\n\t" \
"buffer_load_dwordx4 v83, s[48:51], s43 offen lds\n\t" \
"s_waitcnt lgkmcnt(9)\n\t" \
"v_mfma_f32_32x32x16_bf16 %0, v[72:75], v[64:67], %0\n\t" \
"ds_read_b64_tr_b16 v[56:57], v92 offset:45056\n\t" \
"ds_read_b64_tr_b16 v[58:59], v93 offset:47104\n\t" \
"s_waitcnt lgkmcnt(10)\n\t" \
"v_mfma_f32_32x32x16_bf16 %1, v[72:75], v[68:71], %1\n\t" \
"ds_read_b128 v[48:51], v85 offset:35840\n\t" \
"s_waitcnt lgkmcnt(9)\n\t" \
"v_mfma_f32_32x32x16_bf16 %2, v[76:79], v[64:67], %2\n\t" \
"ds_read_b128 v[52:55], v85 offset:39936\n\t" \
"ds_read_b64_tr_b16 v[60:61], v94 offset:45056\n\t" \
"v_mfma_f32_32x32x16_bf16 %3, v[76:79], v[68:71], %3\n\t" \
"ds_read_b64_tr_b16 v[62:63], v95 offset:47104\n\t" \
"s_waitcnt lgkmcnt(9)\n\t" \
"v_mfma_f32_32x32x16_bf16 %0, v[40:43], v[32:35], %0\n\t" \
"s_waitcnt vmcnt(16)\n\t" \
"s_barrier\n\t" \
"s_waitcnt lgkmcnt(8)\n\t" \
"v_mfma_f32_32x32x16_bf16 %1, v[40:43], v[36:39], %1\n\t" \
"ds_read_b64_tr_b16 v[72:73], v96 offset:0\n\t" \
"ds_read_b64_tr_b16 v[74:75], v97 offset:2048\n\t" \
"ds_read_b128 v[64:67], v86 offset:0\n\t" \
"s_waitcnt lgkmcnt(9)\n\t" \
"v_mfma_f32_32x32x16_bf16 %2, v[44:47], v[32:35], %2\n\t" \
"ds_read_b128 v[68:71], v86 offset:4096\n\t" \
"ds_read_b64_tr_b16 v[76:77], v98 offset:0\n\t" \
"ds_read_b64_tr_b16 v[78:79], v99 offset:2048\n\t" \
"v_mfma_f32_32x32x16_bf16 %3, v[44:47], v[36:39], %3\n\t" \
"s_add_u32 m0, %9, 65536\n\t" \
"s_add_u32 s40, %7, 69632\n\t" \
"buffer_load_dwordx4 %11, %4, s40 offen lds\n\t" \
"s_add_u32 m0, %9, 66560\n\t" \
"s_add_u32 s41, %7, 70656\n\t" \
"buffer_load_dwordx4 %11, %4, s41 offen lds\n\t" \
"s_add_u32 m0, %9, 67584\n\t" \
"s_add_u32 s40, %7, 71680\n\t" \
"buffer_load_dwordx4 %11, %4, s40 offen lds\n\t" \
"s_waitcnt lgkmcnt(9)\n\t" \
"v_mfma_f32_32x32x16_bf16 %0, v[56:59], v[48:51], %0\n\t" \
"ds_read_b64_tr_b16 v[40:41], v96 offset:4096\n\t" \
"ds_read_b64_tr_b16 v[42:43], v97 offset:6144\n\t" \
"ds_read_b128 v[32:35], v86 offset:1024\n\t" \
"s_waitcnt lgkmcnt(11)\n\t" \
"v_mfma_f32_32x32x16_bf16 %1, v[56:59], v[52:55], %1\n\t" \
"ds_read_b128 v[36:39], v86 offset:5120\n\t" \
"ds_read_b64_tr_b16 v[44:45], v98 offset:4096\n\t" \
"s_waitcnt lgkmcnt(11)\n\t" \
"v_mfma_f32_32x32x16_bf16 %2, v[60:63], v[48:51], %2\n\t" \
"ds_read_b64_tr_b16 v[46:47], v99 offset:6144\n\t" \
"s_add_u32 m0, %9, 68608\n\t" \
"s_add_u32 s41, %7, 72704\n\t" \
"buffer_load_dwordx4 %11, %4, s41 offen lds\n\t" \
"v_mfma_f32_32x32x16_bf16 %3, v[60:63], v[52:55], %3\n\t" \
"s_cmp_ge_u32 17, %10\n\t" \
"s_cselect_b64 s[48:49], %6, %5\n\t" \
"s_cselect_b32 s50, s53, s52\n\t" \
"s_cselect_b32 s44, 0x400000, 0\n\t" \
"s_sub_u32 s44, %8, s44\n\t" \
"s_add_u32 m0, %9, 81920\n\t" \
"s_add_u32 s42, s44, 1114112\n\t" \
"buffer_load_dwordx4 v80, s[48:51], s42 offen lds\n\t" \
"s_waitcnt lgkmcnt(9)\n\t" \
"v_mfma_f32_32x32x16_bf16 %0, v[72:75], v[64:67], %0\n\t" \
"ds_read_b64_tr_b16 v[56:57], v96 offset:8192\n\t" \
"ds_read_b64_tr_b16 v[58:59], v97 offset:10240\n\t" \
"ds_read_b128 v[48:51], v86 offset:2048\n\t" \
"s_waitcnt lgkmcnt(11)\n\t" \
"v_mfma_f32_32x32x16_bf16 %1, v[72:75], v[68:71], %1\n\t" \
"ds_read_b128 v[52:55], v86 offset:6144\n\t" \
"ds_read_b64_tr_b16 v[60:61], v98 offset:8192\n\t" \
"s_waitcnt lgkmcnt(11)\n\t" \
"v_mfma_f32_32x32x16_bf16 %2, v[76:79], v[64:67], %2\n\t" \
"ds_read_b64_tr_b16 v[62:63], v99 offset:10240\n\t" \
"s_add_u32 m0, %9, 82944\n\t" \
"s_add_u32 s43, s44, 1118208\n\t" \
"buffer_load_dwordx4 v81, s[48:51], s43 offen lds\n\t" \
"v_mfma_f32_32x32x16_bf16 %3, v[76:79], v[68:71], %3\n\t" \
"s_add_u32 m0, %9, 83968\n\t" \
"s_add_u32 s42, s44, 1122304\n\t" \
"buffer_load_dwordx4 v82, s[48:51], s42 offen lds\n\t" \
"s_add_u32 m0, %9, 84992\n\t" \
"s_add_u32 s43, s44, 1126400\n\t" \
"buffer_load_dwordx4 v83, s[48:51], s43 offen lds\n\t" \
"s_waitcnt lgkmcnt(9)\n\t" \
"v_mfma_f32_32x32x16_bf16 %0, v[40:43], v[32:35], %0\n\t" \
"ds_read_b64_tr_b16 v[72:73], v96 offset:12288\n\t" \
"ds_read_b64_tr_b16 v[74:75], v97 offset:14336\n\t" \
"s_waitcnt lgkmcnt(10)\n\t" \
"v_mfma_f32_32x32x16_bf16 %1, v[40:43], v[36:39], %1\n\t" \
"ds_read_b128 v[64:67], v86 offset:3072\n\t" \
"s_waitcnt lgkmcnt(9)\n\t" \
"v_mfma_f32_32x32x16_bf16 %2, v[44:47], v[32:35], %2\n\t" \
"ds_read_b128 v[68:71], v86 offset:7168\n\t" \
"ds_read_b64_tr_b16 v[76:77], v98 offset:12288\n\t" \
"v_mfma_f32_32x32x16_bf16 %3, v[44:47], v[36:39], %3\n\t" \
"ds_read_b64_tr_b16 v[78:79], v99 offset:14336\n\t" \
"s_waitcnt lgkmcnt(9)\n\t" \
"v_mfma_f32_32x32x16_bf16 %0, v[56:59], v[48:51], %0\n\t" \
"s_waitcnt vmcnt(16)\n\t" \
"s_barrier\n\t" \
"s_waitcnt lgkmcnt(8)\n\t" \
"v_mfma_f32_32x32x16_bf16 %1, v[56:59], v[52:55], %1\n\t" \
"ds_read_b64_tr_b16 v[40:41], v88 offset:0\n\t" \
"ds_read_b64_tr_b16 v[42:43], v89 offset:2048\n\t" \
"s_waitcnt lgkmcnt(8)\n\t" \
"v_mfma_f32_32x32x16_bf16 %2, v[60:63], v[48:51], %2\n\t" \
"ds_read_b128 v[32:35], v84 offset:0\n\t" \
"ds_read_b128 v[36:39], v84 offset:4096\n\t" \
"v_mfma_f32_32x32x16_bf16 %3, v[60:63], v[52:55], %3\n\t" \
"ds_read_b64_tr_b16 v[44:45], v90 offset:0\n\t" \
"ds_read_b64_tr_b16 v[46:47], v91 offset:2048\n\t" \
"s_waitcnt lgkmcnt(9)\n\t" \
"v_mfma_f32_32x32x16_bf16 %0, v[72:75], v[64:67], %0\n\t" \
"ds_read_b64_tr_b16 v[56:57], v88 offset:4096\n\t" \
"ds_read_b64_tr_b16 v[58:59], v89 offset:6144\n\t" \
"s_waitcnt lgkmcnt(10)\n\t" \
"v_mfma_f32_32x32x16_bf16 %1, v[72:75], v[68:71], %1\n\t" \
"ds_read_b128 v[48:51], v84 offset:1024\n\t" \
"s_waitcnt lgkmcnt(9)\n\t" \
"v_mfma_f32_32x32x16_bf16 %2, v[76:79], v[64:67], %2\n\t" \
"ds_read_b128 v[52:55], v84 offset:5120\n\t" \
"ds_read_b64_tr_b16 v[60:61], v90 offset:4096\n\t" \
"v_mfma_f32_32x32x16_bf16 %3, v[76:79], v[68:71], %3\n\t" \
"ds_read_b64_tr_b16 v[62:63], v91 offset:6144\n\t" \
"s_waitcnt lgkmcnt(9)\n\t" \
"v_mfma_f32_32x32x16_bf16 %0, v[40:43], v[32:35], %0\n\t" \
"ds_read_b64_tr_b16 v[72:73], v88 offset:8192\n\t" \
"ds_read_b64_tr_b16 v[74:75], v89 offset:10240\n\t" \
"s_waitcnt lgkmcnt(10)\n\t" \
"v_mfma_f32_32x32x16_bf16 %1, v[40:43], v[36:39], %1\n\t" \
"ds_read_b128 v[64:67], v84 offset:2048\n\t" \
"s_waitcnt lgkmcnt(9)\n\t" \
"v_mfma_f32_32x32x16_bf16 %2, v[44:47], v[32:35], %2\n\t" \
"ds_read_b128 v[68:71], v84 offset:6144\n\t" \
"ds_read_b64_tr_b16 v[76:77], v90 offset:8192\n\t" \
"v_mfma_f32_32x32x16_bf16 %3, v[44:47], v[36:39], %3\n\t" \
"ds_read_b64_tr_b16 v[78:79], v91 offset:10240\n\t" \
"s_waitcnt lgkmcnt(9)\n\t" \
"v_mfma_f32_32x32x16_bf16 %0, v[56:59], v[48:51], %0\n\t" \
"ds_read_b64_tr_b16 v[40:41], v88 offset:12288\n\t" \
"ds_read_b64_tr_b16 v[42:43], v89 offset:14336\n\t" \
"s_waitcnt lgkmcnt(10)\n\t" \
"v_mfma_f32_32x32x16_bf16 %1, v[56:59], v[52:55], %1\n\t" \
"ds_read_b128 v[32:35], v84 offset:3072\n\t" \
"s_waitcnt lgkmcnt(9)\n\t" \
"v_mfma_f32_32x32x16_bf16 %2, v[60:63], v[48:51], %2\n\t" \
"ds_read_b128 v[36:39], v84 offset:7168\n\t" \
"ds_read_b64_tr_b16 v[44:45], v90 offset:12288\n\t" \
"v_mfma_f32_32x32x16_bf16 %3, v[60:63], v[52:55], %3\n\t" \
"ds_read_b64_tr_b16 v[46:47], v91 offset:14336\n\t" \
"s_waitcnt lgkmcnt(9)\n\t" \
"v_mfma_f32_32x32x16_bf16 %0, v[72:75], v[64:67], %0\n\t" \
"s_waitcnt vmcnt(8)\n\t" \
"s_barrier\n\t" \
"s_waitcnt lgkmcnt(8)\n\t" \
"v_mfma_f32_32x32x16_bf16 %1, v[72:75], v[68:71], %1\n\t" \
"ds_read_b64_tr_b16 v[56:57], v88 offset:32768\n\t" \
"ds_read_b64_tr_b16 v[58:59], v89 offset:34816\n\t" \
"s_waitcnt lgkmcnt(8)\n\t" \
"v_mfma_f32_32x32x16_bf16 %2, v[76:79], v[64:67], %2\n\t" \
"ds_read_b128 v[48:51], v84 offset:32768\n\t" \
"ds_read_b128 v[52:55], v84 offset:36864\n\t" \
"v_mfma_f32_32x32x16_bf16 %3, v[76:79], v[68:71], %3\n\t" \
"ds_read_b64_tr_b16 v[60:61], v90 offset:32768\n\t" \
"ds_read_b64_tr_b16 v[62:63], v91 offset:34816\n\t" \
"s_waitcnt lgkmcnt(9)\n\t" \
"v_mfma_f32_32x32x16_bf16 %0, v[40:43], v[32:35], %0\n\t" \
"ds_read_b64_tr_b16 v[72:73], v88 offset:36864\n\t" \
"ds_read_b64_tr_b16 v[74:75], v89 offset:38912\n\t" \
"s_waitcnt lgkmcnt(10)\n\t" \
"v_mfma_f32_32x32x16_bf16 %1, v[40:43], v[36:39], %1\n\t" \
"ds_read_b128 v[64:67], v84 offset:33792\n\t" \
"s_waitcnt lgkmcnt(9)\n\t" \
"v_mfma_f32_32x32x16_bf16 %2, v[44:47], v[32:35], %2\n\t" \
"ds_read_b128 v[68:71], v84 offset:37888\n\t" \
"ds_read_b64_tr_b16 v[76:77], v90 offset:36864\n\t" \
"v_mfma_f32_32x32x16_bf16 %3, v[44:47], v[36:39], %3\n\t" \
"ds_read_b64_tr_b16 v[78:79], v91 offset:38912\n\t" \
"s_waitcnt lgkmcnt(9)\n\t" \
"v_mfma_f32_32x32x16_bf16 %0, v[56:59], v[48:51], %0\n\t" \
"ds_read_b64_tr_b16 v[40:41], v88 offset:40960\n\t" \
"ds_read_b64_tr_b16 v[42:43], v89 offset:43008\n\t" \
"s_waitcnt lgkmcnt(10)\n\t" \
"v_mfma_f32_32x32x16_bf16 %1, v[56:59], v[52:55], %1\n\t" \
"ds_read_b128 v[32:35], v84 offset:34816\n\t" \
"s_waitcnt lgkmcnt(9)\n\t" \
"v_mfma_f32_32x32x16_bf16 %2, v[60:63], v[48:51], %2\n\t" \
"ds_read_b128 v[36:39], v84 offset:38912\n\t" \
"ds_read_b64_tr_b16 v[44:45], v90 offset:40960\n\t" \
"v_mfma_f32_32x32x16_bf16 %3, v[60:63], v[52:55], %3\n\t" \
"ds_read_b64_tr_b16 v[46:47], v91 offset:43008\n\t" \
"s_waitcnt lgkmcnt(9)\n\t" \
"v_mfma_f32_32x32x16_bf16 %0, v[72:75], v[64:67], %0\n\t" \
"ds_read_b64_tr_b16 v[56:57], v88 offset:45056\n\t" \
"ds_read_b64_tr_b16 v[58:59], v89 offset:47104\n\t" \
"s_waitcnt lgkmcnt(10)\n\t" \
"v_mfma_f32_32x32x16_bf16 %1, v[72:75], v[68:71], %1\n\t" \
"ds_read_b128 v[48:51], v84 offset:35840\n\t" \
"s_waitcnt lgkmcnt(9)\n\t" \
"v_mfma_f32_32x32x16_bf16 %2, v[76:79], v[64:67], %2\n\t" \
"ds_read_b128 v[52:55], v84 offset:39936\n\t" \
"ds_read_b64_tr_b16 v[60:61], v90 offset:45056\n\t" \
"v_mfma_f32_32x32x16_bf16 %3, v[76:79], v[68:71], %3\n\t" \
"ds_read_b64_tr_b16 v[62:63], v91 offset:47104\n\t" \
"s_waitcnt lgkmcnt(9)\n\t" \
"v_mfma_f32_32x32x16_bf16 %0, v[40:43], v[32:35], %0\n\t" \
"s_waitcnt vmcnt(0)\n\t" \
"s_barrier\n\t" \
"s_waitcnt lgkmcnt(8)\n\t" \
"v_mfma_f32_32x32x16_bf16 %1, v[40:43], v[36:39], %1\n\t" \
"ds_read_b64_tr_b16 v[72:73], v92 offset:0\n\t" \
"ds_read_b64_tr_b16 v[74:75], v93 offset:2048\n\t" \
"s_waitcnt lgkmcnt(8)\n\t" \
"v_mfma_f32_32x32x16_bf16 %2, v[44:47], v[32:35], %2\n\t" \
"ds_read_b128 v[64:67], v85 offset:0\n\t" \
"ds_read_b128 v[68:71], v85 offset:4096\n\t" \
"v_mfma_f32_32x32x16_bf16 %3, v[44:47], v[36:39], %3\n\t" \
"ds_read_b64_tr_b16 v[76:77], v94 offset:0\n\t" \
"ds_read_b64_tr_b16 v[78:79], v95 offset:2048\n\t" \
"s_waitcnt lgkmcnt(9)\n\t" \
"v_mfma_f32_32x32x16_bf16 %0, v[56:59], v[48:51], %0\n\t" \
"ds_read_b64_tr_b16 v[40:41], v92 offset:4096\n\t" \
"ds_read_b64_tr_b16 v[42:43], v93 offset:6144\n\t" \
"s_waitcnt lgkmcnt(10)\n\t" \
"v_mfma_f32_32x32x16_bf16 %1, v[56:59], v[52:55], %1\n\t" \
"ds_read_b128 v[32:35], v85 offset:1024\n\t" \
"s_waitcnt lgkmcnt(9)\n\t" \
"v_mfma_f32_32x32x16_bf16 %2, v[60:63], v[48:51], %2\n\t" \
"ds_read_b128 v[36:39], v85 offset:5120\n\t" \
"ds_read_b64_tr_b16 v[44:45], v94 offset:4096\n\t" \
"v_mfma_f32_32x32x16_bf16 %3, v[60:63], v[52:55], %3\n\t" \
"ds_read_b64_tr_b16 v[46:47], v95 offset:6144\n\t" \
"s_waitcnt lgkmcnt(9)\n\t" \
"v_mfma_f32_32x32x16_bf16 %0, v[72:75], v[64:67], %0\n\t" \
"ds_read_b64_tr_b16 v[56:57], v92 offset:8192\n\t" \
"ds_read_b64_tr_b16 v[58:59], v93 offset:10240\n\t" \
"s_waitcnt lgkmcnt(10)\n\t" \
"v_mfma_f32_32x32x16_bf16 %1, v[72:75], v[68:71], %1\n\t" \
"ds_read_b128 v[48:51], v85 offset:2048\n\t" \
"s_waitcnt lgkmcnt(9)\n\t" \
"v_mfma_f32_32x32x16_bf16 %2, v[76:79], v[64:67], %2\n\t" \
"ds_read_b128 v[52:55], v85 offset:6144\n\t" \
"ds_read_b64_tr_b16 v[60:61], v94 offset:8192\n\t" \
"v_mfma_f32_32x32x16_bf16 %3, v[76:79], v[68:71], %3\n\t" \
"ds_read_b64_tr_b16 v[62:63], v95 offset:10240\n\t" \
"s_waitcnt lgkmcnt(9)\n\t" \
"v_mfma_f32_32x32x16_bf16 %0, v[40:43], v[32:35], %0\n\t" \
"ds_read_b64_tr_b16 v[72:73], v92 offset:12288\n\t" \
"ds_read_b64_tr_b16 v[74:75], v93 offset:14336\n\t" \
"s_waitcnt lgkmcnt(10)\n\t" \
"v_mfma_f32_32x32x16_bf16 %1, v[40:43], v[36:39], %1\n\t" \
"ds_read_b128 v[64:67], v85 offset:3072\n\t" \
"s_waitcnt lgkmcnt(9)\n\t" \
"v_mfma_f32_32x32x16_bf16 %2, v[44:47], v[32:35], %2\n\t" \
"ds_read_b128 v[68:71], v85 offset:7168\n\t" \
"ds_read_b64_tr_b16 v[76:77], v94 offset:12288\n\t" \
"v_mfma_f32_32x32x16_bf16 %3, v[44:47], v[36:39], %3\n\t" \
"ds_read_b64_tr_b16 v[78:79], v95 offset:14336\n\t" \
"s_waitcnt lgkmcnt(9)\n\t" \
"v_mfma_f32_32x32x16_bf16 %0, v[56:59], v[48:51], %0\n\t" \
"s_waitcnt lgkmcnt(8)\n\t" \
"v_mfma_f32_32x32x16_bf16 %1, v[56:59], v[52:55], %1\n\t" \
"s_waitcnt lgkmcnt(6)\n\t" \
"v_mfma_f32_32x32x16_bf16 %2, v[60:63], v[48:51], %2\n\t" \
"v_mfma_f32_32x32x16_bf16 %3, v[60:63], v[52:55], %3\n\t" \
"s_waitcnt lgkmcnt(3)\n\t" \
"v_mfma_f32_32x32x16_bf16 %0, v[72:75], v[64:67], %0\n\t" \
"s_waitcnt lgkmcnt(2)\n\t" \
"v_mfma_f32_32x32x16_bf16 %1, v[72:75], v[68:71], %1\n\t" \
"s_waitcnt lgkmcnt(0)\n\t" \
"v_mfma_f32_32x32x16_bf16 %2, v[76:79], v[64:67], %2\n\t" \
"v_mfma_f32_32x32x16_bf16 %3, v[76:79], v[68:71], %3\n\t" \
"s_nop 15\n\t" \
"s_nop 3\n\t"
#define OUT_ASM_CLOBBERS "v32", "v33", "v34", "v35", "v36", "v37", "v38", "v39", "v40", "v41", "v42", "v43", "v44", "v45", "v46", "v47", "v48", "v49", "v50", "v51", "v52", "v53", "v54", "v55", "v56", "v57", "v58", "v59", "v60", "v61", "v62", "v63", "v64", "v65", "v66", "v67", "v68", "v69", "v70", "v71", "v72", "v73", "v74", "v75", "v76", "v77", "v78", "v79", "v80", "v81", "v82", "v83", "v84", "v85", "v86", "v88", "v89", "v90", "v91", "v92", "v93", "v94", "v95", "v96", "v97", "v98", "v99", "s40", "s41", "s42", "s43", "s44", "s48", "s49", "s50", "s51", "s52", "s53", "m0", "memory", "scc"

constexpr int O_STG = 5;
constexpr int O_LDS = O_STG * 32768;
struct OutArgs { char* ws; const bf16_t* wo; const bf16_t* wob; bf16_t* out; };
typedef __attribute__((address_space(1))) unsigned gu32;

__global__ __launch_bounds__(256, 1) void out_kernel(OutArgs p) {
    extern __shared__ __attribute__((aligned(16))) char smem_[];
    const unsigned ldsb = (unsigned)(size_t)smem_;
    const int tid = threadIdx.x, wid = __builtin_amdgcn_readfirstlane(tid >> 6), lane = tid & 63;
    const int l32 = lane & 31, hi = lane >> 5, wm = wid >> 1, wn = wid & 1;
    const int xcd = blockIdx.x & 7, idx = blockIdx.x >> 3, ks = xcd & 3, tm = (xcd >> 2) * 8 + (idx >> 2), tn = idx & 3;
    const u32x4 srdA = make_srd(p.ws + WS_A + (size_t)tm * 4 * NK16 * 1024, 4 * NK16 * 1024);
    const unsigned asrc = (unsigned)lane * 16u;
    const unsigned wsrc = (unsigned)((lane >> 4) * 1024 + tn * 256 + (((lane & 15) ^ ((lane >> 4) << 2)) * 16));
    const int tq = (lane & 15) >> 2, tp = lane & 3, tb = (lane >> 4) & 1;
    f32x16 acc[2][2];
    {
        const unsigned s_ao = (unsigned)(wid * NK16 + ks * 72) * 1024u, s_wo = (unsigned)(ks * 1152 + wid * 16) * 1024u, s_m0 = ldsb + (unsigned)wid * 4096u, s_nsec = ks == 3 ? 10u : 18u;
        const unsigned i_aoff = ldsb + (unsigned)(wm * 8192 + lane * 16);
        const unsigned i_b0 = ldsb + (unsigned)(16384 + (4 * hi + tq) * 256 + (((8 * wn + 2 * tb + (tp >> 1)) ^ ((tq << 2) | hi)) * 16) + 8 * (tp & 1));
        asm volatile(OUT_ASM_TEXT
                     : "=&v"(acc[0][0]), "=&v"(acc[0][1]), "=&v"(acc[1][0]), "=&v"(acc[1][1])
                     : "s"(srdA), "s"(p.wo), "s"(p.wob), "s"(s_ao), "s"(s_wo), "s"(s_m0), "s"(s_nsec), "v"(asrc), "v"(wsrc), "v"(i_aoff), "v"(i_b0)
                     : OUT_ASM_CLOBBERS);
    }
    const int tile = tm * 4 + tn;
    const __amdgpu_buffer_rsrc_t rsX = __builtin_amdgcn_make_buffer_rsrc((void*)(p.ws + WS_X + (size_t)tile * 16 * 16384), 0, 16 * 16384, 0x00020000);
#pragma unroll
    for (int t4 = 0; t4 < 4; ++t4) {
        if (t4 != ks) {
            const f32x16& t = acc[t4 & 1][t4 >> 1];
#pragma unroll
            for (int g4 = 0; g4 < 4; ++g4) {
                const f32x4 v = {t[4 * g4], t[4 * g4 + 1], t[4 * g4 + 2], t[4 * g4 + 3]};
                __builtin_amdgcn_raw_buffer_store_b128(__builtin_bit_cast(u32x4, v), rsX, (unsigned)(lane * 16), (unsigned)((t4 * 4 + ks) * 16384 + (wid * 4 + g4) * 1024), 16);
            }
        }
    }
    asm volatile("s_waitcnt vmcnt(0)\n\ts_barrier" ::: "memory");
    gu32* flags = (gu32*)(p.ws + WS_F) + tile * 64;
    if (tid < 4 && tid != ks) __hip_atomic_fetch_add(flags + tid * 16, 1u, __ATOMIC_RELAXED, __HIP_MEMORY_SCOPE_AGENT);
    f32x16 own;
#pragma unroll
    for (int e = 0; e < 16; ++e) own[e] = (ks & 2) ? ((ks & 1) ? acc[1][1][e] : acc[0][1][e]) : ((ks & 1) ? acc[1][0][e] : acc[0][0][e]);
#pragma unroll 1
    for (unsigned spins = 0; spins < (1u << 22); ++spins) {
        if ((unsigned)__builtin_amdgcn_readfirstlane(__hip_atomic_load(flags + ks * 16, __ATOMIC_RELAXED, __HIP_MEMORY_SCOPE_AGENT)) >= 3u) break;
        __builtin_amdgcn_s_sleep(2);
    }
    asm volatile("" ::: "memory");
    u32x4 in[3][4];
#pragma unroll
    for (int j = 0; j < 3; ++j) {
        const int src = (ks + 1 + j) & 3;
#pragma unroll
        for (int g4 = 0; g4 < 4; ++g4) in[j][g4] = __builtin_amdgcn_raw_buffer_load_b128(rsX, (unsigned)(lane * 16), (unsigned)((ks * 4 + src) * 16384 + (wid * 4 + g4) * 1024), 16);
    }
    asm volatile("s_waitcnt vmcnt(0)" ::: "memory");
    unsigned pk[8];
#pragma unroll
    for (int g4 = 0; g4 < 4; ++g4) {
        f32x4 s = {own[4 * g4], own[4 * g4 + 1], own[4 * g4 + 2], own[4 * g4 + 3]};
#pragma unroll
        for (int j = 0; j < 3; ++j) s += __builtin_bit_cast(f32x4, in[j][g4]);
        pk[2 * g4] = cvtpk(s[0], s[1]); pk[2 * g4 + 1] = cvtpk(s[2], s[3]);
    }
    bf16_t* orow = p.out + (size_t)(tm * 128 + wm * 64 + (ks >> 1) * 32 + l32) * DM + tn * 128 + wn * 64 + (ks & 1) * 32;
#pragma unroll
    for (int g2 = 0; g2 < 2; ++g2) {
        auto r0 = __builtin_amdgcn_permlane32_swap(pk[4 * g2], pk[4 * g2 + 2], false, false);
        auto r1 = __builtin_amdgcn_permlane32_swap(pk[4 * g2 + 1], pk[4 * g2 + 3], false, false);
        u32x4 w = {r0[0], r1[0], r0[1], r1[1]};
        *(u32x4*)(orow + 16 * g2 + 8 * hi) = w;
    }
}

extern "C" void kernel_launch(void* const* d_in, const int* in_sizes, int n_in, void* d_out, int out_size, void* d_ws, size_t ws_size, hipStream_t stream) {
    static bool init = false;
    if (!init) {
        (void)hipFuncSetAttribute((const void*)attn_kernel, hipFuncAttributeMaxDynamicSharedMemorySize, A_LDS);
        (void)hipFuncSetAttribute((const void*)out_kernel, hipFuncAttributeMaxDynamicSharedMemorySize, O_LDS);
        init = true;
    }
    PreArgs pa; pa.xa = (const bf16_t*)d_in[0]; pa.gw = (const bf16_t*)d_in[9]; pa.wob = (const bf16_t*)d_in[8]; pa.ws = (char*)d_ws;
    pa.wuk = (const bf16_t*)d_in[3]; pa.wuv = (const bf16_t*)d_in[4]; pa.ckv = (const bf16_t*)d_in[1];
    pre_kernel<<<256, 512, 0, stream>>>(pa);
    AttnArgs aa; aa.ws = (char*)d_ws; aa.xa = (const bf16_t*)d_in[0]; aa.wq = (const bf16_t*)d_in[2]; aa.wqkv = (const bf16_t*)d_in[7];
    aa.ckv = (const bf16_t*)d_in[1]; aa.wuk = (const bf16_t*)d_in[3]; aa.wuv = (const bf16_t*)d_in[4]; aa.wo = (const bf16_t*)d_in[5];
    attn_kernel<<<256, 256, A_LDS, stream>>>(aa);
    OutArgs oa; oa.ws = (char*)d_ws; oa.wo = (const bf16_t*)d_in[5]; oa.wob = (const bf16_t*)d_in[8]; oa.out = (bf16_t*)d_out;
    out_kernel<<<256, 256, O_LDS, stream>>>(oa);
}
```
